```python
import torch

def _softmax(x, axis=-1):
    return torch.softmax(x, dim=axis)
def _gelu(x):
    return torch.nn.functional.gelu(x, approximate="tanh")
def _silu(x):
    return torch.nn.functional.silu(x)
def _sigmoid(x):
    return torch.sigmoid(x)
def _layernorm(x, axis=-1, eps=1e-5):
    m = x.mean(dim=axis, keepdim=True)
    v = x.var(dim=axis, keepdim=True, unbiased=False)
    return (x - m) / torch.sqrt(v + eps)
def _scatter_add(tgt, idx, src, axis=0):
    return tgt.clone().index_add_(axis, idx, src)
def _segment_sum(src, seg, n):
    out = torch.zeros((n,) + src.shape[1:], dtype=src.dtype)
    return out.index_add_(0, seg, src)
def _pad(x, pw):
    flat = tuple(v for pair in reversed(pw) for v in pair)
    return torch.nn.functional.pad(x, flat)

kernel_name = 'parallel_sum__gqa_block_cached__dense_activation_gelu__decode'

def setup_inputs():

    _w = (lambda a: (lambda t: t.to(torch.bfloat16) if t.is_floating_point() else t)(torch.as_tensor(a)))
    def _A_setup():
        B, Sq, D = 1, 1, 448
        Hq, Hkv, Sk, Dh = 16, 8, 65536, 256
        assert Hq % Hkv == 0, (Hq, Hkv)
        return {
            "x": _w((lambda *s: torch.randn(*s, dtype=torch.bfloat16) * 0.05)(B, Sq, D)),
            "k_cache": _w((lambda *s: torch.randn(*s, dtype=torch.bfloat16) * 3.2)(B, Hkv, Sk, Dh)),
            "v_cache": _w((lambda *s: torch.randn(*s, dtype=torch.bfloat16) * 1.6)(B, Hkv, Sk, Dh)),
            "wq": _w((lambda *s: torch.randn(*s, dtype=torch.bfloat16) * 3.2)(D, Hq * Dh) * (D ** -0.5)),
            "wo": _w((lambda *s: torch.randn(*s, dtype=torch.bfloat16) * 1.6)(Hq * Dh, D) * ((Hq * Dh) ** -0.5)),
        }
    _A_in = _A_setup()

    def _B_setup():
        B, S, D, F = 1, 1, 448, 1792
        return {
            "x": _w((lambda *s: torch.randn(*s, dtype=torch.bfloat16) * 0.05)(B, S, D)),
            "w1": _w((lambda *s: torch.randn(*s, dtype=torch.bfloat16) * 1.6)(D, F) * (D ** -0.5)),
            "b1": _w((lambda *s: torch.randn(*s, dtype=torch.bfloat16) * 1.6)(F) * 0.01),
            "w2": _w((lambda *s: torch.randn(*s, dtype=torch.bfloat16) * 1.6)(F, D) * (F ** -0.5)),
            "b2": _w((lambda *s: torch.randn(*s, dtype=torch.bfloat16) * 1.6)(D) * 0.01),
        }
    _B_in = _B_setup()

    return {
        "A_x": _A_in["x"],
        "A_k_cache": _A_in["k_cache"],
        "A_v_cache": _A_in["v_cache"],
        "A_wq": _A_in["wq"],
        "A_wo": _A_in["wo"],
        "B_x": _B_in["x"],
        "B_w1": _B_in["w1"],
        "B_b1": _B_in["b1"],
        "B_w2": _B_in["w2"],
        "B_b2": _B_in["b2"],
    }

def reference(A_x, A_k_cache, A_v_cache, A_wq, A_wo, B_x, B_w1, B_b1, B_w2, B_b2):
    def _A_ref(x, k_cache, v_cache, wq, wo):
        B, Sq, D = x.shape
        Hq, Dh = 16, 256
        Hkv = k_cache.shape[1]
        rep = Hq // Hkv
        q = (lambda x, *d: x.permute(*d))((x @ wq).reshape(B, Sq, Hq, Dh), 0, 2, 1, 3)
        k = (lambda x, n, axis: torch.repeat_interleave(x, n, dim=axis))(k_cache, rep, axis=1)
        v = (lambda x, n, axis: torch.repeat_interleave(x, n, dim=axis))(v_cache, rep, axis=1)
        scores = torch.einsum("bhqd,bhkd->bhqk", q, k) * (Dh ** -0.5)
        p = _softmax(scores, axis=-1)
        o = torch.einsum("bhqk,bhkd->bhqd", p, v)
        return (lambda x, *d: x.permute(*d))(o, 0, 2, 1, 3).reshape(B, Sq, Hq * Dh) @ wo

    def _B_ref(x, w1, b1, w2, b2):
        h = torch.einsum("bsd,df->bsf", x, w1) + b1
        h = _gelu(h)
        return torch.einsum("bsf,fd->bsd", h, w2) + b2

    x = A_x
    xn = _layernorm(x, axis=-1)
    return x + _A_ref(xn, A_k_cache, A_v_cache, A_wq, A_wo) + _B_ref(xn, B_w1, B_b1, B_w2, B_b2)
```

```cpp
#include <hip/hip_runtime.h>
#include <hip/hip_bf16.h>
#include <cstdint>

typedef unsigned u32x4 __attribute__((ext_vector_type(4)));
typedef float f32x4 __attribute__((ext_vector_type(4)));
typedef short bf16x2s __attribute__((ext_vector_type(2)));
typedef __bf16 bf16x2 __attribute__((ext_vector_type(2)));

constexpr int D = 448, HQ = 16, HKV = 8, SK = 65536, DH = 256, F = 1792;
#define ROUND_SCORE 1
constexpr int NSPLIT = 64;
constexpr int NW = 4;
constexpr int KEYS_PER_BLOCK = SK / NSPLIT;
constexpr int TILE = 16;
constexpr int NTILE = KEYS_PER_BLOCK / (NW * TILE);

__device__ __forceinline__ float bf2f(unsigned short b) { return __uint_as_float(((unsigned)b) << 16); }
__device__ __forceinline__ float rbf(float x) {
  unsigned u = __float_as_uint(x);
  u = (u + 0x7FFFu + ((u >> 16) & 1u)) & 0xFFFF0000u;
  return __uint_as_float(u);
}
__device__ __forceinline__ float dot2(unsigned k, unsigned q, float acc) {
  return __builtin_amdgcn_fdot2_f32_bf16(__builtin_bit_cast(bf16x2, k), __builtin_bit_cast(bf16x2, q), acc, false);
}
template <int CTRL> __device__ __forceinline__ float dpp(float x) {
  return __builtin_bit_cast(float, __builtin_amdgcn_mov_dpp(__builtin_bit_cast(int, x), CTRL, 0xf, 0xf, true));
}
constexpr int XOR1 = 0xB1, XOR2 = 0x4E, HMIR = 0x141, MIR = 0x140;

__device__ __forceinline__ float wave_sum(float v) {
  for (int o = 32; o > 0; o >>= 1) v += __shfl_xor(v, o);
  return v;
}

__device__ __forceinline__ void layernorm_to_lds(const unsigned short* x, float* xn) {
  const int tid = threadIdx.x;
  if (tid < 64) {
    float v[7]; float s = 0.f;
    for (int i = 0; i < 7; ++i) { v[i] = bf2f(x[tid + 64 * i]); s += v[i]; }
    s = wave_sum(s);
    const float mf = s / D;
    float ss = 0.f;
    for (int i = 0; i < 7; ++i) { float d = v[i] - mf; ss += d * d; }
    ss = wave_sum(ss);
    const float m = rbf(mf), var = rbf(ss / D);
    const float den = rbf(sqrtf(rbf(var + 1e-5f)));
    for (int i = 0; i < 7; ++i) xn[tid + 64 * i] = rbf(rbf(v[i] - m) / den);
  }
  __syncthreads();
}

__global__ __launch_bounds__(256) void k_pre(const unsigned short* x, const unsigned short* wq, const unsigned short* w1,
                                             const unsigned short* b1, unsigned short* qout, float* hout) {
  __shared__ float xn[D];
  __shared__ float red[32][64];
  layernorm_to_lds(x, xn);
  const int b = blockIdx.x, tid = threadIdx.x;
  const bool isq = b < 64;
  const unsigned short* W = isq ? wq : w1;
  const int ld = isq ? 4096 : F;
  const int c0 = isq ? b * 64 : (b - 64) * 64;
  const int cc = tid & 7, r0 = tid >> 3;
  float acc[8] = {0,0,0,0,0,0,0,0};
#pragma unroll
  for (int i = 0; i < 14; ++i) {
    const int r = r0 + 32 * i;
    u32x4 w = *(const u32x4*)(W + (size_t)r * ld + c0 + cc * 8);
    const float xv = xn[r];
#pragma unroll
    for (int j = 0; j < 4; ++j) { acc[2*j] += xv * __uint_as_float(w[j] << 16); acc[2*j+1] += xv * __uint_as_float(w[j] & 0xffff0000u); }
  }
#pragma unroll
  for (int j = 0; j < 8; ++j) red[r0][cc * 8 + j] = acc[j];
  __syncthreads();
  if (tid < 64) {
    float s = 0.f;
    for (int r = 0; r < 32; ++r) s += red[r][tid];
    if (isq) qout[c0 + tid] = (unsigned short)(__float_as_uint(rbf(s)) >> 16);
    else {
      float hv = rbf(rbf(s) + bf2f(b1[c0 + tid]));
      float t = 0.7978845608028654f * (hv + 0.044715f * hv * hv * hv);
      hout[c0 + tid] = rbf(0.5f * hv * (1.f + tanhf(t)));
    }
  }
}

__global__ __launch_bounds__(256) void k_attn(const unsigned short* __restrict__ kc, const unsigned short* __restrict__ vc,
                                              const unsigned short* __restrict__ q, float* __restrict__ pml, float* __restrict__ po) {
  __shared__ float lds[NW * 2 * 2 * (DH + 4)];
  const int h = blockIdx.x & 7, s = blockIdx.x >> 3;
  const int tid = threadIdx.x, lane = tid & 63, wave = __builtin_amdgcn_readfirstlane(tid >> 6);
  const int half = lane >> 5, c = lane & 31;
  const u32x4 q0 = *(const u32x4*)(q + (2 * h) * DH + 8 * c), q1 = *(const u32x4*)(q + (2 * h + 1) * DH + 8 * c);
  const size_t base = ((size_t)h * SK + (size_t)s * KEYS_PER_BLOCK + wave * TILE) * DH + lane * 8;
  const u32x4* kp = (const u32x4*)(kc + base);
  const u32x4* vp = (const u32x4*)(vc + base);
  constexpr float C2 = 0.0625f * 1.4426950408889634f;
  float m = -INFINITY, l = 0.f;
  float acc0[8], acc1[8];
#pragma unroll
  for (int j = 0; j < 8; ++j) { acc0[j] = 0.f; acc1[j] = 0.f; }
  u32x4 Kn[8], Vn[8];
#pragma unroll
  for (int i = 0; i < 8; ++i) Kn[i] = __builtin_nontemporal_load(kp + i * 64);
#pragma unroll
  for (int i = 0; i < 8; ++i) Vn[i] = __builtin_nontemporal_load(vp + i * 64);
  for (int t = 0; t < NTILE; ++t) {
    u32x4 K[8], V[8];
#pragma unroll
    for (int i = 0; i < 8; ++i) { K[i] = Kn[i]; V[i] = Vn[i]; }
    if (t + 1 < NTILE) {
      kp += NW * TILE * DH / 8; vp += NW * TILE * DH / 8;
#pragma unroll
      for (int i = 0; i < 8; ++i) Kn[i] = __builtin_nontemporal_load(kp + i * 64);
#pragma unroll
      for (int i = 0; i < 8; ++i) Vn[i] = __builtin_nontemporal_load(vp + i * 64);
    }
    float sc[8];
#pragma unroll
    for (int i = 0; i < 8; ++i) {
      float a = 0.f, b = 0.f;
#pragma unroll
      for (int j = 0; j < 4; ++j) { a = dot2(K[i][j], q0[j], a); b = dot2(K[i][j], q1[j], b); }
      auto r = __builtin_amdgcn_permlane16_swap(__float_as_uint(a), __float_as_uint(b), false, false);
      float x = __uint_as_float(r[0]) + __uint_as_float(r[1]);
      x += dpp<XOR1>(x); x += dpp<XOR2>(x); x += dpp<HMIR>(x); x += dpp<MIR>(x);
      sc[i] = (ROUND_SCORE ? rbf(x) : x) * C2;
    }
    float mx = sc[0];
#pragma unroll
    for (int i = 1; i < 8; ++i) mx = fmaxf(mx, sc[i]);
    const float mn = fmaxf(m, mx);
    const float alpha = __builtin_amdgcn_exp2f(m - mn);
    m = mn;
    float ps = 0.f;
    float p0[8], p1[8];
#pragma unroll
    for (int i = 0; i < 8; ++i) {
      const float p = __builtin_amdgcn_exp2f(sc[i] - mn); ps += p;
      auto r = __builtin_amdgcn_permlane16_swap(__float_as_uint(p), __float_as_uint(p), false, false);
      p0[i] = __uint_as_float(r[0]); p1[i] = __uint_as_float(r[1]);
    }
    l = l * alpha + ps;
    {
      auto r = __builtin_amdgcn_permlane16_swap(__float_as_uint(alpha), __float_as_uint(alpha), false, false);
      const float a0 = __uint_as_float(r[0]), a1 = __uint_as_float(r[1]);
#pragma unroll
      for (int j = 0; j < 8; ++j) { acc0[j] *= a0; acc1[j] *= a1; }
    }
#pragma unroll
    for (int i = 0; i < 8; ++i) {
#pragma unroll
      for (int j = 0; j < 4; ++j) {
        const float lo = __uint_as_float(V[i][j] << 16), hi = __uint_as_float(V[i][j] & 0xffff0000u);
        acc0[2*j] += p0[i] * lo; acc0[2*j+1] += p0[i] * hi;
        acc1[2*j] += p1[i] * lo; acc1[2*j+1] += p1[i] * hi;
      }
    }
  }
  float* rec0 = lds + ((wave * 2 + half) * 2 + 0) * (DH + 4);
  float* rec1 = lds + ((wave * 2 + half) * 2 + 1) * (DH + 4);
  *(f32x4*)(rec0 + 4 + 8 * c) = f32x4{acc0[0], acc0[1], acc0[2], acc0[3]};
  *(f32x4*)(rec0 + 8 + 8 * c) = f32x4{acc0[4], acc0[5], acc0[6], acc0[7]};
  *(f32x4*)(rec1 + 4 + 8 * c) = f32x4{acc1[0], acc1[1], acc1[2], acc1[3]};
  *(f32x4*)(rec1 + 8 + 8 * c) = f32x4{acc1[4], acc1[5], acc1[6], acc1[7]};
  if (c == 0) { rec0[0] = m; rec0[1] = l; }
  if (c == 16) { rec1[0] = m; rec1[1] = l; }
  __syncthreads();
#pragma unroll
  for (int g = 0; g < 2; ++g) {
    float M = -INFINITY;
#pragma unroll
    for (int w = 0; w < NW * 2; ++w) M = fmaxf(M, lds[(w * 2 + g) * (DH + 4)]);
    float L = 0.f, o = 0.f;
#pragma unroll
    for (int w = 0; w < NW * 2; ++w) {
      const float* rec = lds + (w * 2 + g) * (DH + 4);
      const float wt = __builtin_amdgcn_exp2f(rec[0] - M);
      L += wt * rec[1]; o += wt * rec[4 + tid];
    }
    const int slot = (2 * h + g) * NSPLIT + s;
    po[(size_t)slot * DH + tid] = o;
    if (tid == 0) { pml[2 * slot] = M; pml[2 * slot + 1] = L; }
  }
}

__global__ __launch_bounds__(256) void k_comb(const float* __restrict__ pml, const float* __restrict__ po, const float* __restrict__ hbuf,
                                              const unsigned short* __restrict__ wo, const unsigned short* __restrict__ w2, float* __restrict__ y) {
  __shared__ float red[16][17];
  __shared__ float o[16];
  const int j = blockIdx.x, tid = threadIdx.x;
  const int head = j >> 4, d0 = (j & 15) * 16;
  float M = -INFINITY;
  for (int s = 0; s < NSPLIT; ++s) M = fmaxf(M, pml[2 * (head * NSPLIT + s)]);
  float L = 0.f;
  for (int s = 0; s < NSPLIT; ++s) L += __builtin_amdgcn_exp2f(pml[2 * (head * NSPLIT + s)] - M) * pml[2 * (head * NSPLIT + s) + 1];
  const int d = tid & 15, sg = tid >> 4;
  float a = 0.f;
  for (int s = sg; s < NSPLIT; s += 16) a += __builtin_amdgcn_exp2f(pml[2 * (head * NSPLIT + s)] - M) * po[(size_t)(head * NSPLIT + s) * DH + d0 + d];
  red[sg][d] = a;
  __syncthreads();
  if (tid < 16) { float t = 0.f; for (int i = 0; i < 16; ++i) t += red[i][tid]; o[tid] = rbf(t / L); }
  __syncthreads();
  if (tid < 224) {
    float y0 = 0.f, y1 = 0.f, z0 = 0.f, z1 = 0.f;
#pragma unroll
    for (int r = 0; r < 16; ++r) { unsigned w = *(const unsigned*)(wo + (size_t)(j * 16 + r) * D + 2 * tid); y0 += o[r] * __uint_as_float(w << 16); y1 += o[r] * __uint_as_float(w & 0xffff0000u); }
#pragma unroll
    for (int r = 0; r < 7; ++r) { unsigned w = *(const unsigned*)(w2 + (size_t)(j * 7 + r) * D + 2 * tid); float hv = hbuf[j * 7 + r]; z0 += hv * __uint_as_float(w << 16); z1 += hv * __uint_as_float(w & 0xffff0000u); }
    y[(size_t)j * 2 * D + 2 * tid] = y0; y[(size_t)j * 2 * D + 2 * tid + 1] = y1;
    y[(size_t)j * 2 * D + D + 2 * tid] = z0; y[(size_t)j * 2 * D + D + 2 * tid + 1] = z1;
  }
}

__global__ __launch_bounds__(256) void k_fin(const float* __restrict__ y, const unsigned short* __restrict__ x, const unsigned short* __restrict__ b2, unsigned short* __restrict__ out) {
  __shared__ float red[2][16][17];
  const int c0 = blockIdx.x * 16, tid = threadIdx.x, c = tid & 15, g = tid >> 4;
  float a = 0.f, b = 0.f;
  for (int j = g; j < 256; j += 16) { a += y[(size_t)j * 2 * D + c0 + c]; b += y[(size_t)j * 2 * D + D + c0 + c]; }
  red[0][g][c] = a; red[1][g][c] = b;
  __syncthreads();
  if (tid < 16) {
    float ta = 0.f, tb = 0.f;
    for (int i = 0; i < 16; ++i) { ta += red[0][i][tid]; tb += red[1][i][tid]; }
    const float attn = rbf(ta), mlp = rbf(rbf(tb) + bf2f(b2[c0 + tid]));
    const float r = rbf(rbf(bf2f(x[c0 + tid]) + attn) + mlp);
    out[c0 + tid] = (unsigned short)(__float_as_uint(r) >> 16);
  }
}

extern "C" void kernel_launch(void* const* d_in, const int* in_sizes, int n_in, void* d_out, int out_size, void* d_ws, size_t ws_size, hipStream_t stream) {
  const unsigned short* x = (const unsigned short*)d_in[0];
  const unsigned short* kc = (const unsigned short*)d_in[1];
  const unsigned short* vc = (const unsigned short*)d_in[2];
  const unsigned short* wq = (const unsigned short*)d_in[3];
  const unsigned short* wo = (const unsigned short*)d_in[4];
  const unsigned short* w1 = (const unsigned short*)d_in[6];
  const unsigned short* b1 = (const unsigned short*)d_in[7];
  const unsigned short* w2 = (const unsigned short*)d_in[8];
  const unsigned short* b2 = (const unsigned short*)d_in[9];
  char* ws = (char*)d_ws;
  unsigned short* q = (unsigned short*)ws;
  float* hbuf = (float*)(ws + 8192);
  float* pml = (float*)(ws + 16384);
  float* po = (float*)(ws + 65536);
  float* y = (float*)(ws + 65536 + 16 * NSPLIT * 256 * 4);
  k_pre<<<64 + 28, 256, 0, stream>>>(x, wq, w1, b1, q, hbuf);
  k_attn<<<HKV * NSPLIT, 256, 0, stream>>>(kc, vc, q, pml, po);
  k_comb<<<256, 256, 0, stream>>>(pml, po, hbuf, wo, w2, y);
  k_fin<<<28, 256, 0, stream>>>(y, x, b2, (unsigned short*)d_out);
}
```

```cpp
#include <hip/hip_runtime.h>
#include <hip/hip_bf16.h>
#include <cstdint>
#include <type_traits>

typedef unsigned u32x4 __attribute__((ext_vector_type(4)));
typedef unsigned u32x2 __attribute__((ext_vector_type(2)));
typedef float f32x4 __attribute__((ext_vector_type(4)));
typedef float f32x2 __attribute__((ext_vector_type(2)));
typedef __bf16 bf16x2 __attribute__((ext_vector_type(2)));
typedef unsigned short u16;
typedef unsigned long long u64;
#define LAS __attribute__((address_space(3)))

#define DBG 0
#define INFL 28
#define SSTAT 56

constexpr int D = 448, F = 1792, DH = 256;
constexpr int NWG = 256, NC = 4;
constexpr int PIECE = 8192, NSLOT = 18;
constexpr int RPR = 64;
constexpr int TAILR = RPR - SSTAT, POOL = 8 * TAILR;
static_assert(POOL >= 24 && INFL % 4 == 0 && INFL >= 20 && INFL <= 32 && SSTAT % 2 == 0 && SSTAT >= 8 && (SSTAT / 2 + 1) * 64 <= 2048, "loader structure");
constexpr unsigned PD_STRIDE = 33 * 128, Y_STRIDE = 29 * 128;
constexpr size_t WS_E = 0, WS_CTR = 4096, WS_Q = 16384, WS_PD = 65536, WS_Y = WS_PD + (size_t)NWG * PD_STRIDE, WS_DBG = 4u << 20;
constexpr unsigned SPIN_CAP = 1u << 22;
constexpr unsigned L_CTL = NSLOT * PIECE;
constexpr unsigned L_LANDED = L_CTL, L_QFLAG = L_CTL + 8, L_QCNT = L_CTL + 12, L_HCNT = L_CTL + 16, L_FREE = L_CTL + 32;
constexpr unsigned L_Q = L_CTL + 128, L_RED = L_Q + 1024, L_REDO = L_RED + 1024, L_ML2 = L_REDO + 512, L_P = L_ML2 + 64;
constexpr unsigned L_ML = L_P + NC * 128, L_XN = L_ML + 64, L_REC = L_XN + 2048, L_END = L_REC + NC * 2 * DH * 4;
static_assert(L_END <= 160 * 1024 && D <= 2 * DH, "LDS");
constexpr float C2 = 0.0625f * 1.4426950408889634f;

__device__ __forceinline__ float lo16(unsigned w) { return __uint_as_float(w << 16); }
__device__ __forceinline__ float hi16(unsigned w) { return __uint_as_float(w & 0xffff0000u); }
__device__ __forceinline__ unsigned pack_bf16(float a, float b) {
  bf16x2 r = {(__bf16)a, (__bf16)b};
  return __builtin_bit_cast(unsigned, r);
}
__device__ __forceinline__ float rbf(float x) { return lo16(pack_bf16(x, x)); }
__device__ __forceinline__ float dot2(unsigned k, unsigned q, float acc) {
  return __builtin_amdgcn_fdot2_f32_bf16(__builtin_bit_cast(bf16x2, k), __builtin_bit_cast(bf16x2, q), acc, false);
}
template <int CTRL> __device__ __forceinline__ unsigned dppu(unsigned x) { return (unsigned)__builtin_amdgcn_mov_dpp((int)x, CTRL, 0xf, 0xf, true); }
template <int CTRL> __device__ __forceinline__ float dpp(float x) { return __uint_as_float(dppu<CTRL>(__float_as_uint(x))); }
constexpr int XOR1 = 0xB1, XOR2 = 0x4E, HMIR = 0x141, MIR = 0x140, ROR8 = 0x128, ROR4 = 0x124;
__device__ __forceinline__ float swap16_add(float a, float b) {
  auto r = __builtin_amdgcn_permlane16_swap(__float_as_uint(a), __float_as_uint(b), false, false);
  return __uint_as_float(r[0]) + __uint_as_float(r[1]);
}
__device__ __forceinline__ float swap32_add(float a, float b) {
  auto r = __builtin_amdgcn_permlane32_swap(__float_as_uint(a), __float_as_uint(b), false, false);
  return __uint_as_float(r[0]) + __uint_as_float(r[1]);
}
__device__ __forceinline__ float row_sum16(float x) { x += dpp<XOR1>(x); x += dpp<XOR2>(x); x += dpp<HMIR>(x); x += dpp<MIR>(x); return x; }
__device__ __forceinline__ float wave_sum(float x) { x = row_sum16(x); x = swap16_add(x, x); return swap32_add(x, x); }
__device__ __forceinline__ float lane_of(float x, int l) { return __builtin_bit_cast(float, __builtin_amdgcn_readlane(__builtin_bit_cast(int, x), l)); }
__device__ __forceinline__ float row_transpose_sum8(const float (&v)[8], u64 bit1) {
  float t0, t1, t2, t3, r;
  asm volatile("s_nop 1\n\t"
      "v_add_f32_dpp %0, %5, %5 row_ror:8 row_mask:0xf bank_mask:0xf\n\tv_add_f32_dpp %1, %6, %6 row_ror:8 row_mask:0xf bank_mask:0xf\n\t"
      "v_add_f32_dpp %2, %7, %7 row_ror:8 row_mask:0xf bank_mask:0xf\n\tv_add_f32_dpp %3, %8, %8 row_ror:8 row_mask:0xf bank_mask:0xf\n\t"
      "v_add_f32_dpp %0, %9, %9 row_ror:8 row_mask:0xf bank_mask:0xc\n\tv_add_f32_dpp %1, %10, %10 row_ror:8 row_mask:0xf bank_mask:0xc\n\t"
      "v_add_f32_dpp %2, %11, %11 row_ror:8 row_mask:0xf bank_mask:0xc\n\tv_add_f32_dpp %3, %12, %12 row_ror:8 row_mask:0xf bank_mask:0xc\n\t"
      "v_add_f32_dpp %0, %0, %0 row_half_mirror row_mask:0xf bank_mask:0x5\n\tv_add_f32_dpp %1, %1, %1 row_half_mirror row_mask:0xf bank_mask:0x5\n\t"
      "v_add_f32_dpp %0, %2, %2 row_half_mirror row_mask:0xf bank_mask:0xa\n\tv_add_f32_dpp %1, %3, %3 row_half_mirror row_mask:0xf bank_mask:0xa\n\t"
      "s_nop 1\n\t"
      "v_add_f32_dpp %2, %0, %0 quad_perm:[2,3,0,1] row_mask:0xf bank_mask:0xf\n\tv_add_f32_dpp %3, %1, %1 quad_perm:[2,3,0,1] row_mask:0xf bank_mask:0xf\n\t"
      "v_cndmask_b32 %2, %2, %3, %13\n\ts_nop 1\n\t"
      "v_add_f32_dpp %4, %2, %2 quad_perm:[1,0,3,2] row_mask:0xf bank_mask:0xf"
      : "=&v"(t0), "=&v"(t1), "=&v"(t2), "=&v"(t3), "=&v"(r)
      : "v"(v[0]), "v"(v[1]), "v"(v[2]), "v"(v[3]), "v"(v[4]), "v"(v[5]), "v"(v[6]), "v"(v[7]), "s"(bit1));
  return r;
}
__device__ __forceinline__ float head_max(float x) {
  float y;
  asm volatile("s_nop 1\n\tv_max_f32_dpp %0, %0, %0 row_ror:8 row_mask:0xf bank_mask:0xf\n\ts_nop 1\n\tv_max_f32_dpp %0, %0, %0 row_half_mirror row_mask:0xf bank_mask:0xf\n\t"
               "s_nop 1\n\tv_max_f32_dpp %0, %0, %0 quad_perm:[2,3,0,1] row_mask:0xf bank_mask:0xf\n\tv_mov_b32 %1, %0\n\ts_nop 1\n\tv_permlane32_swap_b32 %0, %1\n\tv_max_f32 %0, %0, %1"
               : "+v"(x), "=&v"(y));
  return x;
}
__device__ __forceinline__ float rows_max(float x) {
  float y;
  asm volatile("v_mov_b32 %1, %0\n\ts_nop 1\n\tv_permlane16_swap_b32 %0, %1\n\tv_max_f32 %0, %0, %1\n\tv_mov_b32 %1, %0\n\ts_nop 1\n\tv_permlane32_swap_b32 %0, %1\n\tv_max_f32 %0, %0, %1"
               : "+v"(x), "=&v"(y));
  return x;
}
__device__ __forceinline__ float vmax(float a, float b) { float r; asm("v_max_f32 %0, %1, %2" : "=v"(r) : "v"(a), "v"(b)); return r; }
__device__ __forceinline__ void layer_norm(const u32x4& xv, LAS float* xn, int lane) {
  float xf[8];
#pragma unroll
  for (int k = 0; k < 4; ++k) { xf[2 * k] = lo16(xv[k]); xf[2 * k + 1] = hi16(xv[k]); }
  const float live = lane < 56 ? 1.f : 0.f;
  const float mean = wave_sum((((xf[0] + xf[1]) + (xf[2] + xf[3])) + ((xf[4] + xf[5]) + (xf[6] + xf[7]))) * live) * (1.f / D);
  float ss = 0.f;
#pragma unroll
  for (int k = 0; k < 8; ++k) { const float d = xf[k] - mean; ss += d * d; }
  const float var = wave_sum(ss * live) * (1.f / D);
  const float rden = __builtin_amdgcn_rcpf(rbf(__builtin_amdgcn_sqrtf(rbf(rbf(var) + 1e-5f)))) * live, mb = rbf(mean);
#pragma unroll
  for (int k = 0; k < 8; k += 2) {
    const unsigned c = pack_bf16(xf[k] - mb, xf[k + 1] - mb), n = pack_bf16(lo16(c) * rden, hi16(c) * rden);
    xf[k] = lo16(n); xf[k + 1] = hi16(n);
  }
  *(LAS f32x4*)(xn + 8 * lane) = f32x4{xf[0], xf[1], xf[2], xf[3]}; *(LAS f32x4*)(xn + 8 * lane + 4) = f32x4{xf[4], xf[5], xf[6], xf[7]};
}
__device__ __forceinline__ void slice_partials(const u32x4 (&w)[14], const LAS float* xn, LAS float* red, int tid) {
  f32x2 v[4] = {{0.f, 0.f}, {0.f, 0.f}, {0.f, 0.f}, {0.f, 0.f}};
#pragma unroll
  for (int i = 0; i < 14; ++i) {
    const float xr = xn[(tid >> 3) + 32 * i];
#pragma unroll
    for (int k = 0; k < 4; ++k) v[k] += f32x2{lo16(w[i][k]), hi16(w[i][k])} * xr;
  }
  const float a0 = swap32_add(v[0][0], v[2][0]), a1 = swap32_add(v[0][1], v[2][1]), a2 = swap32_add(v[1][0], v[3][0]), a3 = swap32_add(v[1][1], v[3][1]);
  const float b0 = swap16_add(a0, a2), b1 = swap16_add(a1, a3);
  const float t1 = b0 + dpp<ROR8>(b0), t2 = b1 + dpp<ROR8>(b1);
  red[(tid & 7) * 8 + ((tid >> 3) & 7)] = (tid & 8) ? t2 : t1;
}

#define LD_NT(dst, voff, sbase, imm) asm volatile("global_load_dwordx4 %0, %1, %2 offset:%3 nt" : "=v"(dst) : "v"(voff), "s"(sbase), "i"(imm))
#define LD1_NT(dst, voff, sbase, imm) asm volatile("global_load_dword %0, %1, %2 offset:%3 nt" : "=v"(dst) : "v"(voff), "s"(sbase), "i"(imm))
#define LD4_SC1(dst, voff, sbase, imm) asm volatile("global_load_dwordx4 %0, %1, %2 offset:%3 sc1" : "=v"(dst) : "v"(voff), "s"(sbase), "i"(imm) : "memory")
#define ST4_SC1(voff, val, sbase) asm volatile("global_store_dwordx4 %0, %1, %2 sc1\n\ts_nop 1" :: "v"(voff), "v"(val), "s"(sbase) : "memory")
#define WAITV(n, reg) asm volatile("s_waitcnt vmcnt(%1)" : "+v"(reg) : "i"(n))
#define VMCNT(n) asm volatile("s_waitcnt vmcnt(%0)" :: "i"(n) : "memory")
__device__ __forceinline__ void repoll2(u32x4& g0, u32x4& g1, unsigned o0, unsigned o1, const char* base, u64 m0, u64 m1) {
  asm volatile("s_mov_b64 exec, %5\n\tglobal_load_dwordx4 %0, %2, %4 sc1\n\ts_mov_b64 exec, %6\n\tglobal_load_dwordx4 %1, %3, %4 sc1\n\ts_mov_b64 exec, -1\n\ts_waitcnt vmcnt(0)"
               : "+v"(g0), "+v"(g1) : "v"(o0), "v"(o1), "s"(base), "s"(m0), "s"(m1) : "memory");
}
__device__ __forceinline__ void wg_barrier() { asm volatile("s_waitcnt lgkmcnt(0)\n\ts_barrier" ::: "memory"); }
__device__ __forceinline__ void lds_st(unsigned addr, unsigned v) { asm volatile("ds_write_b32 %0, %1" :: "v"(addr), "v"(v) : "memory"); }
__device__ __forceinline__ unsigned lds_ld(unsigned addr) {
  unsigned v; asm volatile("ds_read_b32 %0, %1\n\ts_waitcnt lgkmcnt(0)" : "=v"(v) : "v"(addr) : "memory"); return __builtin_amdgcn_readfirstlane(v); }
template <int N> __device__ __forceinline__ void dma(const char* g, unsigned voff, unsigned lds) {
  if constexpr (N == 4)
    asm volatile("s_mov_b32 m0, %2\n\ts_nop 0\n\tglobal_load_lds_dwordx4 %0, %1 nt\n\tglobal_load_lds_dwordx4 %0, %1 offset:1024 nt\n\t"
                 "global_load_lds_dwordx4 %0, %1 offset:2048 nt\n\tglobal_load_lds_dwordx4 %0, %1 offset:3072 nt" :: "v"(voff), "s"(g), "s"(lds) : "memory");
  else
    asm volatile("s_mov_b32 m0, %2\n\ts_nop 0\n\tglobal_load_lds_dwordx4 %0, %1 nt\n\tglobal_load_lds_dwordx4 %0, %1 offset:1024 nt" :: "v"(voff), "s"(g), "s"(lds) : "memory");
}

#define STAMP(i) do {} while (0)
#define DBGVAL(i, v) do {} while (0)

__global__ __launch_bounds__(320) void fused_kernel(const u16* __restrict__ x, const char* __restrict__ kc, const char* __restrict__ vc,
                                                    const char* __restrict__ wq, const char* __restrict__ wo, const char* __restrict__ w1,
                                                    const u16* __restrict__ b1, const char* __restrict__ w2, const u16* __restrict__ b2,
                                                    unsigned* __restrict__ out, char* __restrict__ ws) {
  __shared__ __attribute__((aligned(1024))) char smem[L_END];
  const unsigned lbase = (unsigned)(uintptr_t)(LAS char*)smem;
  const int b = blockIdx.x, tid = threadIdx.x, lane = tid & 63, wave = __builtin_amdgcn_readfirstlane(tid >> 6);
  const int role = b >> 1;
  const bool own_o = ((b ^ (b >> 3)) & 1) == 0, has_mlp = own_o && (role & 3) == 0 && role < 112;
  const bool make_q = !own_o && (role & 1) == 0, own_out = !own_o && (role & 1) == 1 && role < 112;
  STAMP(0);
  if (wave == NC) {
    __builtin_amdgcn_s_setprio(3);
    if (lane < 8 + NSLOT) lds_st(lbase + L_CTL + lane * 4, lane == 1 ? 0xffffffffu : 0u);
    const unsigned voff = lane * 16;
    const int q = b >> 3;
    unsigned* ctr = (unsigned*)(ws + WS_CTR) + q * 64;
    int nq = 0, slot = 0, gen = 0;
    constexpr int LAG = INFL / 4 - 1;
    int frs = 0;
    auto put = [&](const char* g, int u) {
      if ((u & 1) == 0 && frs < gen) { while ((int)lds_ld(lbase + L_FREE + slot * 4) < gen) __builtin_amdgcn_s_sleep(1); }
      dma<4>(g, voff, lbase + slot * PIECE + (u & 1) * 4096);
      ++nq;
      if (u & 1) { if (++slot == NSLOT) { slot = 0; ++gen; } }
    };
    auto settle = [&](int u) {
      if (u & 1) {
        unsigned fr;
        asm volatile("ds_read_b32 %0, %1\n\ts_waitcnt vmcnt(%2) lgkmcnt(0)" : "=&v"(fr) : "v"(lbase + L_FREE + slot * 4), "i"(INFL - 4) : "memory");
        frs = __builtin_amdgcn_readfirstlane(fr);
      } else VMCNT(INFL - 4);
      lds_st(lbase + L_LANDED, max(nq - LAG, 0));
    };
    unsigned tv = 0;
    auto ticket = [&]() { asm volatile("s_mov_b64 exec, 1\n\tglobal_atomic_add %0, %1, %2, %3 sc0\n\ts_mov_b64 exec, -1" : "=v"(tv) : "v"(0u), "v"(1u), "s"(ctr) : "memory"); };
    size_t voffs = (size_t)b * (1u << 20);
    {
      const char* g = kc + voffs;
      put(g, 0); put(g + 4096, 1);
      wg_barrier();
      for (int p = 1; p < 2 * SSTAT; ++p) { g += PIECE; put(g, 0); settle(0); put(g + 4096, 1); settle(1); }
      g = vc + voffs;
      for (int p = 0; p < 2 * SSTAT - 6; ++p) { put(g, 0); settle(0); put(g + 4096, 1); settle(1); g += PIECE; }
      while (lds_ld(lbase + L_QFLAG) == 0) __builtin_amdgcn_s_sleep(1);
#pragma unroll
      for (int u = 0; u < 8; ++u) { if (u == 8 - INFL / 4) ticket(); put(g + u * 4096, u); settle(u); }
      voffs += (size_t)(SSTAT - 1) * (2 * PIECE);
    }
    for (;;) {
      asm volatile("" : "+v"(tv));
      const int k = __builtin_amdgcn_readfirstlane(tv);
      constexpr int A = POOL > 24 ? POOL - 24 : 0, N4 = (A + 3) / 4;
      int cur, sz;
      if (k < N4) { cur = 4 * k; sz = min(4, A - cur); } else if (k < N4 + 8) { cur = A + 2 * (k - N4); sz = 2; } else { cur = A + 16 + (k - N4 - 8); sz = 1; }
      if (cur >= POOL) break;
      for (; sz > 0; --sz, ++cur) {
        const size_t off = (size_t)(8 * q + cur / TAILR) * (1u << 20) + (size_t)(SSTAT + cur % TAILR) * (2 * PIECE);
        const char* kp = kc + off; const char* vp = vc + voffs;
#pragma unroll
        for (int u = 0; u < 8; ++u) {
          if (u == 8 - INFL / 4 && sz == 1) ticket();
          put(u < 4 ? kp + u * 4096 : vp + (u - 4) * 4096, u);
          settle(u);
        }
        voffs = off;
      }
    }
    lds_st(lbase + L_LANDED + 4, nq);
    STAMP(1); DBGVAL(15, nq + 4);
    slot += 2; if (slot >= NSLOT) { slot -= NSLOT; ++gen; }
    frs = 0;
#pragma unroll
    for (int u = 0; u < 4; ++u) { put(vc + voffs + u * 4096, u); settle(u); }
#pragma unroll
    for (int i = LAG - 1; i >= 0; --i) { VMCNT(4 * i); lds_st(lbase + L_LANDED, nq - i + (i < 4 ? 4 : 0)); }
    STAMP(2);
  } else {
    const int h = b >> 5;
    LAS float* xn = (LAS float*)(smem + L_XN);
    LAS float* red = (LAS float*)(smem + L_RED);
    u32x2 ev, tag;
    auto arrive = [&](unsigned cnt) -> int {
      unsigned arr;
      asm volatile("s_mov_b64 exec, 1\n\tds_add_rtn_u32 %0, %1, %2\n\ts_mov_b64 exec, -1\n\ts_waitcnt lgkmcnt(0)" : "=&v"(arr) : "v"(lbase + cnt), "v"(1u) : "memory");
      return __builtin_amdgcn_readfirstlane(arr);
    };
    auto make_tag = [&]() {
      const u64 e = (u64)(unsigned)__builtin_amdgcn_readfirstlane(ev[1]) << 32 | (unsigned)__builtin_amdgcn_readfirstlane(ev[0]);
      const u64 z = e * 6364136223846793005ull + 1442695040888963407ull;
      tag = u32x2{(unsigned)z, (unsigned)(z >> 32)};
    };
    if (make_q) {
      const int qi = role >> 1;
      if (wave == 0 && (qi & 4) == 0) asm volatile("global_store_dword %0, %1, %2 sc1" :: "v"(0u), "v"(0u), "s"(ws + WS_CTR + ((qi >> 3) * 4 + (qi & 3)) * 256) : "memory");
      u32x4 xv, wv[14];
      asm volatile("global_load_dwordx4 %0, %1, %2" : "=v"(xv) : "v"((lane < 56 ? lane : 55) * 16u), "s"(x));
      const unsigned o = (unsigned)(tid >> 3) * 8192u + (tid & 7) * 16u;
#pragma unroll
      for (int i = 0; i < 14; ++i) LD_NT(wv[i], o, wq + qi * 128 + i * (32 * 8192), 0);
      asm volatile("global_load_dwordx2 %0, %1, %2 sc1" : "=v"(ev) : "v"(0u), "s"(ws + WS_E) : "memory");
      STAMP(4);
      wg_barrier();
      WAITV(15, xv);
      STAMP(7);
      layer_norm(xv, xn, lane);
      STAMP(1);
#pragma unroll
      for (int i = 0; i < 14; ++i) WAITV(1, wv[i]);
      STAMP(2);
      slice_partials(wv, xn, red + wave * 64, tid);
      WAITV(0, ev);
      make_tag();
      if (arrive(L_QCNT) == NC - 1 && lane < 16) {
        const LAS f32x4* rq = (const LAS f32x4*)red + lane;
        const f32x4 s = (rq[0] + rq[16]) + (rq[32] + rq[48]);
        const u32x4 g = {pack_bf16(s[0], s[1]), pack_bf16(s[2], s[3]), tag[0], tag[1]};
        ST4_SC1((unsigned)(qi * 256 + lane * 16), g, ws + WS_Q);
      }
      STAMP(3);
    } else {
      asm volatile("global_load_dwordx2 %0, %1, %2 sc1" : "=v"(ev) : "v"(0u), "s"(ws + WS_E) : "memory");
      wg_barrier();
      WAITV(0, ev);
      make_tag();
    }
    bool bad = false;
    unsigned o_pd = (unsigned)(b * PD_STRIDE + tid * 16), o_y = (unsigned)(role * Y_STRIDE + (tid < 224 ? tid : 0) * 16);
    unsigned o_g0 = own_o ? (unsigned)((h * 32 + (tid >> 4)) * PD_STRIDE + (role & 15) * 256 + (tid & 15) * 16)
                          : (unsigned)((tid >> 2) * Y_STRIDE + (own_out ? role >> 1 : 0) * 64 + (tid & 3) * 16);
    unsigned o_g1 = o_g0 + (own_o ? 16 * PD_STRIDE : 64 * Y_STRIDE);
    asm volatile("" : "+v"(o_pd), "+v"(o_y), "+v"(o_g0), "+v"(o_g1));
    const char* gbase = ws + (own_o ? WS_PD : WS_Y);
    float m = -INFINITY, macc = -INFINITY, l = 0.f;
    f32x2 a0[4], a1[4];
#pragma unroll
    for (int k = 0; k < 4; ++k) { a0[k] = f32x2{0.f, 0.f}; a1[k] = f32x2{0.f, 0.f}; }
    unsigned landed = 0, endq = 0xffffffffu;
    unsigned pc = wave; int slot = wave, gen = 1;
    u32x4 q0, q1;
    LAS u16* scw = (LAS u16*)(smem + L_REC + wave * 2048) + (lane >> 1);
    auto refresh = [&]() {
      u32x2 c; asm volatile("ds_read_b64 %0, %1\n\ts_waitcnt lgkmcnt(0)" : "=v"(c) : "v"(lbase + L_LANDED) : "memory");
      landed = __builtin_amdgcn_readfirstlane(c[0]); endq = __builtin_amdgcn_readfirstlane(c[1]);
    };
    auto advance = [&](int n) { pc += n; slot += n; if (slot >= NSLOT) { slot -= NSLOT; ++gen; } };
    auto k_read = [&](u32x4 (&R)[8]) {
      const LAS u32x4* kt = (const LAS u32x4*)(smem + slot * PIECE) + lane;
#pragma unroll
      for (int i = 0; i < 8; ++i) R[i] = kt[i * 64];
      lds_st(lbase + L_FREE + slot * 4, gen);
    };
    auto k_scores = [&](const u32x4 (&R)[8], int t) {
      float sc[8];
#pragma unroll
      for (int i = 0; i < 8; ++i) {
        float a = 0.f, c = 0.f;
#pragma unroll
        for (int k = 0; k < 4; ++k) { a = dot2(R[i][k], q0[k], a); c = dot2(R[i][k], q1[k], c); }
        sc[i] = swap16_add(a, c);
      }
      const float s = row_transpose_sum8(sc, 0xccccccccccccccccull);
      const unsigned sb = pack_bf16(s, s);
      scw[t * 32] = (u16)sb;
      m = vmax(m, lo16(sb));
    };
    auto v_step = [&](int t) {
      LAS float* pw = (LAS float*)(smem + L_P) + wave * 32;
      const float p = __builtin_amdgcn_exp2f((lo16(scw[t * 32]) - m) * C2);
      pw[lane >> 1] = p;
      l += p;
      f32x4 pp[4];
#pragma unroll
      for (int k = 0; k < 4; ++k) pp[k] = *(const LAS f32x4*)(pw + (lane >> 5) * 16 + 4 * k);
      const LAS u32x4* vt = (const LAS u32x4*)(smem + slot * PIECE) + lane;
#pragma unroll
      for (int hf = 0; hf < 2; ++hf) {
        while (landed < 2 * pc + 1 + hf) refresh();
        u32x4 R[4];
#pragma unroll
        for (int i = 0; i < 4; ++i) R[i] = vt[(hf * 4 + i) * 64];
        if (hf == 1) lds_st(lbase + L_FREE + slot * 4, gen);
#pragma unroll
        for (int i = 0; i < 4; ++i)
#pragma unroll
          for (int k = 0; k < 4; ++k) { const f32x2 v = {lo16(R[i][k]), hi16(R[i][k])}; a0[k] += v * pp[hf][i]; a1[k] += v * pp[2 + hf][i]; }
        asm volatile("" : "+v"(a0[0]), "+v"(a0[1]), "+v"(a0[2]), "+v"(a0[3]), "+v"(a1[0]), "+v"(a1[1]), "+v"(a1[2]), "+v"(a1[3]));
      }
    };
    {
      u32x4 R0[8], R1[8], R2[8];
      while (landed < 2 * pc + 2) refresh();
      k_read(R0); advance(4);
      while (landed < 2 * pc + 2) refresh();
      k_read(R1); advance(4);
      while (landed < 2 * pc + 2) refresh();
      k_read(R2); advance(4);
      if (wave == 0) {
        u32x4 g0 = {0u, 0u, ~tag[0], 0u}, g1 = g0;
        const unsigned qo = (unsigned)(h * 2048 + lane * 32);
        unsigned spins = 0;
        for (;;) {
          const u64 miss = __ballot(!(g0[2] == tag[0] && g0[3] == tag[1] && g1[2] == tag[0] && g1[3] == tag[1]));
          if (miss == 0) break;
          if (++spins > SPIN_CAP) { bad = true; break; }
          repoll2(g0, g1, qo, qo + 16, ws + WS_Q, miss, miss);
        }
        *(LAS u32x4*)(smem + L_Q + lane * 16) = u32x4{g0[0], g0[1], g1[0], g1[1]};
        lds_st(lbase + L_QFLAG, 1u);
        DBGVAL(15, spins);
      } else {
        while (lds_ld(lbase + L_QFLAG) == 0) __builtin_amdgcn_s_sleep(1);
      }
      q0 = *(const LAS u32x4*)(smem + L_Q + (lane & 31) * 16); q1 = *(const LAS u32x4*)(smem + L_Q + 512 + (lane & 31) * 16);
      STAMP(5);
      k_scores(R0, 0); k_scores(R1, 1); k_scores(R2, 2);
      STAMP(6);
    }
    for (int t = 3; t < SSTAT / 2; ++t) {
      u32x4 R[8];
      while (landed < 2 * pc + 2) refresh();
      k_read(R); advance(4);
      k_scores(R, t);
    }
    m = head_max(m); macc = m;
    f32x2 y = {0.f, 0.f}, wof[32];
    unsigned xo = 0, bo = 0;
    const unsigned tc = (tid < 224 ? tid : 223) * 4u;
    if (has_mlp) {
      const int hg = role >> 2;
      u32x4 xv, wv[14]; unsigned b1w;
      asm volatile("global_load_dwordx4 %0, %1, %2" : "=v"(xv) : "v"((lane < 56 ? lane : 55) * 16u), "s"(x));
      asm volatile("global_load_ushort %0, %1, %2" : "=v"(b1w) : "v"(lane * 2u), "s"(b1 + hg * 64));
      const unsigned o = (unsigned)(tid >> 3) * (F * 2u) + (tid & 7) * 16u;
#pragma unroll
      for (int i = 0; i < 14; ++i) LD_NT(wv[i], o, w1 + hg * 128 + i * (32 * F * 2), 0);
      WAITV(14, xv); WAITV(14, b1w);
      layer_norm(xv, xn, lane);
#pragma unroll
      for (int i = 0; i < 14; ++i) WAITV(0, wv[i]);
      slice_partials(wv, xn, red + wave * 64, tid);
      arrive(L_HCNT);
      while (lds_ld(lbase + L_HCNT) < NC) __builtin_amdgcn_s_sleep(1);
      const float hv = rbf(rbf((red[lane] + red[64 + lane]) + (red[128 + lane] + red[192 + lane])) + lo16(b1w));
      const float u = 0.7978845608028654f * (hv + 0.044715f * hv * hv * hv);
      const float e = __builtin_amdgcn_exp2f(u * 2.885390081777927f);
      const float ge = rbf(hv * (1.f - __builtin_amdgcn_rcpf(e + 1.f)));
#pragma unroll
      for (int c = 0; c < 2; ++c) {
        unsigned w[32];
#pragma unroll
        for (int r = 0; r < 32; ++r) LD1_NT(w[r], tc, w2 + (size_t)(hg * 64 + c * 32 + (r & ~3)) * (D * 2), (r & 3) * (D * 2));
#pragma unroll
        for (int r = 0; r < 32; ++r) WAITV(0, w[r]);
#pragma unroll
        for (int r = 0; r < 32; ++r) y += f32x2{lo16(w[r]), hi16(w[r])} * lane_of(ge, c * 32 + r);
      }
    }
    if (own_o) {
      unsigned w[32];
#pragma unroll
      for (int r = 0; r < 32; ++r) LD1_NT(w[r], tc, wo + (size_t)(role * 32 + (r & ~3)) * (D * 2), (r & 3) * (D * 2));
#pragma unroll
      for (int r = 0; r < 32; ++r) WAITV(0, w[r]);
#pragma unroll
      for (int r = 0; r < 32; ++r) wof[r] = f32x2{lo16(w[r]), hi16(w[r])};
    } else {
#pragma unroll
      for (int r = 0; r < 32; ++r) wof[r] = f32x2{0.f, 0.f};
      asm volatile("global_load_dword %0, %2, %3\n\tglobal_load_dword %1, %2, %4\n\ts_waitcnt vmcnt(0)"
                   : "=&v"(xo), "=&v"(bo) : "v"((lane & 3) * 4u), "s"(x + (own_out ? role >> 1 : 0) * 8), "s"(b2 + (own_out ? role >> 1 : 0) * 8));
    }
#pragma unroll
    for (int r = 0; r < 32; ++r) asm volatile("" : "+v"(wof[r]));
    asm volatile("" : "+v"(y));
    const int nv = SSTAT / 2 - (wave >> 1);
    for (int t = 0; t < nv; ++t) { v_step(t); advance(4); }
    if (wave >= 2) { advance(2); v_step(SSTAT / 2 - 1); advance(2); } else { pc -= 2; slot -= 2; if (slot < 0) { slot += NSLOT; --gen; } }
    for (;;) {
      while (landed < 2 * pc + 2 && endq >= 2 * pc + 2) refresh();
      if (endq < 2 * pc + 2) break;
      u32x4 R[8];
      k_read(R); advance(6);
      k_scores(R, 0);
      m = head_max(m);
      const float alpha = __builtin_amdgcn_exp2f((macc - m) * C2);
      macc = m; l *= alpha;
      const float al0 = lane_of(alpha, 0), al1 = lane_of(alpha, 16);
#pragma unroll
      for (int k = 0; k < 4; ++k) { a0[k] *= al0; a1[k] *= al1; }
      v_step(0); advance(2);
    }
    STAMP(8);
    LAS float* rec = (LAS float*)(smem + L_REC);
    LAS f32x2* mlr = (LAS f32x2*)(smem + L_ML);
    {
      f32x4 o0, o1;
#pragma unroll
      for (int k = 0; k < 2; ++k) {
        o0[2 * k] = swap32_add(a0[k][0], a1[k][0]); o0[2 * k + 1] = swap32_add(a0[k][1], a1[k][1]);
        o1[2 * k] = swap32_add(a0[2 + k][0], a1[2 + k][0]); o1[2 * k + 1] = swap32_add(a0[2 + k][1], a1[2 + k][1]);
      }
      *(LAS f32x4*)(rec + wave * 512 + 8 * lane) = o0; *(LAS f32x4*)(rec + wave * 512 + 8 * lane + 4) = o1;
      l += dpp<ROR8>(l); l += dpp<HMIR>(l); l += dpp<XOR2>(l); l = swap32_add(l, l);
      if ((lane & 47) == 0) mlr[(lane >> 4) * NC + wave] = f32x2{m, l};
    }
    wg_barrier();
    const unsigned tag16 = tag[0] & 0xffffu;
    {
      const LAS f32x4* mp = (const LAS f32x4*)(mlr + (tid >> 7) * NC);
      const f32x4 m01 = mp[0], m23 = mp[1];
      const LAS f32x2* ap = (const LAS f32x2*)rec + tid;
      const f32x2 v0 = ap[0], v1 = ap[256], v2 = ap[512], v3 = ap[768];
      const float M = vmax(vmax(m01[0], m01[2]), vmax(m23[0], m23[2]));
      const float f0 = __builtin_amdgcn_exp2f((m01[0] - M) * C2), f1 = __builtin_amdgcn_exp2f((m01[2] - M) * C2);
      const float f2 = __builtin_amdgcn_exp2f((m23[0] - M) * C2), f3 = __builtin_amdgcn_exp2f((m23[2] - M) * C2);
      const f32x2 o = (v0 * f0 + v1 * f1) + (v2 * f2 + v3 * f3);
      const unsigned L = __float_as_uint((f0 * m01[1] + f1 * m01[3]) + (f2 * m23[1] + f3 * m23[3]));
      const unsigned pay = (lane & 2) ? L << 16 : ((lane & 1) ? L : __float_as_uint(M)) & 0xffff0000u;
      const u32x4 gd = {__float_as_uint(o[0]), __float_as_uint(o[1]), pay | tag16, tag[1]};
      ST4_SC1(o_pd, gd, ws + WS_PD);
    }
    STAMP(9);
    u32x4 g0, g1;
    if (own_o) {
      g0 = u32x4{0u, 0u, 0u, ~tag[1]}; g1 = g0;
      unsigned spins = 0;
      for (;;) {
        const u64 m0 = __ballot(!((g0[2] & 0xffffu) == tag16 && g0[3] == tag[1])), m1 = __ballot(!((g1[2] & 0xffffu) == tag16 && g1[3] == tag[1]));
        if ((m0 | m1) == 0) break;
        if (++spins > SPIN_CAP) { bad = true; break; }
        repoll2(g0, g1, o_g0, o_g1, gbase, m0, m1);
      }
      STAMP(10);
      const float ma = hi16(dppu<0x00>(g0[2])), mb = hi16(dppu<0x00>(g1[2]));
      const float Mw = rows_max(vmax(ma, mb));
      const float fa = __builtin_amdgcn_exp2f((ma - Mw) * C2), fb = __builtin_amdgcn_exp2f((mb - Mw) * C2);
      const float la = __uint_as_float((dppu<0x55>(g0[2]) & 0xffff0000u) | (dppu<0xAA>(g0[2]) >> 16));
      const float lb = __uint_as_float((dppu<0x55>(g1[2]) & 0xffff0000u) | (dppu<0xAA>(g1[2]) >> 16));
      const float oe = fa * __uint_as_float(g0[0]) + fb * __uint_as_float(g1[0]), oo = fa * __uint_as_float(g0[1]) + fb * __uint_as_float(g1[1]);
      const float Lp = fa * la + fb * lb;
      const float t = swap32_add(swap16_add(oe, oo), swap16_add(Lp, Lp));
      LAS float* redo = (LAS float*)(smem + L_REDO);
      LAS float* ml2 = (LAS float*)(smem + L_ML2);
      if (lane < 32) redo[(2 * (lane & 15) + (lane >> 4)) * NC + wave] = t;
      if (lane == 32) { ml2[wave] = Mw; ml2[NC + wave] = t; }
      wg_barrier();
      const f32x4 ov = ((const LAS f32x4*)redo)[lane & 31], Mv = ((const LAS f32x4*)ml2)[0], Lv = ((const LAS f32x4*)ml2)[1];
      const float M = vmax(vmax(Mv[0], Mv[1]), vmax(Mv[2], Mv[3]));
      f32x4 fv;
#pragma unroll
      for (int w = 0; w < NC; ++w) fv[w] = __builtin_amdgcn_exp2f((Mv[w] - M) * C2);
      const f32x4 on4 = ov * fv, L4 = Lv * fv;
      const float on = rbf(((on4[0] + on4[1]) + (on4[2] + on4[3])) * __builtin_amdgcn_rcpf((L4[0] + L4[1]) + (L4[2] + L4[3])));
      f32x2 y1 = {0.f, 0.f}, y2 = y1, y3 = y1;
#pragma unroll
      for (int r = 0; r < 32; r += 4) {
        y += wof[r] * lane_of(on, r); y1 += wof[r + 1] * lane_of(on, r + 1); y2 += wof[r + 2] * lane_of(on, r + 2); y3 += wof[r + 3] * lane_of(on, r + 3);
      }
      y = (y + y1) + (y2 + y3);
      if (bad) y[0] = __uint_as_float(0x7fc00000u);
      if (tid < 224) {
        const u32x4 gy = {__float_as_uint(y[0]), __float_as_uint(y[1]), tag[0], tag[1]};
        ST4_SC1(o_y, gy, ws + WS_Y);
      }
      STAMP(11);
    } else if (own_out) {
      g0 = u32x4{0u, 0u, ~tag[0], 0u}; g1 = g0;
      unsigned spins = 0;
      for (;;) {
        const u64 m0 = __ballot(!(g0[2] == tag[0] && g0[3] == tag[1])), m1 = __ballot(!(g1[2] == tag[0] && g1[3] == tag[1]));
        if ((m0 | m1) == 0) break;
        if (++spins > SPIN_CAP) { bad = true; break; }
        const u64 gate = (spins == 1 || (m0 & m1) != ~0ull) ? ~0ull : 1ull;
        repoll2(g0, g1, o_g0, o_g1, gbase, m0 & gate, m1 & gate);
      }
      STAMP(12);
      float s0 = __uint_as_float(g0[0]) + __uint_as_float(g1[0]), s1 = __uint_as_float(g0[1]) + __uint_as_float(g1[1]);
      if (bad) s0 = __uint_as_float(0x7fc00000u);
      s0 += dpp<ROR8>(s0); s1 += dpp<ROR8>(s1); s0 += dpp<ROR4>(s0); s1 += dpp<ROR4>(s1);
      float ss = swap16_add(s0, s1); ss = swap32_add(ss, ss);
      LAS float* redy = (LAS float*)(smem + L_REDO);
      if ((lane & 44) == 0) redy[wave * 8 + 2 * (lane & 3) + (lane >> 4)] = ss;
      wg_barrier();
      if (tid < 4) {
        const LAS f32x2* ry = (const LAS f32x2*)redy + tid;
        const f32x2 t = (ry[0] + ry[4]) + (ry[8] + ry[12]);
        out[(role >> 1) * 4 + tid] = pack_bf16(lo16(xo) + t[0] + lo16(bo), hi16(xo) + t[1] + hi16(bo));
        if (role == 1 && tid == 0) *(u32x2*)(ws + WS_E) = tag;
      }
      STAMP(13);
    }
  }
}

extern "C" void kernel_launch(void* const* d_in, const int* in_sizes, int n_in, void* d_out, int out_size, void* d_ws, size_t ws_size, hipStream_t stream) {
  fused_kernel<<<NWG, 320, 0, stream>>>((const u16*)d_in[0], (const char*)d_in[1], (const char*)d_in[2], (const char*)d_in[3], (const char*)d_in[4],
                                        (const char*)d_in[6], (const u16*)d_in[7], (const char*)d_in[8], (const u16*)d_in[9], (unsigned*)d_out, (char*)d_ws);
}
```

```cpp
#include <hip/hip_runtime.h>
#include <hip/hip_bf16.h>
#include <cstdint>
#include <cstdio>
#include <type_traits>

typedef unsigned u32x4 __attribute__((ext_vector_type(4)));
typedef unsigned u32x2 __attribute__((ext_vector_type(2)));
typedef float f32x4 __attribute__((ext_vector_type(4)));
typedef float f32x2 __attribute__((ext_vector_type(2)));
typedef __bf16 bf16x2 __attribute__((ext_vector_type(2)));
typedef unsigned short u16;
typedef unsigned long long u64;
#define LAS __attribute__((address_space(3)))

#define DBG 0
#define INFL 28
#define SSTAT 56

constexpr int D = 448, F = 1792, DH = 256;
constexpr int NWG = 256, NC = 4;
constexpr int PIECE = 8192, NSLOT = 18;
constexpr int RPR = 64;
constexpr int TAILR = RPR - SSTAT, POOL = 8 * TAILR;
static_assert(POOL >= 24 && INFL % 4 == 0 && INFL >= 20 && INFL <= 32 && SSTAT % 2 == 0 && SSTAT >= 8 && (SSTAT / 2 + 1) * 64 <= 2048, "loader structure");
constexpr unsigned PD_STRIDE = 33 * 128, Y_STRIDE = 29 * 128;
constexpr size_t WS_E = 0, WS_CTR = 4096, WS_Q = 16384, WS_PD = 65536, WS_Y = WS_PD + (size_t)NWG * PD_STRIDE, WS_DBG = 4u << 20;
constexpr unsigned SPIN_CAP = 1u << 22;
constexpr unsigned L_CTL = NSLOT * PIECE;
constexpr unsigned L_LANDED = L_CTL, L_QFLAG = L_CTL + 8, L_QCNT = L_CTL + 12, L_HCNT = L_CTL + 16, L_FREE = L_CTL + 32;
constexpr unsigned L_Q = L_CTL + 128, L_RED = L_Q + 1024, L_REDO = L_RED + 1024, L_ML2 = L_REDO + 512, L_P = L_ML2 + 64;
constexpr unsigned L_ML = L_P + NC * 128, L_XN = L_ML + 64, L_REC = L_XN + 2048, L_END = L_REC + NC * 2 * DH * 4;
static_assert(L_END <= 160 * 1024 && D <= 2 * DH, "LDS");
constexpr float C2 = 0.0625f * 1.4426950408889634f;

__device__ __forceinline__ float lo16(unsigned w) { return __uint_as_float(w << 16); }
__device__ __forceinline__ float hi16(unsigned w) { return __uint_as_float(w & 0xffff0000u); }
__device__ __forceinline__ unsigned pack_bf16(float a, float b) {
  bf16x2 r = {(__bf16)a, (__bf16)b};
  return __builtin_bit_cast(unsigned, r);
}
__device__ __forceinline__ float rbf(float x) { return lo16(pack_bf16(x, x)); }
__device__ __forceinline__ float dot2(unsigned k, unsigned q, float acc) {
  return __builtin_amdgcn_fdot2_f32_bf16(__builtin_bit_cast(bf16x2, k), __builtin_bit_cast(bf16x2, q), acc, false);
}
template <int CTRL> __device__ __forceinline__ unsigned dppu(unsigned x) { return (unsigned)__builtin_amdgcn_mov_dpp((int)x, CTRL, 0xf, 0xf, true); }
template <int CTRL> __device__ __forceinline__ float dpp(float x) { return __uint_as_float(dppu<CTRL>(__float_as_uint(x))); }
constexpr int XOR1 = 0xB1, XOR2 = 0x4E, HMIR = 0x141, MIR = 0x140, ROR8 = 0x128, ROR4 = 0x124;
__device__ __forceinline__ float swap16_add(float a, float b) {
  auto r = __builtin_amdgcn_permlane16_swap(__float_as_uint(a), __float_as_uint(b), false, false);
  return __uint_as_float(r[0]) + __uint_as_float(r[1]);
}
__device__ __forceinline__ float swap32_add(float a, float b) {
  auto r = __builtin_amdgcn_permlane32_swap(__float_as_uint(a), __float_as_uint(b), false, false);
  return __uint_as_float(r[0]) + __uint_as_float(r[1]);
}
__device__ __forceinline__ float row_sum16(float x) { x += dpp<XOR1>(x); x += dpp<XOR2>(x); x += dpp<HMIR>(x); x += dpp<MIR>(x); return x; }
__device__ __forceinline__ float wave_sum(float x) { x = row_sum16(x); x = swap16_add(x, x); return swap32_add(x, x); }
__device__ __forceinline__ float lane_of(float x, int l) { return __builtin_bit_cast(float, __builtin_amdgcn_readlane(__builtin_bit_cast(int, x), l)); }
__device__ __forceinline__ float row_transpose_sum8(const float (&v)[8], u64 bit1) {
  float t0, t1, t2, t3, r;
  asm volatile("s_nop 1\n\t"
      "v_add_f32_dpp %0, %5, %5 row_ror:8 row_mask:0xf bank_mask:0xf\n\tv_add_f32_dpp %1, %6, %6 row_ror:8 row_mask:0xf bank_mask:0xf\n\t"
      "v_add_f32_dpp %2, %7, %7 row_ror:8 row_mask:0xf bank_mask:0xf\n\tv_add_f32_dpp %3, %8, %8 row_ror:8 row_mask:0xf bank_mask:0xf\n\t"
      "v_add_f32_dpp %0, %9, %9 row_ror:8 row_mask:0xf bank_mask:0xc\n\tv_add_f32_dpp %1, %10, %10 row_ror:8 row_mask:0xf bank_mask:0xc\n\t"
      "v_add_f32_dpp %2, %11, %11 row_ror:8 row_mask:0xf bank_mask:0xc\n\tv_add_f32_dpp %3, %12, %12 row_ror:8 row_mask:0xf bank_mask:0xc\n\t"
      "v_add_f32_dpp %0, %0, %0 row_half_mirror row_mask:0xf bank_mask:0x5\n\tv_add_f32_dpp %1, %1, %1 row_half_mirror row_mask:0xf bank_mask:0x5\n\t"
      "v_add_f32_dpp %0, %2, %2 row_half_mirror row_mask:0xf bank_mask:0xa\n\tv_add_f32_dpp %1, %3, %3 row_half_mirror row_mask:0xf bank_mask:0xa\n\t"
      "s_nop 1\n\t"
      "v_add_f32_dpp %2, %0, %0 quad_perm:[2,3,0,1] row_mask:0xf bank_mask:0xf\n\tv_add_f32_dpp %3, %1, %1 quad_perm:[2,3,0,1] row_mask:0xf bank_mask:0xf\n\t"
      "v_cndmask_b32 %2, %2, %3, %13\n\ts_nop 1\n\t"
      "v_add_f32_dpp %4, %2, %2 quad_perm:[1,0,3,2] row_mask:0xf bank_mask:0xf"
      : "=&v"(t0), "=&v"(t1), "=&v"(t2), "=&v"(t3), "=&v"(r)
      : "v"(v[0]), "v"(v[1]), "v"(v[2]), "v"(v[3]), "v"(v[4]), "v"(v[5]), "v"(v[6]), "v"(v[7]), "s"(bit1));
  return r;
}
__device__ __forceinline__ float head_max(float x) {
  float y;
  asm volatile("s_nop 1\n\tv_max_f32_dpp %0, %0, %0 row_ror:8 row_mask:0xf bank_mask:0xf\n\ts_nop 1\n\tv_max_f32_dpp %0, %0, %0 row_half_mirror row_mask:0xf bank_mask:0xf\n\t"
               "s_nop 1\n\tv_max_f32_dpp %0, %0, %0 quad_perm:[2,3,0,1] row_mask:0xf bank_mask:0xf\n\tv_mov_b32 %1, %0\n\ts_nop 1\n\tv_permlane32_swap_b32 %0, %1\n\tv_max_f32 %0, %0, %1"
               : "+v"(x), "=&v"(y));
  return x;
}
__device__ __forceinline__ float rows_max(float x) {
  float y;
  asm volatile("v_mov_b32 %1, %0\n\ts_nop 1\n\tv_permlane16_swap_b32 %0, %1\n\tv_max_f32 %0, %0, %1\n\tv_mov_b32 %1, %0\n\ts_nop 1\n\tv_permlane32_swap_b32 %0, %1\n\tv_max_f32 %0, %0, %1"
               : "+v"(x), "=&v"(y));
  return x;
}
__device__ __forceinline__ float vmax(float a, float b) { float r; asm("v_max_f32 %0, %1, %2" : "=v"(r) : "v"(a), "v"(b)); return r; }
__device__ __forceinline__ void layer_norm(const u32x4& xv, LAS float* xn, int lane) {
  float xf[8];
#pragma unroll
  for (int k = 0; k < 4; ++k) { xf[2 * k] = lo16(xv[k]); xf[2 * k + 1] = hi16(xv[k]); }
  const float live = lane < 56 ? 1.f : 0.f;
  const float mean = wave_sum((((xf[0] + xf[1]) + (xf[2] + xf[3])) + ((xf[4] + xf[5]) + (xf[6] + xf[7]))) * live) * (1.f / D);
  float ss = 0.f;
#pragma unroll
  for (int k = 0; k < 8; ++k) { const float d = xf[k] - mean; ss += d * d; }
  const float var = wave_sum(ss * live) * (1.f / D);
  const float rden = __builtin_amdgcn_rcpf(rbf(__builtin_amdgcn_sqrtf(rbf(rbf(var) + 1e-5f)))) * live, mb = rbf(mean);
#pragma unroll
  for (int k = 0; k < 8; k += 2) {
    const unsigned c = pack_bf16(xf[k] - mb, xf[k + 1] - mb), n = pack_bf16(lo16(c) * rden, hi16(c) * rden);
    xf[k] = lo16(n); xf[k + 1] = hi16(n);
  }
  *(LAS f32x4*)(xn + 8 * lane) = f32x4{xf[0], xf[1], xf[2], xf[3]}; *(LAS f32x4*)(xn + 8 * lane + 4) = f32x4{xf[4], xf[5], xf[6], xf[7]};
}
__device__ __forceinline__ void slice_partials(const u32x4 (&w)[14], const LAS float* xn, LAS float* red, int tid) {
  f32x2 v[4] = {{0.f, 0.f}, {0.f, 0.f}, {0.f, 0.f}, {0.f, 0.f}};
#pragma unroll
  for (int i = 0; i < 14; ++i) {
    const float xr = xn[(tid >> 3) + 32 * i];
#pragma unroll
    for (int k = 0; k < 4; ++k) v[k] += f32x2{lo16(w[i][k]), hi16(w[i][k])} * xr;
  }
  const float a0 = swap32_add(v[0][0], v[2][0]), a1 = swap32_add(v[0][1], v[2][1]), a2 = swap32_add(v[1][0], v[3][0]), a3 = swap32_add(v[1][1], v[3][1]);
  const float b0 = swap16_add(a0, a2), b1 = swap16_add(a1, a3);
  const float t1 = b0 + dpp<ROR8>(b0), t2 = b1 + dpp<ROR8>(b1);
  red[(tid & 7) * 8 + ((tid >> 3) & 7)] = (tid & 8) ? t2 : t1;
}

#define LD_NT(dst, voff, sbase, imm) asm volatile("global_load_dwordx4 %0, %1, %2 offset:%3 nt" : "=v"(dst) : "v"(voff), "s"(sbase), "i"(imm))
#define LD1_NT(dst, voff, sbase, imm) asm volatile("global_load_dword %0, %1, %2 offset:%3 nt" : "=v"(dst) : "v"(voff), "s"(sbase), "i"(imm))
#define LD4_SC1(dst, voff, sbase, imm) asm volatile("global_load_dwordx4 %0, %1, %2 offset:%3 sc1" : "=v"(dst) : "v"(voff), "s"(sbase), "i"(imm) : "memory")
#define ST4_SC1(voff, val, sbase) asm volatile("global_store_dwordx4 %0, %1, %2 sc1\n\ts_nop 1" :: "v"(voff), "v"(val), "s"(sbase) : "memory")
#define WAITV(n, reg) asm volatile("s_waitcnt vmcnt(%1)" : "+v"(reg) : "i"(n))
#define VMCNT(n) asm volatile("s_waitcnt vmcnt(%0)" :: "i"(n) : "memory")
__device__ __forceinline__ void repoll2(u32x4& g0, u32x4& g1, unsigned o0, unsigned o1, const char* base, u64 m0, u64 m1) {
  asm volatile("s_mov_b64 exec, %5\n\tglobal_load_dwordx4 %0, %2, %4 sc1\n\ts_mov_b64 exec, %6\n\tglobal_load_dwordx4 %1, %3, %4 sc1\n\ts_mov_b64 exec, -1\n\ts_waitcnt vmcnt(0)"
               : "+v"(g0), "+v"(g1) : "v"(o0), "v"(o1), "s"(base), "s"(m0), "s"(m1) : "memory");
}
__device__ __forceinline__ void wg_barrier() { asm volatile("s_waitcnt lgkmcnt(0)\n\ts_barrier" ::: "memory"); }
__device__ __forceinline__ void lds_st(unsigned addr, unsigned v) { asm volatile("ds_write_b32 %0, %1" :: "v"(addr), "v"(v) : "memory"); }
__device__ __forceinline__ unsigned lds_ld(unsigned addr) {
  unsigned v; asm volatile("ds_read_b32 %0, %1\n\ts_waitcnt lgkmcnt(0)" : "=v"(v) : "v"(addr) : "memory"); return __builtin_amdgcn_readfirstlane(v); }
template <int N> __device__ __forceinline__ void dma(const char* g, unsigned voff, unsigned lds) {
  if constexpr (N == 4)
    asm volatile("s_mov_b32 m0, %2\n\ts_nop 0\n\tglobal_load_lds_dwordx4 %0, %1 nt\n\tglobal_load_lds_dwordx4 %0, %1 offset:1024 nt\n\t"
                 "global_load_lds_dwordx4 %0, %1 offset:2048 nt\n\tglobal_load_lds_dwordx4 %0, %1 offset:3072 nt" :: "v"(voff), "s"(g), "s"(lds) : "memory");
  else
    asm volatile("s_mov_b32 m0, %2\n\ts_nop 0\n\tglobal_load_lds_dwordx4 %0, %1 nt\n\tglobal_load_lds_dwordx4 %0, %1 offset:1024 nt" :: "v"(voff), "s"(g), "s"(lds) : "memory");
}

#define STAMP(i) do {} while (0)
#define DBGVAL(i, v) do {} while (0)

__global__ __launch_bounds__(320) void fused_kernel(const u16* __restrict__ x, const char* __restrict__ kc, const char* __restrict__ vc,
                                                    const char* __restrict__ wq, const char* __restrict__ wo, const char* __restrict__ w1,
                                                    const u16* __restrict__ b1, const char* __restrict__ w2, const u16* __restrict__ b2,
                                                    unsigned* __restrict__ out, char* __restrict__ ws) {
  __shared__ __attribute__((aligned(1024))) char smem[L_END];
  const unsigned lbase = (unsigned)(uintptr_t)(LAS char*)smem;
  const int b = blockIdx.x, tid = threadIdx.x, lane = tid & 63, wave = __builtin_amdgcn_readfirstlane(tid >> 6);
  const int role = b >> 1;
  const bool own_o = ((b ^ (b >> 3)) & 1) == 0, has_mlp = own_o && (role & 3) == 0 && role < 112;
  const bool make_q = !own_o && (role & 1) == 0, own_out = !own_o && (role & 1) == 1 && role < 112;
  STAMP(0);
  if (wave == NC) {
    __builtin_amdgcn_s_setprio(3);
    if (lane < 8 + NSLOT) lds_st(lbase + L_CTL + lane * 4, lane == 1 ? 0xffffffffu : 0u);
    const unsigned voff = lane * 16;
    const int q = b >> 3;
    unsigned* ctr = (unsigned*)(ws + WS_CTR) + q * 64;
    int nq = 0, slot = 0, gen = 0;
    constexpr int LAG = INFL / 4 - 1;
    int frs = 0;
    auto put = [&](const char* g, int u) {
      if ((u & 1) == 0 && frs < gen) { while ((int)lds_ld(lbase + L_FREE + slot * 4) < gen) __builtin_amdgcn_s_sleep(1); }
      dma<4>(g, voff, lbase + slot * PIECE + (u & 1) * 4096);
      ++nq;
      if (u & 1) { if (++slot == NSLOT) { slot = 0; ++gen; } }
    };
    auto settle = [&](int u) {
      if (u & 1) {
        unsigned fr;
        asm volatile("ds_read_b32 %0, %1\n\ts_waitcnt vmcnt(%2) lgkmcnt(0)" : "=&v"(fr) : "v"(lbase + L_FREE + slot * 4), "i"(INFL - 4) : "memory");
        frs = __builtin_amdgcn_readfirstlane(fr);
      } else VMCNT(INFL - 4);
      lds_st(lbase + L_LANDED, max(nq - LAG, 0));
    };
    unsigned tv = 0;
    auto ticket = [&]() { asm volatile("s_mov_b64 exec, 1\n\tglobal_atomic_add %0, %1, %2, %3 sc0\n\ts_mov_b64 exec, -1" : "=v"(tv) : "v"(0u), "v"(1u), "s"(ctr) : "memory"); };
    size_t voffs = (size_t)b * (1u << 20);
    {
      const char* g = kc + voffs;
      put(g, 0); put(g + 4096, 1);
      wg_barrier();
      for (int p = 1; p < 2 * SSTAT; ++p) { g += PIECE; put(g, 0); settle(0); put(g + 4096, 1); settle(1); }
      g = vc + voffs;
      for (int p = 0; p < 2 * SSTAT - 6; ++p) { put(g, 0); settle(0); put(g + 4096, 1); settle(1); g += PIECE; }
      while (lds_ld(lbase + L_QFLAG) == 0) __builtin_amdgcn_s_sleep(1);
#pragma unroll
      for (int u = 0; u < 8; ++u) { if (u == 8 - INFL / 4) ticket(); put(g + u * 4096, u); settle(u); }
      voffs += (size_t)(SSTAT - 1) * (2 * PIECE);
    }
    for (;;) {
      asm volatile("" : "+v"(tv));
      const int k = __builtin_amdgcn_readfirstlane(tv);
      constexpr int A = POOL > 24 ? POOL - 24 : 0, N4 = (A + 3) / 4;
      int cur, sz;
      if (k < N4) { cur = 4 * k; sz = min(4, A - cur); } else if (k < N4 + 8) { cur = A + 2 * (k - N4); sz = 2; } else { cur = A + 16 + (k - N4 - 8); sz = 1; }
      if (cur >= POOL) break;
      for (; sz > 0; --sz, ++cur) {
        const size_t off = (size_t)(8 * q + cur / TAILR) * (1u << 20) + (size_t)(SSTAT + cur % TAILR) * (2 * PIECE);
        const char* kp = kc + off; const char* vp = vc + voffs;
#pragma unroll
        for (int u = 0; u < 8; ++u) {
          if (u == 8 - INFL / 4 && sz == 1) ticket();
          put(u < 4 ? kp + u * 4096 : vp + (u - 4) * 4096, u);
          settle(u);
        }
        voffs = off;
      }
    }
    lds_st(lbase + L_LANDED + 4, nq);
    STAMP(1); DBGVAL(15, nq + 4);
    slot += 2; if (slot >= NSLOT) { slot -= NSLOT; ++gen; }
    frs = 0;
#pragma unroll
    for (int u = 0; u < 4; ++u) { put(vc + voffs + u * 4096, u); settle(u); }
#pragma unroll
    for (int i = LAG - 1; i >= 0; --i) { VMCNT(4 * i); lds_st(lbase + L_LANDED, nq - i + (i < 4 ? 4 : 0)); }
    STAMP(2);
  } else {
    const int h = b >> 5;
    LAS float* xn = (LAS float*)(smem + L_XN);
    LAS float* red = (LAS float*)(smem + L_RED);
    u32x2 ev, tag;
    auto arrive = [&](unsigned cnt) -> int {
      unsigned arr;
      asm volatile("s_mov_b64 exec, 1\n\tds_add_rtn_u32 %0, %1, %2\n\ts_mov_b64 exec, -1\n\ts_waitcnt lgkmcnt(0)" : "=&v"(arr) : "v"(lbase + cnt), "v"(1u) : "memory");
      return __builtin_amdgcn_readfirstlane(arr);
    };
    auto make_tag = [&]() {
      const u64 e = (u64)(unsigned)__builtin_amdgcn_readfirstlane(ev[1]) << 32 | (unsigned)__builtin_amdgcn_readfirstlane(ev[0]);
      const u64 z = e * 6364136223846793005ull + 1442695040888963407ull;
      tag = u32x2{(unsigned)z, (unsigned)(z >> 32)};
    };
    if (make_q) {
      const int qi = role >> 1;
      if (wave == 0 && (qi & 4) == 0) asm volatile("global_store_dword %0, %1, %2 sc1" :: "v"(0u), "v"(0u), "s"(ws + WS_CTR + ((qi >> 3) * 4 + (qi & 3)) * 256) : "memory");
      u32x4 xv, wv[14];
      asm volatile("global_load_dwordx4 %0, %1, %2" : "=v"(xv) : "v"((lane < 56 ? lane : 55) * 16u), "s"(x));
      const unsigned o = (unsigned)(tid >> 3) * 8192u + (tid & 7) * 16u;
#pragma unroll
      for (int i = 0; i < 14; ++i) LD_NT(wv[i], o, wq + qi * 128 + i * (32 * 8192), 0);
      asm volatile("global_load_dwordx2 %0, %1, %2 sc1" : "=v"(ev) : "v"(0u), "s"(ws + WS_E) : "memory");
      STAMP(4);
      wg_barrier();
      WAITV(15, xv);
      STAMP(7);
      layer_norm(xv, xn, lane);
      STAMP(1);
#pragma unroll
      for (int i = 0; i < 14; ++i) WAITV(1, wv[i]);
      STAMP(2);
      slice_partials(wv, xn, red + wave * 64, tid);
      WAITV(0, ev);
      make_tag();
      if (arrive(L_QCNT) == NC - 1 && lane < 16) {
        const LAS f32x4* rq = (const LAS f32x4*)red + lane;
        const f32x4 s = (rq[0] + rq[16]) + (rq[32] + rq[48]);
        const u32x4 g = {pack_bf16(s[0], s[1]), pack_bf16(s[2], s[3]), tag[0], tag[1]};
        ST4_SC1((unsigned)(qi * 256 + lane * 16), g, ws + WS_Q);
      }
      STAMP(3);
    } else {
      asm volatile("global_load_dwordx2 %0, %1, %2 sc1" : "=v"(ev) : "v"(0u), "s"(ws + WS_E) : "memory");
      wg_barrier();
      WAITV(0, ev);
      make_tag();
    }
    bool bad = false;
    unsigned o_pd = (unsigned)(b * PD_STRIDE + tid * 16), o_y = (unsigned)(role * Y_STRIDE + (tid < 224 ? tid : 0) * 16);
    unsigned o_g0 = own_o ? (unsigned)((h * 32 + (tid >> 4)) * PD_STRIDE + (role & 15) * 256 + (tid & 15) * 16)
                          : (unsigned)((tid >> 2) * Y_STRIDE + (own_out ? role >> 1 : 0) * 64 + (tid & 3) * 16);
    unsigned o_g1 = o_g0 + (own_o ? 16 * PD_STRIDE : 64 * Y_STRIDE);
    asm volatile("" : "+v"(o_pd), "+v"(o_y), "+v"(o_g0), "+v"(o_g1));
    const char* gbase = ws + (own_o ? WS_PD : WS_Y);
    float m = -INFINITY, macc = -INFINITY, l = 0.f;
    f32x2 a0[4], a1[4];
#pragma unroll
    for (int k = 0; k < 4; ++k) { a0[k] = f32x2{0.f, 0.f}; a1[k] = f32x2{0.f, 0.f}; }
    unsigned landed = 0, endq = 0xffffffffu;
    unsigned pc = wave; int slot = wave, gen = 1;
    u32x4 q0, q1;
    LAS u16* scw = (LAS u16*)(smem + L_REC + wave * 2048) + (lane >> 1);
    auto refresh = [&]() {
      u32x2 c; asm volatile("ds_read_b64 %0, %1\n\ts_waitcnt lgkmcnt(0)" : "=v"(c) : "v"(lbase + L_LANDED) : "memory");
      landed = __builtin_amdgcn_readfirstlane(c[0]); endq = __builtin_amdgcn_readfirstlane(c[1]);
    };
    auto advance = [&](int n) { pc += n; slot += n; if (slot >= NSLOT) { slot -= NSLOT; ++gen; } };
    auto k_read = [&](u32x4 (&R)[8]) {
      const LAS u32x4* kt = (const LAS u32x4*)(smem + slot * PIECE) + lane;
#pragma unroll
      for (int i = 0; i < 8; ++i) R[i] = kt[i * 64];
      lds_st(lbase + L_FREE + slot * 4, gen);
    };
    auto k_scores = [&](const u32x4 (&R)[8], int t) {
      float sc[8];
#pragma unroll
      for (int i = 0; i < 8; ++i) {
        float a = 0.f, c = 0.f;
#pragma unroll
        for (int k = 0; k < 4; ++k) { a = dot2(R[i][k], q0[k], a); c = dot2(R[i][k], q1[k], c); }
        sc[i] = swap16_add(a, c);
      }
      const float s = row_transpose_sum8(sc, 0xccccccccccccccccull);
      const unsigned sb = pack_bf16(s, s);
      scw[t * 32] = (u16)sb;
      m = vmax(m, lo16(sb));
    };
    auto v_step = [&](int t) {
      LAS float* pw = (LAS float*)(smem + L_P) + wave * 32;
      const float p = __builtin_amdgcn_exp2f((lo16(scw[t * 32]) - m) * C2);
      pw[lane >> 1] = p;
      l += p;
      f32x4 pp[4];
#pragma unroll
      for (int k = 0; k < 4; ++k) pp[k] = *(const LAS f32x4*)(pw + (lane >> 5) * 16 + 4 * k);
      const LAS u32x4* vt = (const LAS u32x4*)(smem + slot * PIECE) + lane;
#pragma unroll
      for (int hf = 0; hf < 2; ++hf) {
        while (landed < 2 * pc + 1 + hf) refresh();
        u32x4 R[4];
#pragma unroll
        for (int i = 0; i < 4; ++i) R[i] = vt[(hf * 4 + i) * 64];
        if (hf == 1) lds_st(lbase + L_FREE + slot * 4, gen);
#pragma unroll
        for (int i = 0; i < 4; ++i)
#pragma unroll
          for (int k = 0; k < 4; ++k) { const f32x2 v = {lo16(R[i][k]), hi16(R[i][k])}; a0[k] += v * pp[hf][i]; a1[k] += v * pp[2 + hf][i]; }
        asm volatile("" : "+v"(a0[0]), "+v"(a0[1]), "+v"(a0[2]), "+v"(a0[3]), "+v"(a1[0]), "+v"(a1[1]), "+v"(a1[2]), "+v"(a1[3]));
      }
    };
    {
      u32x4 R0[8], R1[8], R2[8];
      while (landed < 2 * pc + 2) refresh();
      k_read(R0); advance(4);
      while (landed < 2 * pc + 2) refresh();
      k_read(R1); advance(4);
      while (landed < 2 * pc + 2) refresh();
      k_read(R2); advance(4);
      if (wave == 0) {
        u32x4 g0 = {0u, 0u, ~tag[0], 0u}, g1 = g0;
        const unsigned qo = (unsigned)(h * 2048 + lane * 32);
        unsigned spins = 0;
        for (;;) {
          const u64 miss = __ballot(!(g0[2] == tag[0] && g0[3] == tag[1] && g1[2] == tag[0] && g1[3] == tag[1]));
          if (miss == 0) break;
          if (++spins > SPIN_CAP) { bad = true; break; }
          repoll2(g0, g1, qo, qo + 16, ws + WS_Q, miss, miss);
        }
        *(LAS u32x4*)(smem + L_Q + lane * 16) = u32x4{g0[0], g0[1], g1[0], g1[1]};
        lds_st(lbase + L_QFLAG, 1u);
        DBGVAL(15, spins);
      } else {
        while (lds_ld(lbase + L_QFLAG) == 0) __builtin_amdgcn_s_sleep(1);
      }
      q0 = *(const LAS u32x4*)(smem + L_Q + (lane & 31) * 16); q1 = *(const LAS u32x4*)(smem + L_Q + 512 + (lane & 31) * 16);
      STAMP(5);
      k_scores(R0, 0); k_scores(R1, 1); k_scores(R2, 2);
      STAMP(6);
    }
    for (int t = 3; t < SSTAT / 2; ++t) {
      u32x4 R[8];
      while (landed < 2 * pc + 2) refresh();
      k_read(R); advance(4);
      k_scores(R, t);
    }
    m = head_max(m); macc = m;
    f32x2 y = {0.f, 0.f}, wof[32];
    unsigned xo = 0, bo = 0;
    const unsigned tc = (tid < 224 ? tid : 223) * 4u;
    if (has_mlp) {
      const int hg = role >> 2;
      u32x4 xv, wv[14]; unsigned b1w;
      asm volatile("global_load_dwordx4 %0, %1, %2" : "=v"(xv) : "v"((lane < 56 ? lane : 55) * 16u), "s"(x));
      asm volatile("global_load_ushort %0, %1, %2" : "=v"(b1w) : "v"(lane * 2u), "s"(b1 + hg * 64));
      const unsigned o = (unsigned)(tid >> 3) * (F * 2u) + (tid & 7) * 16u;
#pragma unroll
      for (int i = 0; i < 14; ++i) LD_NT(wv[i], o, w1 + hg * 128 + i * (32 * F * 2), 0);
      WAITV(14, xv); WAITV(14, b1w);
      layer_norm(xv, xn, lane);
#pragma unroll
      for (int i = 0; i < 14; ++i) WAITV(0, wv[i]);
      slice_partials(wv, xn, red + wave * 64, tid);
      arrive(L_HCNT);
      while (lds_ld(lbase + L_HCNT) < NC) __builtin_amdgcn_s_sleep(1);
      const float hv = rbf(rbf((red[lane] + red[64 + lane]) + (red[128 + lane] + red[192 + lane])) + lo16(b1w));
      const float u = 0.7978845608028654f * (hv + 0.044715f * hv * hv * hv);
      const float e = __builtin_amdgcn_exp2f(u * 2.885390081777927f);
      const float ge = rbf(hv * (1.f - __builtin_amdgcn_rcpf(e + 1.f)));
#pragma unroll
      for (int c = 0; c < 2; ++c) {
        unsigned w[32];
#pragma unroll
        for (int r = 0; r < 32; ++r) LD1_NT(w[r], tc, w2 + (size_t)(hg * 64 + c * 32 + (r & ~3)) * (D * 2), (r & 3) * (D * 2));
#pragma unroll
        for (int r = 0; r < 32; ++r) WAITV(0, w[r]);
#pragma unroll
        for (int r = 0; r < 32; ++r) y += f32x2{lo16(w[r]), hi16(w[r])} * lane_of(ge, c * 32 + r);
      }
    }
    if (own_o) {
      unsigned w[32];
#pragma unroll
      for (int r = 0; r < 32; ++r) LD1_NT(w[r], tc, wo + (size_t)(role * 32 + (r & ~3)) * (D * 2), (r & 3) * (D * 2));
#pragma unroll
      for (int r = 0; r < 32; ++r) WAITV(0, w[r]);
#pragma unroll
      for (int r = 0; r < 32; ++r) wof[r] = f32x2{lo16(w[r]), hi16(w[r])};
    } else {
#pragma unroll
      for (int r = 0; r < 32; ++r) wof[r] = f32x2{0.f, 0.f};
      asm volatile("global_load_dword %0, %2, %3\n\tglobal_load_dword %1, %2, %4\n\ts_waitcnt vmcnt(0)"
                   : "=&v"(xo), "=&v"(bo) : "v"((lane & 3) * 4u), "s"(x + (own_out ? role >> 1 : 0) * 8), "s"(b2 + (own_out ? role >> 1 : 0) * 8));
    }
#pragma unroll
    for (int r = 0; r < 32; ++r) asm volatile("" : "+v"(wof[r]));
    asm volatile("" : "+v"(y));
    const int nv = SSTAT / 2 - (wave >> 1);
    for (int t = 0; t < nv; ++t) { v_step(t); advance(4); }
    if (wave >= 2) { advance(2); v_step(SSTAT / 2 - 1); advance(2); } else { pc -= 2; slot -= 2; if (slot < 0) { slot += NSLOT; --gen; } }
    for (;;) {
      while (landed < 2 * pc + 2 && endq >= 2 * pc + 2) refresh();
      if (endq < 2 * pc + 2) break;
      u32x4 R[8];
      k_read(R); advance(6);
      k_scores(R, 0);
      m = head_max(m);
      const float alpha = __builtin_amdgcn_exp2f((macc - m) * C2);
      macc = m; l *= alpha;
      const float al0 = lane_of(alpha, 0), al1 = lane_of(alpha, 16);
#pragma unroll
      for (int k = 0; k < 4; ++k) { a0[k] *= al0; a1[k] *= al1; }
      v_step(0); advance(2);
    }
    STAMP(8);
    LAS float* rec = (LAS float*)(smem + L_REC);
    LAS f32x2* mlr = (LAS f32x2*)(smem + L_ML);
    {
      f32x4 o0, o1;
#pragma unroll
      for (int k = 0; k < 2; ++k) {
        o0[2 * k] = swap32_add(a0[k][0], a1[k][0]); o0[2 * k + 1] = swap32_add(a0[k][1], a1[k][1]);
        o1[2 * k] = swap32_add(a0[2 + k][0], a1[2 + k][0]); o1[2 * k + 1] = swap32_add(a0[2 + k][1], a1[2 + k][1]);
      }
      *(LAS f32x4*)(rec + wave * 512 + 8 * lane) = o0; *(LAS f32x4*)(rec + wave * 512 + 8 * lane + 4) = o1;
      l += dpp<ROR8>(l); l += dpp<HMIR>(l); l += dpp<XOR2>(l); l = swap32_add(l, l);
      if ((lane & 47) == 0) mlr[(lane >> 4) * NC + wave] = f32x2{m, l};
    }
    wg_barrier();
    const unsigned tag16 = tag[0] & 0xffffu;
    {
      const LAS f32x4* mp = (const LAS f32x4*)(mlr + (tid >> 7) * NC);
      const f32x4 m01 = mp[0], m23 = mp[1];
      const LAS f32x2* ap = (const LAS f32x2*)rec + tid;
      const f32x2 v0 = ap[0], v1 = ap[256], v2 = ap[512], v3 = ap[768];
      const float M = vmax(vmax(m01[0], m01[2]), vmax(m23[0], m23[2]));
      const float f0 = __builtin_amdgcn_exp2f((m01[0] - M) * C2), f1 = __builtin_amdgcn_exp2f((m01[2] - M) * C2);
      const float f2 = __builtin_amdgcn_exp2f((m23[0] - M) * C2), f3 = __builtin_amdgcn_exp2f((m23[2] - M) * C2);
      const f32x2 o = (v0 * f0 + v1 * f1) + (v2 * f2 + v3 * f3);
      const unsigned L = __float_as_uint((f0 * m01[1] + f1 * m01[3]) + (f2 * m23[1] + f3 * m23[3]));
      const unsigned pay = (lane & 2) ? L << 16 : ((lane & 1) ? L : __float_as_uint(M)) & 0xffff0000u;
      const u32x4 gd = {__float_as_uint(o[0]), __float_as_uint(o[1]), pay | tag16, tag[1]};
      ST4_SC1(o_pd, gd, ws + WS_PD);
    }
    STAMP(9);
    u32x4 g0, g1;
    if (own_o) {
      g0 = u32x4{0u, 0u, 0u, ~tag[1]}; g1 = g0;
      unsigned spins = 0;
      for (;;) {
        const u64 m0 = __ballot(!((g0[2] & 0xffffu) == tag16 && g0[3] == tag[1])), m1 = __ballot(!((g1[2] & 0xffffu) == tag16 && g1[3] == tag[1]));
        if ((m0 | m1) == 0) break;
        if (++spins > SPIN_CAP) { bad = true; break; }
        repoll2(g0, g1, o_g0, o_g1, gbase, m0, m1);
      }
      STAMP(10);
      const float ma = hi16(dppu<0x00>(g0[2])), mb = hi16(dppu<0x00>(g1[2]));
      const float Mw = rows_max(vmax(ma, mb));
      const float fa = __builtin_amdgcn_exp2f((ma - Mw) * C2), fb = __builtin_amdgcn_exp2f((mb - Mw) * C2);
      const float la = __uint_as_float((dppu<0x55>(g0[2]) & 0xffff0000u) | (dppu<0xAA>(g0[2]) >> 16));
      const float lb = __uint_as_float((dppu<0x55>(g1[2]) & 0xffff0000u) | (dppu<0xAA>(g1[2]) >> 16));
      const float oe = fa * __uint_as_float(g0[0]) + fb * __uint_as_float(g1[0]), oo = fa * __uint_as_float(g0[1]) + fb * __uint_as_float(g1[1]);
      const float Lp = fa * la + fb * lb;
      const float t = swap32_add(swap16_add(oe, oo), swap16_add(Lp, Lp));
      LAS float* redo = (LAS float*)(smem + L_REDO);
      LAS float* ml2 = (LAS float*)(smem + L_ML2);
      if (lane < 32) redo[(2 * (lane & 15) + (lane >> 4)) * NC + wave] = t;
      if (lane == 32) { ml2[wave] = Mw; ml2[NC + wave] = t; }
      wg_barrier();
      const f32x4 ov = ((const LAS f32x4*)redo)[lane & 31], Mv = ((const LAS f32x4*)ml2)[0], Lv = ((const LAS f32x4*)ml2)[1];
      const float M = vmax(vmax(Mv[0], Mv[1]), vmax(Mv[2], Mv[3]));
      f32x4 fv;
#pragma unroll
      for (int w = 0; w < NC; ++w) fv[w] = __builtin_amdgcn_exp2f((Mv[w] - M) * C2);
      const f32x4 on4 = ov * fv, L4 = Lv * fv;
      const float on = rbf(((on4[0] + on4[1]) + (on4[2] + on4[3])) * __builtin_amdgcn_rcpf((L4[0] + L4[1]) + (L4[2] + L4[3])));
      f32x2 y1 = {0.f, 0.f}, y2 = y1, y3 = y1;
#pragma unroll
      for (int r = 0; r < 32; r += 4) {
        y += wof[r] * lane_of(on, r); y1 += wof[r + 1] * lane_of(on, r + 1); y2 += wof[r + 2] * lane_of(on, r + 2); y3 += wof[r + 3] * lane_of(on, r + 3);
      }
      y = (y + y1) + (y2 + y3);
      if (bad) y[0] = __uint_as_float(0x7fc00000u);
      if (tid < 224) {
        const u32x4 gy = {__float_as_uint(y[0]), __float_as_uint(y[1]), tag[0], tag[1]};
        ST4_SC1(o_y, gy, ws + WS_Y);
      }
      STAMP(11);
    } else if (own_out) {
      g0 = u32x4{0u, 0u, ~tag[0], 0u}; g1 = g0;
      unsigned spins = 0;
      for (;;) {
        const u64 m0 = __ballot(!(g0[2] == tag[0] && g0[3] == tag[1])), m1 = __ballot(!(g1[2] == tag[0] && g1[3] == tag[1]));
        if ((m0 | m1) == 0) break;
        if (++spins > SPIN_CAP) { bad = true; break; }
        const u64 gate = (spins == 1 || (m0 & m1) != ~0ull) ? ~0ull : 1ull;
        repoll2(g0, g1, o_g0, o_g1, gbase, m0 & gate, m1 & gate);
      }
      STAMP(12);
      float s0 = __uint_as_float(g0[0]) + __uint_as_float(g1[0]), s1 = __uint_as_float(g0[1]) + __uint_as_float(g1[1]);
      if (bad) s0 = __uint_as_float(0x7fc00000u);
      s0 += dpp<ROR8>(s0); s1 += dpp<ROR8>(s1); s0 += dpp<ROR4>(s0); s1 += dpp<ROR4>(s1);
      float ss = swap16_add(s0, s1); ss = swap32_add(ss, ss);
      LAS float* redy = (LAS float*)(smem + L_REDO);
      if ((lane & 44) == 0) redy[wave * 8 + 2 * (lane & 3) + (lane >> 4)] = ss;
      wg_barrier();
      if (tid < 4) {
        const LAS f32x2* ry = (const LAS f32x2*)redy + tid;
        const f32x2 t = (ry[0] + ry[4]) + (ry[8] + ry[12]);
        out[(role >> 1) * 4 + tid] = pack_bf16(lo16(xo) + t[0] + lo16(bo), hi16(xo) + t[1] + hi16(bo));
        if (role == 1 && tid == 0) *(u32x2*)(ws + WS_E) = tag;
      }
      STAMP(13);
    }
  }
}

extern "C" void kernel_launch(void* const* d_in, const int* in_sizes, int n_in, void* d_out, int out_size, void* d_ws, size_t ws_size, hipStream_t stream) {
  for (int i = 0; i < n_in; ++i) { hipPointerAttribute_t a; hipError_t e = hipPointerGetAttributes(&a, d_in[i]); fprintf(stderr, "in%d %p size %d err %d type %d flags %u managed %d\n", i, d_in[i], in_sizes[i], (int)e, (int)a.type, a.allocationFlags, a.isManaged);
    void* base; size_t sz; if (hipMemGetAddressRange(&base, &sz, d_in[i]) == hipSuccess) fprintf(stderr, "    range base %p size %zu\n", base, sz); }
  fprintf(stderr, "out %p ws %p\n", d_out, d_ws);
  fused_kernel<<<NWG, 320, 0, stream>>>((const u16*)d_in[0], (const char*)d_in[1], (const char*)d_in[2], (const char*)d_in[3], (const char*)d_in[4],
                                        (const char*)d_in[6], (const u16*)d_in[7], (const char*)d_in[8], (const u16*)d_in[9], (unsigned*)d_out, (char*)d_ws);
}
```

```cpp
#include <hip/hip_runtime.h>
#include <hip/hip_bf16.h>
#include <cstdint>
#include <type_traits>

typedef unsigned u32x4 __attribute__((ext_vector_type(4)));
typedef unsigned u32x2 __attribute__((ext_vector_type(2)));
typedef float f32x4 __attribute__((ext_vector_type(4)));
typedef float f32x2 __attribute__((ext_vector_type(2)));
typedef __bf16 bf16x2 __attribute__((ext_vector_type(2)));
typedef unsigned short u16;
typedef unsigned long long u64;
#define LAS __attribute__((address_space(3)))

#define DBG 0
#define INFL 28
#define SSTAT 56

constexpr int D = 448, F = 1792, DH = 256;
constexpr int NWG = 256, NC = 4;
constexpr int PIECE = 8192, NSLOT = 18;
constexpr int RPR = 64;
constexpr int TAILR = RPR - SSTAT, POOL = 8 * TAILR;
static_assert(POOL >= 24 && INFL % 4 == 0 && INFL >= 20 && INFL <= 32 && SSTAT % 2 == 0 && SSTAT >= 8 && (SSTAT / 2 + 1) * 64 <= 2048, "loader structure");
constexpr unsigned PD_STRIDE = 33 * 128, Y_STRIDE = 29 * 128;
constexpr size_t WS_E = 0, WS_CTR = 4096, WS_Q = 16384, WS_PD = 65536, WS_Y = WS_PD + (size_t)NWG * PD_STRIDE, WS_DBG = 4u << 20;
constexpr unsigned SPIN_CAP = 1u << 22;
constexpr unsigned L_CTL = NSLOT * PIECE;
constexpr unsigned L_LANDED = L_CTL, L_QFLAG = L_CTL + 8, L_QCNT = L_CTL + 12, L_HCNT = L_CTL + 16, L_FREE = L_CTL + 32;
constexpr unsigned L_Q = L_CTL + 128, L_RED = L_Q + 1024, L_REDO = L_RED + 1024, L_ML2 = L_REDO + 512, L_P = L_ML2 + 64;
constexpr unsigned L_ML = L_P + NC * 128, L_XN = L_ML + 64, L_REC = L_XN + 2048, L_END = L_REC + NC * 2 * DH * 4;
static_assert(L_END <= 160 * 1024 && D <= 2 * DH, "LDS");
constexpr float C2 = 0.0625f * 1.4426950408889634f;

__device__ __forceinline__ float lo16(unsigned w) { return __uint_as_float(w << 16); }
__device__ __forceinline__ float hi16(unsigned w) { return __uint_as_float(w & 0xffff0000u); }
__device__ __forceinline__ unsigned pack_bf16(float a, float b) {
  bf16x2 r = {(__bf16)a, (__bf16)b};
  return __builtin_bit_cast(unsigned, r);
}
__device__ __forceinline__ float rbf(float x) { return lo16(pack_bf16(x, x)); }
__device__ __forceinline__ float dot2(unsigned k, unsigned q, float acc) {
  return __builtin_amdgcn_fdot2_f32_bf16(__builtin_bit_cast(bf16x2, k), __builtin_bit_cast(bf16x2, q), acc, false);
}
template <int CTRL> __device__ __forceinline__ unsigned dppu(unsigned x) { return (unsigned)__builtin_amdgcn_mov_dpp((int)x, CTRL, 0xf, 0xf, true); }
template <int CTRL> __device__ __forceinline__ float dpp(float x) { return __uint_as_float(dppu<CTRL>(__float_as_uint(x))); }
constexpr int XOR1 = 0xB1, XOR2 = 0x4E, HMIR = 0x141, MIR = 0x140, ROR8 = 0x128, ROR4 = 0x124;
__device__ __forceinline__ float swap16_add(float a, float b) {
  auto r = __builtin_amdgcn_permlane16_swap(__float_as_uint(a), __float_as_uint(b), false, false);
  return __uint_as_float(r[0]) + __uint_as_float(r[1]);
}
__device__ __forceinline__ float swap32_add(float a, float b) {
  auto r = __builtin_amdgcn_permlane32_swap(__float_as_uint(a), __float_as_uint(b), false, false);
  return __uint_as_float(r[0]) + __uint_as_float(r[1]);
}
__device__ __forceinline__ float row_sum16(float x) { x += dpp<XOR1>(x); x += dpp<XOR2>(x); x += dpp<HMIR>(x); x += dpp<MIR>(x); return x; }
__device__ __forceinline__ float wave_sum(float x) { x = row_sum16(x); x = swap16_add(x, x); return swap32_add(x, x); }
__device__ __forceinline__ float lane_of(float x, int l) { return __builtin_bit_cast(float, __builtin_amdgcn_readlane(__builtin_bit_cast(int, x), l)); }
__device__ __forceinline__ float row_transpose_sum8(const float (&v)[8], u64 bit1) {
  float t0, t1, t2, t3, r;
  asm volatile("s_nop 1\n\t"
      "v_add_f32_dpp %0, %5, %5 row_ror:8 row_mask:0xf bank_mask:0xf\n\tv_add_f32_dpp %1, %6, %6 row_ror:8 row_mask:0xf bank_mask:0xf\n\t"
      "v_add_f32_dpp %2, %7, %7 row_ror:8 row_mask:0xf bank_mask:0xf\n\tv_add_f32_dpp %3, %8, %8 row_ror:8 row_mask:0xf bank_mask:0xf\n\t"
      "v_add_f32_dpp %0, %9, %9 row_ror:8 row_mask:0xf bank_mask:0xc\n\tv_add_f32_dpp %1, %10, %10 row_ror:8 row_mask:0xf bank_mask:0xc\n\t"
      "v_add_f32_dpp %2, %11, %11 row_ror:8 row_mask:0xf bank_mask:0xc\n\tv_add_f32_dpp %3, %12, %12 row_ror:8 row_mask:0xf bank_mask:0xc\n\t"
      "v_add_f32_dpp %0, %0, %0 row_half_mirror row_mask:0xf bank_mask:0x5\n\tv_add_f32_dpp %1, %1, %1 row_half_mirror row_mask:0xf bank_mask:0x5\n\t"
      "v_add_f32_dpp %0, %2, %2 row_half_mirror row_mask:0xf bank_mask:0xa\n\tv_add_f32_dpp %1, %3, %3 row_half_mirror row_mask:0xf bank_mask:0xa\n\t"
      "s_nop 1\n\t"
      "v_add_f32_dpp %2, %0, %0 quad_perm:[2,3,0,1] row_mask:0xf bank_mask:0xf\n\tv_add_f32_dpp %3, %1, %1 quad_perm:[2,3,0,1] row_mask:0xf bank_mask:0xf\n\t"
      "v_cndmask_b32 %2, %2, %3, %13\n\ts_nop 1\n\t"
      "v_add_f32_dpp %4, %2, %2 quad_perm:[1,0,3,2] row_mask:0xf bank_mask:0xf"
      : "=&v"(t0), "=&v"(t1), "=&v"(t2), "=&v"(t3), "=&v"(r)
      : "v"(v[0]), "v"(v[1]), "v"(v[2]), "v"(v[3]), "v"(v[4]), "v"(v[5]), "v"(v[6]), "v"(v[7]), "s"(bit1));
  return r;
}
__device__ __forceinline__ float head_max(float x) {
  float y;
  asm volatile("s_nop 1\n\tv_max_f32_dpp %0, %0, %0 row_ror:8 row_mask:0xf bank_mask:0xf\n\ts_nop 1\n\tv_max_f32_dpp %0, %0, %0 row_half_mirror row_mask:0xf bank_mask:0xf\n\t"
               "s_nop 1\n\tv_max_f32_dpp %0, %0, %0 quad_perm:[2,3,0,1] row_mask:0xf bank_mask:0xf\n\tv_mov_b32 %1, %0\n\ts_nop 1\n\tv_permlane32_swap_b32 %0, %1\n\tv_max_f32 %0, %0, %1"
               : "+v"(x), "=&v"(y));
  return x;
}
__device__ __forceinline__ float rows_max(float x) {
  float y;
  asm volatile("v_mov_b32 %1, %0\n\ts_nop 1\n\tv_permlane16_swap_b32 %0, %1\n\tv_max_f32 %0, %0, %1\n\tv_mov_b32 %1, %0\n\ts_nop 1\n\tv_permlane32_swap_b32 %0, %1\n\tv_max_f32 %0, %0, %1"
               : "+v"(x), "=&v"(y));
  return x;
}
__device__ __forceinline__ float vmax(float a, float b) { float r; asm("v_max_f32 %0, %1, %2" : "=v"(r) : "v"(a), "v"(b)); return r; }
__device__ __forceinline__ void layer_norm(const u32x4& xv, LAS float* xn, int lane) {
  float xf[8];
#pragma unroll
  for (int k = 0; k < 4; ++k) { xf[2 * k] = lo16(xv[k]); xf[2 * k + 1] = hi16(xv[k]); }
  const float live = lane < 56 ? 1.f : 0.f;
  const float mean = wave_sum((((xf[0] + xf[1]) + (xf[2] + xf[3])) + ((xf[4] + xf[5]) + (xf[6] + xf[7]))) * live) * (1.f / D);
  float ss = 0.f;
#pragma unroll
  for (int k = 0; k < 8; ++k) { const float d = xf[k] - mean; ss += d * d; }
  const float var = wave_sum(ss * live) * (1.f / D);
  const float rden = __builtin_amdgcn_rcpf(rbf(__builtin_amdgcn_sqrtf(rbf(rbf(var) + 1e-5f)))) * live, mb = rbf(mean);
#pragma unroll
  for (int k = 0; k < 8; k += 2) {
    const unsigned c = pack_bf16(xf[k] - mb, xf[k + 1] - mb), n = pack_bf16(lo16(c) * rden, hi16(c) * rden);
    xf[k] = lo16(n); xf[k + 1] = hi16(n);
  }
  *(LAS f32x4*)(xn + 8 * lane) = f32x4{xf[0], xf[1], xf[2], xf[3]}; *(LAS f32x4*)(xn + 8 * lane + 4) = f32x4{xf[4], xf[5], xf[6], xf[7]};
}
__device__ __forceinline__ void slice_partials(const u32x4 (&w)[14], const LAS float* xn, LAS float* red, int tid) {
  f32x2 v[4] = {{0.f, 0.f}, {0.f, 0.f}, {0.f, 0.f}, {0.f, 0.f}};
#pragma unroll
  for (int i = 0; i < 14; ++i) {
    const float xr = xn[(tid >> 3) + 32 * i];
#pragma unroll
    for (int k = 0; k < 4; ++k) v[k] += f32x2{lo16(w[i][k]), hi16(w[i][k])} * xr;
  }
  const float a0 = swap32_add(v[0][0], v[2][0]), a1 = swap32_add(v[0][1], v[2][1]), a2 = swap32_add(v[1][0], v[3][0]), a3 = swap32_add(v[1][1], v[3][1]);
  const float b0 = swap16_add(a0, a2), b1 = swap16_add(a1, a3);
  const float t1 = b0 + dpp<ROR8>(b0), t2 = b1 + dpp<ROR8>(b1);
  red[(tid & 7) * 8 + ((tid >> 3) & 7)] = (tid & 8) ? t2 : t1;
}

#define LD_NT(dst, voff, sbase, imm) asm volatile("global_load_dwordx4 %0, %1, %2 offset:%3 nt" : "=v"(dst) : "v"(voff), "s"(sbase), "i"(imm))
#define LD1_NT(dst, voff, sbase, imm) asm volatile("global_load_dword %0, %1, %2 offset:%3 nt" : "=v"(dst) : "v"(voff), "s"(sbase), "i"(imm))
#define LD4_SC1(dst, voff, sbase, imm) asm volatile("global_load_dwordx4 %0, %1, %2 offset:%3 sc1" : "=v"(dst) : "v"(voff), "s"(sbase), "i"(imm) : "memory")
#define ST4_SC1(voff, val, sbase) asm volatile("global_store_dwordx4 %0, %1, %2 sc1\n\ts_nop 1" :: "v"(voff), "v"(val), "s"(sbase) : "memory")
#define WAITV(n, reg) asm volatile("s_waitcnt vmcnt(%1)" : "+v"(reg) : "i"(n))
#define VMCNT(n) asm volatile("s_waitcnt vmcnt(%0)" :: "i"(n) : "memory")
__device__ __forceinline__ void repoll2(u32x4& g0, u32x4& g1, unsigned o0, unsigned o1, const char* base, u64 m0, u64 m1) {
  asm volatile("s_mov_b64 exec, %5\n\tglobal_load_dwordx4 %0, %2, %4 sc1\n\ts_mov_b64 exec, %6\n\tglobal_load_dwordx4 %1, %3, %4 sc1\n\ts_mov_b64 exec, -1\n\ts_waitcnt vmcnt(0)"
               : "+v"(g0), "+v"(g1) : "v"(o0), "v"(o1), "s"(base), "s"(m0), "s"(m1) : "memory");
}
__device__ __forceinline__ void wg_barrier() { asm volatile("s_waitcnt lgkmcnt(0)\n\ts_barrier" ::: "memory"); }
__device__ __forceinline__ void lds_st(unsigned addr, unsigned v) { asm volatile("ds_write_b32 %0, %1" :: "v"(addr), "v"(v) : "memory"); }
__device__ __forceinline__ unsigned lds_ld(unsigned addr) {
  unsigned v; asm volatile("ds_read_b32 %0, %1\n\ts_waitcnt lgkmcnt(0)" : "=v"(v) : "v"(addr) : "memory"); return __builtin_amdgcn_readfirstlane(v); }
#define VB0 128
#define KB0 256
#define PLAINPOL ""
__device__ __forceinline__ void dma4(const char* g, unsigned voff, unsigned lds, bool plain) {
  if (plain)
    asm volatile("s_mov_b32 m0, %2\n\ts_nop 0\n\tglobal_load_lds_dwordx4 %0, %1 " PLAINPOL "\n\tglobal_load_lds_dwordx4 %0, %1 offset:1024 " PLAINPOL "\n\t"
                 "global_load_lds_dwordx4 %0, %1 offset:2048 " PLAINPOL "\n\tglobal_load_lds_dwordx4 %0, %1 offset:3072 " PLAINPOL :: "v"(voff), "s"(g), "s"(lds) : "memory");
  else
    asm volatile("s_mov_b32 m0, %2\n\ts_nop 0\n\tglobal_load_lds_dwordx4 %0, %1 nt\n\tglobal_load_lds_dwordx4 %0, %1 offset:1024 nt\n\t"
                 "global_load_lds_dwordx4 %0, %1 offset:2048 nt\n\tglobal_load_lds_dwordx4 %0, %1 offset:3072 nt" :: "v"(voff), "s"(g), "s"(lds) : "memory");
}

#define STAMP(i) do {} while (0)
#define DBGVAL(i, v) do {} while (0)

__global__ __launch_bounds__(320) void fused_kernel(const u16* __restrict__ x, const char* __restrict__ kc, const char* __restrict__ vc,
                                                    const char* __restrict__ wq, const char* __restrict__ wo, const char* __restrict__ w1,
                                                    const u16* __restrict__ b1, const char* __restrict__ w2, const u16* __restrict__ b2,
                                                    unsigned* __restrict__ out, char* __restrict__ ws) {
  __shared__ __attribute__((aligned(1024))) char smem[L_END];
  const unsigned lbase = (unsigned)(uintptr_t)(LAS char*)smem;
  const int b = blockIdx.x, tid = threadIdx.x, lane = tid & 63, wave = __builtin_amdgcn_readfirstlane(tid >> 6);
  const int role = b >> 1;
  const bool own_o = ((b ^ (b >> 3)) & 1) == 0, has_mlp = own_o && (role & 3) == 0 && role < 112;
  const bool make_q = !own_o && (role & 1) == 0, own_out = !own_o && (role & 1) == 1 && role < 112;
  STAMP(0);
  if (wave == NC) {
    __builtin_amdgcn_s_setprio(3);
    if (lane < 8 + NSLOT) lds_st(lbase + L_CTL + lane * 4, lane == 1 ? 0xffffffffu : 0u);
    const unsigned voff = lane * 16;
    const int q = b >> 3;
    unsigned* ctr = (unsigned*)(ws + WS_CTR) + q * 64;
    int nq = 0, slot = 0, gen = 0;
    constexpr int LAG = INFL / 4 - 1;
    int frs = 0;
    auto put = [&](const char* g, int u, bool plain) {
      if ((u & 1) == 0 && frs < gen) { while ((int)lds_ld(lbase + L_FREE + slot * 4) < gen) __builtin_amdgcn_s_sleep(1); }
      dma4(g, voff, lbase + slot * PIECE + (u & 1) * 4096, plain);
      ++nq;
      if (u & 1) { if (++slot == NSLOT) { slot = 0; ++gen; } }
    };
    auto settle = [&](int u) {
      if (u & 1) {
        unsigned fr;
        asm volatile("ds_read_b32 %0, %1\n\ts_waitcnt vmcnt(%2) lgkmcnt(0)" : "=&v"(fr) : "v"(lbase + L_FREE + slot * 4), "i"(INFL - 4) : "memory");
        frs = __builtin_amdgcn_readfirstlane(fr);
      } else VMCNT(INFL - 4);
      lds_st(lbase + L_LANDED, max(nq - LAG, 0));
    };
    unsigned tv = 0;
    auto ticket = [&]() { asm volatile("s_mov_b64 exec, 1\n\tglobal_atomic_add %0, %1, %2, %3 sc0\n\ts_mov_b64 exec, -1" : "=v"(tv) : "v"(0u), "v"(1u), "s"(ctr) : "memory"); };
    size_t voffs = (size_t)b * (1u << 20);
    {
      const char* g = kc + voffs;
      const bool pk = b >= KB0, pv = b >= VB0;
      put(g, 0, pk); put(g + 4096, 1, pk);
      wg_barrier();
      for (int p = 1; p < 2 * SSTAT; ++p) { g += PIECE; put(g, 0, pk); settle(0); put(g + 4096, 1, pk); settle(1); }
      g = vc + voffs;
      for (int p = 0; p < 2 * SSTAT - 6; ++p) { put(g, 0, pv); settle(0); put(g + 4096, 1, pv); settle(1); g += PIECE; }
      while (lds_ld(lbase + L_QFLAG) == 0) __builtin_amdgcn_s_sleep(1);
#pragma unroll
      for (int u = 0; u < 8; ++u) { if (u == 8 - INFL / 4) ticket(); put(g + u * 4096, u, pv); settle(u); }
      voffs += (size_t)(SSTAT - 1) * (2 * PIECE);
    }
    for (;;) {
      asm volatile("" : "+v"(tv));
      const int k = __builtin_amdgcn_readfirstlane(tv);
      constexpr int A = POOL > 24 ? POOL - 24 : 0, N4 = (A + 3) / 4;
      int cur, sz;
      if (k < N4) { cur = 4 * k; sz = min(4, A - cur); } else if (k < N4 + 8) { cur = A + 2 * (k - N4); sz = 2; } else { cur = A + 16 + (k - N4 - 8); sz = 1; }
      if (cur >= POOL) break;
      for (; sz > 0; --sz, ++cur) {
        const size_t off = (size_t)(8 * q + cur / TAILR) * (1u << 20) + (size_t)(SSTAT + cur % TAILR) * (2 * PIECE);
        const char* kp = kc + off; const char* vp = vc + voffs;
#pragma unroll
        for (int u = 0; u < 8; ++u) {
          if (u == 8 - INFL / 4 && sz == 1) ticket();
          put(u < 4 ? kp + u * 4096 : vp + (u - 4) * 4096, u, u < 4 ? 8 * q >= KB0 : 8 * q >= VB0);
          settle(u);
        }
        voffs = off;
      }
    }
    lds_st(lbase + L_LANDED + 4, nq);
    STAMP(1); DBGVAL(15, nq + 4);
    slot += 2; if (slot >= NSLOT) { slot -= NSLOT; ++gen; }
    frs = 0;
#pragma unroll
    for (int u = 0; u < 4; ++u) { put(vc + voffs + u * 4096, u, 8 * q >= VB0); settle(u); }
#pragma unroll
    for (int i = LAG - 1; i >= 0; --i) { VMCNT(4 * i); lds_st(lbase + L_LANDED, nq - i + (i < 4 ? 4 : 0)); }
    STAMP(2);
  } else {
    const int h = b >> 5;
    LAS float* xn = (LAS float*)(smem + L_XN);
    LAS float* red = (LAS float*)(smem + L_RED);
    u32x2 ev, tag;
    auto arrive = [&](unsigned cnt) -> int {
      unsigned arr;
      asm volatile("s_mov_b64 exec, 1\n\tds_add_rtn_u32 %0, %1, %2\n\ts_mov_b64 exec, -1\n\ts_waitcnt lgkmcnt(0)" : "=&v"(arr) : "v"(lbase + cnt), "v"(1u) : "memory");
      return __builtin_amdgcn_readfirstlane(arr);
    };
    auto make_tag = [&]() {
      const u64 e = (u64)(unsigned)__builtin_amdgcn_readfirstlane(ev[1]) << 32 | (unsigned)__builtin_amdgcn_readfirstlane(ev[0]);
      const u64 z = e * 6364136223846793005ull + 1442695040888963407ull;
      tag = u32x2{(unsigned)z, (unsigned)(z >> 32)};
    };
    if (make_q) {
      const int qi = role >> 1;
      if (wave == 0 && (qi & 4) == 0) asm volatile("global_store_dword %0, %1, %2 sc1" :: "v"(0u), "v"(0u), "s"(ws + WS_CTR + ((qi >> 3) * 4 + (qi & 3)) * 256) : "memory");
      u32x4 xv, wv[14];
      asm volatile("global_load_dwordx4 %0, %1, %2" : "=v"(xv) : "v"((lane < 56 ? lane : 55) * 16u), "s"(x));
      const unsigned o = (unsigned)(tid >> 3) * 8192u + (tid & 7) * 16u;
#pragma unroll
      for (int i = 0; i < 14; ++i) LD_NT(wv[i], o, wq + qi * 128 + i * (32 * 8192), 0);
      asm volatile("global_load_dwordx2 %0, %1, %2 sc1" : "=v"(ev) : "v"(0u), "s"(ws + WS_E) : "memory");
      STAMP(4);
      wg_barrier();
      WAITV(15, xv);
      STAMP(7);
      layer_norm(xv, xn, lane);
      STAMP(1);
#pragma unroll
      for (int i = 0; i < 14; ++i) WAITV(1, wv[i]);
      STAMP(2);
      slice_partials(wv, xn, red + wave * 64, tid);
      WAITV(0, ev);
      make_tag();
      if (arrive(L_QCNT) == NC - 1 && lane < 16) {
        const LAS f32x4* rq = (const LAS f32x4*)red + lane;
        const f32x4 s = (rq[0] + rq[16]) + (rq[32] + rq[48]);
        const u32x4 g = {pack_bf16(s[0], s[1]), pack_bf16(s[2], s[3]), tag[0], tag[1]};
        ST4_SC1((unsigned)(qi * 256 + lane * 16), g, ws + WS_Q);
      }
      STAMP(3);
    } else {
      asm volatile("global_load_dwordx2 %0, %1, %2 sc1" : "=v"(ev) : "v"(0u), "s"(ws + WS_E) : "memory");
      wg_barrier();
      WAITV(0, ev);
      make_tag();
    }
    bool bad = false;
    unsigned o_pd = (unsigned)(b * PD_STRIDE + tid * 16), o_y = (unsigned)(role * Y_STRIDE + (tid < 224 ? tid : 0) * 16);
    unsigned o_g0 = own_o ? (unsigned)((h * 32 + (tid >> 4)) * PD_STRIDE + (role & 15) * 256 + (tid & 15) * 16)
                          : (unsigned)((tid >> 2) * Y_STRIDE + (own_out ? role >> 1 : 0) * 64 + (tid & 3) * 16);
    unsigned o_g1 = o_g0 + (own_o ? 16 * PD_STRIDE : 64 * Y_STRIDE);
    asm volatile("" : "+v"(o_pd), "+v"(o_y), "+v"(o_g0), "+v"(o_g1));
    const char* gbase = ws + (own_o ? WS_PD : WS_Y);
    float m = -INFINITY, macc = -INFINITY, l = 0.f;
    f32x2 a0[4], a1[4];
#pragma unroll
    for (int k = 0; k < 4; ++k) { a0[k] = f32x2{0.f, 0.f}; a1[k] = f32x2{0.f, 0.f}; }
    unsigned landed = 0, endq = 0xffffffffu;
    unsigned pc = wave; int slot = wave, gen = 1;
    u32x4 q0, q1;
    LAS u16* scw = (LAS u16*)(smem + L_REC + wave * 2048) + (lane >> 1);
    auto refresh = [&]() {
      u32x2 c; asm volatile("ds_read_b64 %0, %1\n\ts_waitcnt lgkmcnt(0)" : "=v"(c) : "v"(lbase + L_LANDED) : "memory");
      landed = __builtin_amdgcn_readfirstlane(c[0]); endq = __builtin_amdgcn_readfirstlane(c[1]);
    };
    auto advance = [&](int n) { pc += n; slot += n; if (slot >= NSLOT) { slot -= NSLOT; ++gen; } };
    auto k_read = [&](u32x4 (&R)[8]) {
      const LAS u32x4* kt = (const LAS u32x4*)(smem + slot * PIECE) + lane;
#pragma unroll
      for (int i = 0; i < 8; ++i) R[i] = kt[i * 64];
      lds_st(lbase + L_FREE + slot * 4, gen);
    };
    auto k_scores = [&](const u32x4 (&R)[8], int t) {
      float sc[8];
#pragma unroll
      for (int i = 0; i < 8; ++i) {
        float a = 0.f, c = 0.f;
#pragma unroll
        for (int k = 0; k < 4; ++k) { a = dot2(R[i][k], q0[k], a); c = dot2(R[i][k], q1[k], c); }
        sc[i] = swap16_add(a, c);
      }
      const float s = row_transpose_sum8(sc, 0xccccccccccccccccull);
      const unsigned sb = pack_bf16(s, s);
      scw[t * 32] = (u16)sb;
      m = vmax(m, lo16(sb));
    };
    auto v_step = [&](int t) {
      LAS float* pw = (LAS float*)(smem + L_P) + wave * 32;
      const float p = __builtin_amdgcn_exp2f((lo16(scw[t * 32]) - m) * C2);
      pw[lane >> 1] = p;
      l += p;
      f32x4 pp[4];
#pragma unroll
      for (int k = 0; k < 4; ++k) pp[k] = *(const LAS f32x4*)(pw + (lane >> 5) * 16 + 4 * k);
      const LAS u32x4* vt = (const LAS u32x4*)(smem + slot * PIECE) + lane;
#pragma unroll
      for (int hf = 0; hf < 2; ++hf) {
        while (landed < 2 * pc + 1 + hf) refresh();
        u32x4 R[4];
#pragma unroll
        for (int i = 0; i < 4; ++i) R[i] = vt[(hf * 4 + i) * 64];
        if (hf == 1) lds_st(lbase + L_FREE + slot * 4, gen);
#pragma unroll
        for (int i = 0; i < 4; ++i)
#pragma unroll
          for (int k = 0; k < 4; ++k) { const f32x2 v = {lo16(R[i][k]), hi16(R[i][k])}; a0[k] += v * pp[hf][i]; a1[k] += v * pp[2 + hf][i]; }
        asm volatile("" : "+v"(a0[0]), "+v"(a0[1]), "+v"(a0[2]), "+v"(a0[3]), "+v"(a1[0]), "+v"(a1[1]), "+v"(a1[2]), "+v"(a1[3]));
      }
    };
    {
      u32x4 R0[8], R1[8], R2[8];
      while (landed < 2 * pc + 2) refresh();
      k_read(R0); advance(4);
      while (landed < 2 * pc + 2) refresh();
      k_read(R1); advance(4);
      while (landed < 2 * pc + 2) refresh();
      k_read(R2); advance(4);
      if (wave == 0) {
        u32x4 g0 = {0u, 0u, ~tag[0], 0u}, g1 = g0;
        const unsigned qo = (unsigned)(h * 2048 + lane * 32);
        unsigned spins = 0;
        for (;;) {
          const u64 miss = __ballot(!(g0[2] == tag[0] && g0[3] == tag[1] && g1[2] == tag[0] && g1[3] == tag[1]));
          if (miss == 0) break;
          if (++spins > SPIN_CAP) { bad = true; break; }
          repoll2(g0, g1, qo, qo + 16, ws + WS_Q, miss, miss);
        }
        *(LAS u32x4*)(smem + L_Q + lane * 16) = u32x4{g0[0], g0[1], g1[0], g1[1]};
        lds_st(lbase + L_QFLAG, 1u);
        DBGVAL(15, spins);
      } else {
        while (lds_ld(lbase + L_QFLAG) == 0) __builtin_amdgcn_s_sleep(1);
      }
      q0 = *(const LAS u32x4*)(smem + L_Q + (lane & 31) * 16); q1 = *(const LAS u32x4*)(smem + L_Q + 512 + (lane & 31) * 16);
      STAMP(5);
      k_scores(R0, 0); k_scores(R1, 1); k_scores(R2, 2);
      STAMP(6);
    }
    for (int t = 3; t < SSTAT / 2; ++t) {
      u32x4 R[8];
      while (landed < 2 * pc + 2) refresh();
      k_read(R); advance(4);
      k_scores(R, t);
    }
    m = head_max(m); macc = m;
    f32x2 y = {0.f, 0.f}, wof[32];
    unsigned xo = 0, bo = 0;
    const unsigned tc = (tid < 224 ? tid : 223) * 4u;
    if (has_mlp) {
      const int hg = role >> 2;
      u32x4 xv, wv[14]; unsigned b1w;
      asm volatile("global_load_dwordx4 %0, %1, %2" : "=v"(xv) : "v"((lane < 56 ? lane : 55) * 16u), "s"(x));
      asm volatile("global_load_ushort %0, %1, %2" : "=v"(b1w) : "v"(lane * 2u), "s"(b1 + hg * 64));
      const unsigned o = (unsigned)(tid >> 3) * (F * 2u) + (tid & 7) * 16u;
#pragma unroll
      for (int i = 0; i < 14; ++i) LD_NT(wv[i], o, w1 + hg * 128 + i * (32 * F * 2), 0);
      WAITV(14, xv); WAITV(14, b1w);
      layer_norm(xv, xn, lane);
#pragma unroll
      for (int i = 0; i < 14; ++i) WAITV(0, wv[i]);
      slice_partials(wv, xn, red + wave * 64, tid);
      arrive(L_HCNT);
      while (lds_ld(lbase + L_HCNT) < NC) __builtin_amdgcn_s_sleep(1);
      const float hv = rbf(rbf((red[lane] + red[64 + lane]) + (red[128 + lane] + red[192 + lane])) + lo16(b1w));
      const float u = 0.7978845608028654f * (hv + 0.044715f * hv * hv * hv);
      const float e = __builtin_amdgcn_exp2f(u * 2.885390081777927f);
      const float ge = rbf(hv * (1.f - __builtin_amdgcn_rcpf(e + 1.f)));
#pragma unroll
      for (int c = 0; c < 2; ++c) {
        unsigned w[32];
#pragma unroll
        for (int r = 0; r < 32; ++r) LD1_NT(w[r], tc, w2 + (size_t)(hg * 64 + c * 32 + (r & ~3)) * (D * 2), (r & 3) * (D * 2));
#pragma unroll
        for (int r = 0; r < 32; ++r) WAITV(0, w[r]);
#pragma unroll
        for (int r = 0; r < 32; ++r) y += f32x2{lo16(w[r]), hi16(w[r])} * lane_of(ge, c * 32 + r);
      }
    }
    if (own_o) {
      unsigned w[32];
#pragma unroll
      for (int r = 0; r < 32; ++r) LD1_NT(w[r], tc, wo + (size_t)(role * 32 + (r & ~3)) * (D * 2), (r & 3) * (D * 2));
#pragma unroll
      for (int r = 0; r < 32; ++r) WAITV(0, w[r]);
#pragma unroll
      for (int r = 0; r < 32; ++r) wof[r] = f32x2{lo16(w[r]), hi16(w[r])};
    } else {
#pragma unroll
      for (int r = 0; r < 32; ++r) wof[r] = f32x2{0.f, 0.f};
      asm volatile("global_load_dword %0, %2, %3\n\tglobal_load_dword %1, %2, %4\n\ts_waitcnt vmcnt(0)"
                   : "=&v"(xo), "=&v"(bo) : "v"((lane & 3) * 4u), "s"(x + (own_out ? role >> 1 : 0) * 8), "s"(b2 + (own_out ? role >> 1 : 0) * 8));
    }
#pragma unroll
    for (int r = 0; r < 32; ++r) asm volatile("" : "+v"(wof[r]));
    asm volatile("" : "+v"(y));
    const int nv = SSTAT / 2 - (wave >> 1);
    for (int t = 0; t < nv; ++t) { v_step(t); advance(4); }
    if (wave >= 2) { advance(2); v_step(SSTAT / 2 - 1); advance(2); } else { pc -= 2; slot -= 2; if (slot < 0) { slot += NSLOT; --gen; } }
    for (;;) {
      while (landed < 2 * pc + 2 && endq >= 2 * pc + 2) refresh();
      if (endq < 2 * pc + 2) break;
      u32x4 R[8];
      k_read(R); advance(6);
      k_scores(R, 0);
      m = head_max(m);
      const float alpha = __builtin_amdgcn_exp2f((macc - m) * C2);
      macc = m; l *= alpha;
      const float al0 = lane_of(alpha, 0), al1 = lane_of(alpha, 16);
#pragma unroll
      for (int k = 0; k < 4; ++k) { a0[k] *= al0; a1[k] *= al1; }
      v_step(0); advance(2);
    }
    STAMP(8);
    LAS float* rec = (LAS float*)(smem + L_REC);
    LAS f32x2* mlr = (LAS f32x2*)(smem + L_ML);
    {
      f32x4 o0, o1;
#pragma unroll
      for (int k = 0; k < 2; ++k) {
        o0[2 * k] = swap32_add(a0[k][0], a1[k][0]); o0[2 * k + 1] = swap32_add(a0[k][1], a1[k][1]);
        o1[2 * k] = swap32_add(a0[2 + k][0], a1[2 + k][0]); o1[2 * k + 1] = swap32_add(a0[2 + k][1], a1[2 + k][1]);
      }
      *(LAS f32x4*)(rec + wave * 512 + 8 * lane) = o0; *(LAS f32x4*)(rec + wave * 512 + 8 * lane + 4) = o1;
      l += dpp<ROR8>(l); l += dpp<HMIR>(l); l += dpp<XOR2>(l); l = swap32_add(l, l);
      if ((lane & 47) == 0) mlr[(lane >> 4) * NC + wave] = f32x2{m, l};
    }
    wg_barrier();
    const unsigned tag16 = tag[0] & 0xffffu;
    {
      const LAS f32x4* mp = (const LAS f32x4*)(mlr + (tid >> 7) * NC);
      const f32x4 m01 = mp[0], m23 = mp[1];
      const LAS f32x2* ap = (const LAS f32x2*)rec + tid;
      const f32x2 v0 = ap[0], v1 = ap[256], v2 = ap[512], v3 = ap[768];
      const float M = vmax(vmax(m01[0], m01[2]), vmax(m23[0], m23[2]));
      const float f0 = __builtin_amdgcn_exp2f((m01[0] - M) * C2), f1 = __builtin_amdgcn_exp2f((m01[2] - M) * C2);
      const float f2 = __builtin_amdgcn_exp2f((m23[0] - M) * C2), f3 = __builtin_amdgcn_exp2f((m23[2] - M) * C2);
      const f32x2 o = (v0 * f0 + v1 * f1) + (v2 * f2 + v3 * f3);
      const unsigned L = __float_as_uint((f0 * m01[1] + f1 * m01[3]) + (f2 * m23[1] + f3 * m23[3]));
      const unsigned pay = (lane & 2) ? L << 16 : ((lane & 1) ? L : __float_as_uint(M)) & 0xffff0000u;
      const u32x4 gd = {__float_as_uint(o[0]), __float_as_uint(o[1]), pay | tag16, tag[1]};
      ST4_SC1(o_pd, gd, ws + WS_PD);
    }
    STAMP(9);
    u32x4 g0, g1;
    if (own_o) {
      g0 = u32x4{0u, 0u, 0u, ~tag[1]}; g1 = g0;
      unsigned spins = 0;
      for (;;) {
        const u64 m0 = __ballot(!((g0[2] & 0xffffu) == tag16 && g0[3] == tag[1])), m1 = __ballot(!((g1[2] & 0xffffu) == tag16 && g1[3] == tag[1]));
        if ((m0 | m1) == 0) break;
        if (++spins > SPIN_CAP) { bad = true; break; }
        repoll2(g0, g1, o_g0, o_g1, gbase, m0, m1);
      }
      STAMP(10);
      const float ma = hi16(dppu<0x00>(g0[2])), mb = hi16(dppu<0x00>(g1[2]));
      const float Mw = rows_max(vmax(ma, mb));
      const float fa = __builtin_amdgcn_exp2f((ma - Mw) * C2), fb = __builtin_amdgcn_exp2f((mb - Mw) * C2);
      const float la = __uint_as_float((dppu<0x55>(g0[2]) & 0xffff0000u) | (dppu<0xAA>(g0[2]) >> 16));
      const float lb = __uint_as_float((dppu<0x55>(g1[2]) & 0xffff0000u) | (dppu<0xAA>(g1[2]) >> 16));
      const float oe = fa * __uint_as_float(g0[0]) + fb * __uint_as_float(g1[0]), oo = fa * __uint_as_float(g0[1]) + fb * __uint_as_float(g1[1]);
      const float Lp = fa * la + fb * lb;
      const float t = swap32_add(swap16_add(oe, oo), swap16_add(Lp, Lp));
      LAS float* redo = (LAS float*)(smem + L_REDO);
      LAS float* ml2 = (LAS float*)(smem + L_ML2);
      if (lane < 32) redo[(2 * (lane & 15) + (lane >> 4)) * NC + wave] = t;
      if (lane == 32) { ml2[wave] = Mw; ml2[NC + wave] = t; }
      wg_barrier();
      const f32x4 ov = ((const LAS f32x4*)redo)[lane & 31], Mv = ((const LAS f32x4*)ml2)[0], Lv = ((const LAS f32x4*)ml2)[1];
      const float M = vmax(vmax(Mv[0], Mv[1]), vmax(Mv[2], Mv[3]));
      f32x4 fv;
#pragma unroll
      for (int w = 0; w < NC; ++w) fv[w] = __builtin_amdgcn_exp2f((Mv[w] - M) * C2);
      const f32x4 on4 = ov * fv, L4 = Lv * fv;
      const float on = rbf(((on4[0] + on4[1]) + (on4[2] + on4[3])) * __builtin_amdgcn_rcpf((L4[0] + L4[1]) + (L4[2] + L4[3])));
      f32x2 y1 = {0.f, 0.f}, y2 = y1, y3 = y1;
#pragma unroll
      for (int r = 0; r < 32; r += 4) {
        y += wof[r] * lane_of(on, r); y1 += wof[r + 1] * lane_of(on, r + 1); y2 += wof[r + 2] * lane_of(on, r + 2); y3 += wof[r + 3] * lane_of(on, r + 3);
      }
      y = (y + y1) + (y2 + y3);
      if (bad) y[0] = __uint_as_float(0x7fc00000u);
      if (tid < 224) {
        const u32x4 gy = {__float_as_uint(y[0]), __float_as_uint(y[1]), tag[0], tag[1]};
        ST4_SC1(o_y, gy, ws + WS_Y);
      }
      STAMP(11);
    } else if (own_out) {
      g0 = u32x4{0u, 0u, ~tag[0], 0u}; g1 = g0;
      unsigned spins = 0;
      for (;;) {
        const u64 m0 = __ballot(!(g0[2] == tag[0] && g0[3] == tag[1])), m1 = __ballot(!(g1[2] == tag[0] && g1[3] == tag[1]));
        if ((m0 | m1) == 0) break;
        if (++spins > SPIN_CAP) { bad = true; break; }
        const u64 gate = (spins == 1 || (m0 & m1) != ~0ull) ? ~0ull : 1ull;
        repoll2(g0, g1, o_g0, o_g1, gbase, m0 & gate, m1 & gate);
      }
      STAMP(12);
      float s0 = __uint_as_float(g0[0]) + __uint_as_float(g1[0]), s1 = __uint_as_float(g0[1]) + __uint_as_float(g1[1]);
      if (bad) s0 = __uint_as_float(0x7fc00000u);
      s0 += dpp<ROR8>(s0); s1 += dpp<ROR8>(s1); s0 += dpp<ROR4>(s0); s1 += dpp<ROR4>(s1);
      float ss = swap16_add(s0, s1); ss = swap32_add(ss, ss);
      LAS float* redy = (LAS float*)(smem + L_REDO);
      if ((lane & 44) == 0) redy[wave * 8 + 2 * (lane & 3) + (lane >> 4)] = ss;
      wg_barrier();
      if (tid < 4) {
        const LAS f32x2* ry = (const LAS f32x2*)redy + tid;
        const f32x2 t = (ry[0] + ry[4]) + (ry[8] + ry[12]);
        out[(role >> 1) * 4 + tid] = pack_bf16(lo16(xo) + t[0] + lo16(bo), hi16(xo) + t[1] + hi16(bo));
        if (role == 1 && tid == 0) *(u32x2*)(ws + WS_E) = tag;
      }
      STAMP(13);
    }
  }
}

extern "C" void kernel_launch(void* const* d_in, const int* in_sizes, int n_in, void* d_out, int out_size, void* d_ws, size_t ws_size, hipStream_t stream) {
  fused_kernel<<<NWG, 320, 0, stream>>>((const u16*)d_in[0], (const char*)d_in[1], (const char*)d_in[2], (const char*)d_in[3], (const char*)d_in[4],
                                        (const char*)d_in[6], (const u16*)d_in[7], (const char*)d_in[8], (const u16*)d_in[9], (unsigned*)d_out, (char*)d_ws);
}
```

```cpp
#define DBG 1
#define PROBE_IDX 0
#define PROBE_LOADER 0
#define PROBE_STAT 2
#include <hip/hip_runtime.h>
#include <hip/hip_bf16.h>
#include <cstdint>
#include <type_traits>

typedef unsigned u32x4 __attribute__((ext_vector_type(4)));
typedef unsigned u32x2 __attribute__((ext_vector_type(2)));
typedef float f32x4 __attribute__((ext_vector_type(4)));
typedef float f32x2 __attribute__((ext_vector_type(2)));
typedef __bf16 bf16x2 __attribute__((ext_vector_type(2)));
typedef unsigned short u16;
typedef unsigned long long u64;
#define LAS __attribute__((address_space(3)))

#define INFL 28
#define SSTAT 56

constexpr int D = 448, F = 1792, DH = 256;
constexpr int NWG = 256, NC = 4;
constexpr int PIECE = 8192, NSLOT = 18;
constexpr int RPR = 64;
constexpr int TAILR = RPR - SSTAT, POOL = 8 * TAILR;
static_assert(POOL >= 24 && INFL % 4 == 0 && INFL >= 20 && INFL <= 32 && SSTAT % 2 == 0 && SSTAT >= 8 && (SSTAT / 2 + 1) * 64 <= 2048, "loader structure");
constexpr unsigned PD_STRIDE = 33 * 128, Y_STRIDE = 29 * 128;
constexpr size_t WS_E = 0, WS_CTR = 4096, WS_Q = 16384, WS_PD = 65536, WS_Y = WS_PD + (size_t)NWG * PD_STRIDE, WS_DBG = 4u << 20;
constexpr unsigned SPIN_CAP = 1u << 22;
constexpr unsigned L_CTL = NSLOT * PIECE;
constexpr unsigned L_LANDED = L_CTL, L_QFLAG = L_CTL + 8, L_QCNT = L_CTL + 12, L_HCNT = L_CTL + 16, L_FREE = L_CTL + 32;
constexpr unsigned L_Q = L_CTL + 128, L_RED = L_Q + 1024, L_REDO = L_RED + 1024, L_ML2 = L_REDO + 512, L_P = L_ML2 + 64;
constexpr unsigned L_ML = L_P + NC * 128, L_XN = L_ML + 64, L_REC = L_XN + 2048, L_END = L_REC + NC * 2 * DH * 4;
static_assert(L_END <= 160 * 1024 && D <= 2 * DH, "LDS");
constexpr float C2 = 0.0625f * 1.4426950408889634f;

__device__ __forceinline__ float lo16(unsigned w) { return __uint_as_float(w << 16); }
__device__ __forceinline__ float hi16(unsigned w) { return __uint_as_float(w & 0xffff0000u); }
__device__ __forceinline__ unsigned pack_bf16(float a, float b) {
  bf16x2 r = {(__bf16)a, (__bf16)b};
  return __builtin_bit_cast(unsigned, r);
}
__device__ __forceinline__ float rbf(float x) { return lo16(pack_bf16(x, x)); }
__device__ __forceinline__ float dot2(unsigned k, unsigned q, float acc) {
  return __builtin_amdgcn_fdot2_f32_bf16(__builtin_bit_cast(bf16x2, k), __builtin_bit_cast(bf16x2, q), acc, false);
}
template <int CTRL> __device__ __forceinline__ unsigned dppu(unsigned x) { return (unsigned)__builtin_amdgcn_mov_dpp((int)x, CTRL, 0xf, 0xf, true); }
template <int CTRL> __device__ __forceinline__ float dpp(float x) { return __uint_as_float(dppu<CTRL>(__float_as_uint(x))); }
constexpr int XOR1 = 0xB1, XOR2 = 0x4E, HMIR = 0x141, MIR = 0x140, ROR8 = 0x128, ROR4 = 0x124;
__device__ __forceinline__ float swap16_add(float a, float b) {
  auto r = __builtin_amdgcn_permlane16_swap(__float_as_uint(a), __float_as_uint(b), false, false);
  return __uint_as_float(r[0]) + __uint_as_float(r[1]);
}
__device__ __forceinline__ float swap32_add(float a, float b) {
  auto r = __builtin_amdgcn_permlane32_swap(__float_as_uint(a), __float_as_uint(b), false, false);
  return __uint_as_float(r[0]) + __uint_as_float(r[1]);
}
__device__ __forceinline__ float row_sum16(float x) { x += dpp<XOR1>(x); x += dpp<XOR2>(x); x += dpp<HMIR>(x); x += dpp<MIR>(x); return x; }
__device__ __forceinline__ float wave_sum(float x) { x = row_sum16(x); x = swap16_add(x, x); return swap32_add(x, x); }
__device__ __forceinline__ float lane_of(float x, int l) { return __builtin_bit_cast(float, __builtin_amdgcn_readlane(__builtin_bit_cast(int, x), l)); }
__device__ __forceinline__ float row_transpose_sum8(const float (&v)[8], u64 bit1) {
  float t0, t1, t2, t3, r;
  asm volatile("s_nop 1\n\t"
      "v_add_f32_dpp %0, %5, %5 row_ror:8 row_mask:0xf bank_mask:0xf\n\tv_add_f32_dpp %1, %6, %6 row_ror:8 row_mask:0xf bank_mask:0xf\n\t"
      "v_add_f32_dpp %2, %7, %7 row_ror:8 row_mask:0xf bank_mask:0xf\n\tv_add_f32_dpp %3, %8, %8 row_ror:8 row_mask:0xf bank_mask:0xf\n\t"
      "v_add_f32_dpp %0, %9, %9 row_ror:8 row_mask:0xf bank_mask:0xc\n\tv_add_f32_dpp %1, %10, %10 row_ror:8 row_mask:0xf bank_mask:0xc\n\t"
      "v_add_f32_dpp %2, %11, %11 row_ror:8 row_mask:0xf bank_mask:0xc\n\tv_add_f32_dpp %3, %12, %12 row_ror:8 row_mask:0xf bank_mask:0xc\n\t"
      "v_add_f32_dpp %0, %0, %0 row_half_mirror row_mask:0xf bank_mask:0x5\n\tv_add_f32_dpp %1, %1, %1 row_half_mirror row_mask:0xf bank_mask:0x5\n\t"
      "v_add_f32_dpp %0, %2, %2 row_half_mirror row_mask:0xf bank_mask:0xa\n\tv_add_f32_dpp %1, %3, %3 row_half_mirror row_mask:0xf bank_mask:0xa\n\t"
      "s_nop 1\n\t"
      "v_add_f32_dpp %2, %0, %0 quad_perm:[2,3,0,1] row_mask:0xf bank_mask:0xf\n\tv_add_f32_dpp %3, %1, %1 quad_perm:[2,3,0,1] row_mask:0xf bank_mask:0xf\n\t"
      "v_cndmask_b32 %2, %2, %3, %13\n\ts_nop 1\n\t"
      "v_add_f32_dpp %4, %2, %2 quad_perm:[1,0,3,2] row_mask:0xf bank_mask:0xf"
      : "=&v"(t0), "=&v"(t1), "=&v"(t2), "=&v"(t3), "=&v"(r)
      : "v"(v[0]), "v"(v[1]), "v"(v[2]), "v"(v[3]), "v"(v[4]), "v"(v[5]), "v"(v[6]), "v"(v[7]), "s"(bit1));
  return r;
}
__device__ __forceinline__ float head_max(float x) {
  float y;
  asm volatile("s_nop 1\n\tv_max_f32_dpp %0, %0, %0 row_ror:8 row_mask:0xf bank_mask:0xf\n\ts_nop 1\n\tv_max_f32_dpp %0, %0, %0 row_half_mirror row_mask:0xf bank_mask:0xf\n\t"
               "s_nop 1\n\tv_max_f32_dpp %0, %0, %0 quad_perm:[2,3,0,1] row_mask:0xf bank_mask:0xf\n\tv_mov_b32 %1, %0\n\ts_nop 1\n\tv_permlane32_swap_b32 %0, %1\n\tv_max_f32 %0, %0, %1"
               : "+v"(x), "=&v"(y));
  return x;
}
__device__ __forceinline__ float rows_max(float x) {
  float y;
  asm volatile("v_mov_b32 %1, %0\n\ts_nop 1\n\tv_permlane16_swap_b32 %0, %1\n\tv_max_f32 %0, %0, %1\n\tv_mov_b32 %1, %0\n\ts_nop 1\n\tv_permlane32_swap_b32 %0, %1\n\tv_max_f32 %0, %0, %1"
               : "+v"(x), "=&v"(y));
  return x;
}
__device__ __forceinline__ float vmax(float a, float b) { float r; asm("v_max_f32 %0, %1, %2" : "=v"(r) : "v"(a), "v"(b)); return r; }
__device__ __forceinline__ void layer_norm(const u32x4& xv, LAS float* xn, int lane) {
  float xf[8];
#pragma unroll
  for (int k = 0; k < 4; ++k) { xf[2 * k] = lo16(xv[k]); xf[2 * k + 1] = hi16(xv[k]); }
  const float live = lane < 56 ? 1.f : 0.f;
  const float mean = wave_sum((((xf[0] + xf[1]) + (xf[2] + xf[3])) + ((xf[4] + xf[5]) + (xf[6] + xf[7]))) * live) * (1.f / D);
  float ss = 0.f;
#pragma unroll
  for (int k = 0; k < 8; ++k) { const float d = xf[k] - mean; ss += d * d; }
  const float var = wave_sum(ss * live) * (1.f / D);
  const float rden = __builtin_amdgcn_rcpf(rbf(__builtin_amdgcn_sqrtf(rbf(rbf(var) + 1e-5f)))) * live, mb = rbf(mean);
#pragma unroll
  for (int k = 0; k < 8; k += 2) {
    const unsigned c = pack_bf16(xf[k] - mb, xf[k + 1] - mb), n = pack_bf16(lo16(c) * rden, hi16(c) * rden);
    xf[k] = lo16(n); xf[k + 1] = hi16(n);
  }
  *(LAS f32x4*)(xn + 8 * lane) = f32x4{xf[0], xf[1], xf[2], xf[3]}; *(LAS f32x4*)(xn + 8 * lane + 4) = f32x4{xf[4], xf[5], xf[6], xf[7]};
}
__device__ __forceinline__ void slice_partials(const u32x4 (&w)[14], const LAS float* xn, LAS float* red, int tid) {
  f32x2 v[4] = {{0.f, 0.f}, {0.f, 0.f}, {0.f, 0.f}, {0.f, 0.f}};
#pragma unroll
  for (int i = 0; i < 14; ++i) {
    const float xr = xn[(tid >> 3) + 32 * i];
#pragma unroll
    for (int k = 0; k < 4; ++k) v[k] += f32x2{lo16(w[i][k]), hi16(w[i][k])} * xr;
  }
  const float a0 = swap32_add(v[0][0], v[2][0]), a1 = swap32_add(v[0][1], v[2][1]), a2 = swap32_add(v[1][0], v[3][0]), a3 = swap32_add(v[1][1], v[3][1]);
  const float b0 = swap16_add(a0, a2), b1 = swap16_add(a1, a3);
  const float t1 = b0 + dpp<ROR8>(b0), t2 = b1 + dpp<ROR8>(b1);
  red[(tid & 7) * 8 + ((tid >> 3) & 7)] = (tid & 8) ? t2 : t1;
}

#define LD_NT(dst, voff, sbase, imm) asm volatile("global_load_dwordx4 %0, %1, %2 offset:%3 nt" : "=v"(dst) : "v"(voff), "s"(sbase), "i"(imm))
#define LD1_NT(dst, voff, sbase, imm) asm volatile("global_load_dword %0, %1, %2 offset:%3 nt" : "=v"(dst) : "v"(voff), "s"(sbase), "i"(imm))
#define LD4_SC1(dst, voff, sbase, imm) asm volatile("global_load_dwordx4 %0, %1, %2 offset:%3 sc1" : "=v"(dst) : "v"(voff), "s"(sbase), "i"(imm) : "memory")
#define ST4_SC1(voff, val, sbase) asm volatile("global_store_dwordx4 %0, %1, %2 sc1\n\ts_nop 1" :: "v"(voff), "v"(val), "s"(sbase) : "memory")
#define WAITV(n, reg) asm volatile("s_waitcnt vmcnt(%1)" : "+v"(reg) : "i"(n))
#define VMCNT(n) asm volatile("s_waitcnt vmcnt(%0)" :: "i"(n) : "memory")
__device__ __forceinline__ void repoll2(u32x4& g0, u32x4& g1, unsigned o0, unsigned o1, const char* base, u64 m0, u64 m1) {
  asm volatile("s_mov_b64 exec, %5\n\tglobal_load_dwordx4 %0, %2, %4 sc1\n\ts_mov_b64 exec, %6\n\tglobal_load_dwordx4 %1, %3, %4 sc1\n\ts_mov_b64 exec, -1\n\ts_waitcnt vmcnt(0)"
               : "+v"(g0), "+v"(g1) : "v"(o0), "v"(o1), "s"(base), "s"(m0), "s"(m1) : "memory");
}
__device__ __forceinline__ void wg_barrier() { asm volatile("s_waitcnt lgkmcnt(0)\n\ts_barrier" ::: "memory"); }
__device__ __forceinline__ void lds_st(unsigned addr, unsigned v) { asm volatile("ds_write_b32 %0, %1" :: "v"(addr), "v"(v) : "memory"); }
__device__ __forceinline__ unsigned lds_ld(unsigned addr) {
  unsigned v; asm volatile("ds_read_b32 %0, %1\n\ts_waitcnt lgkmcnt(0)" : "=v"(v) : "v"(addr) : "memory"); return __builtin_amdgcn_readfirstlane(v); }
template <int N> __device__ __forceinline__ void dma(const char* g, unsigned voff, unsigned lds) {
  if constexpr (N == 4)
    asm volatile("s_mov_b32 m0, %2\n\ts_nop 0\n\tglobal_load_lds_dwordx4 %0, %1 nt\n\tglobal_load_lds_dwordx4 %0, %1 offset:1024 nt\n\t"
                 "global_load_lds_dwordx4 %0, %1 offset:2048 nt\n\tglobal_load_lds_dwordx4 %0, %1 offset:3072 nt" :: "v"(voff), "s"(g), "s"(lds) : "memory");
  else
    asm volatile("s_mov_b32 m0, %2\n\ts_nop 0\n\tglobal_load_lds_dwordx4 %0, %1 nt\n\tglobal_load_lds_dwordx4 %0, %1 offset:1024 nt" :: "v"(voff), "s"(g), "s"(lds) : "memory");
}

#define STAMP(i) do { unsigned t_ = (unsigned)__builtin_amdgcn_s_memrealtime(); stamps = (lane == (i)) ? t_ : stamps; } while (0)
#define DBGVAL(i, v) do { stamps = (lane == (i)) ? (unsigned)(v) : stamps; } while (0)

__global__ __launch_bounds__(320) void fused_kernel(const u16* __restrict__ x, const char* __restrict__ kc, const char* __restrict__ vc,
                                                    const char* __restrict__ wq, const char* __restrict__ wo, const char* __restrict__ w1,
                                                    const u16* __restrict__ b1, const char* __restrict__ w2, const u16* __restrict__ b2,
                                                    unsigned* __restrict__ out, char* __restrict__ ws) {
  __shared__ __attribute__((aligned(1024))) char smem[L_END];
  const unsigned lbase = (unsigned)(uintptr_t)(LAS char*)smem;
  const int b = blockIdx.x, tid = threadIdx.x, lane = tid & 63, wave = __builtin_amdgcn_readfirstlane(tid >> 6);
  const int role = b >> 1;
  const bool own_o = ((b ^ (b >> 3)) & 1) == 0, has_mlp = own_o && (role & 3) == 0 && role < 112;
  const bool make_q = !own_o && (role & 1) == 0, own_out = !own_o && (role & 1) == 1 && role < 112;
  unsigned stamps = 0;
  STAMP(0);
  if (wave == NC) {
    __builtin_amdgcn_s_setprio(3);
    if (lane < 8 + NSLOT) lds_st(lbase + L_CTL + lane * 4, lane == 1 ? 0xffffffffu : 0u);
    const unsigned voff = lane * 16;
    const int q = b >> 3;
    unsigned* ctr = (unsigned*)(ws + WS_CTR) + q * 64;
    int nq = 0, slot = 0, gen = 0;
    constexpr int LAG = INFL / 4 - 1;
    int frs = 0;
    auto put = [&](const char* g, int u) {
      if ((u & 1) == 0 && frs < gen) { while ((int)lds_ld(lbase + L_FREE + slot * 4) < gen) __builtin_amdgcn_s_sleep(1); }
      dma<4>(g, voff, lbase + slot * PIECE + (u & 1) * 4096);
      ++nq;
      if (u & 1) { if (++slot == NSLOT) { slot = 0; ++gen; } }
    };
    auto settle = [&](int u) {
      if (u & 1) {
        unsigned fr;
        asm volatile("ds_read_b32 %0, %1\n\ts_waitcnt vmcnt(%2) lgkmcnt(0)" : "=&v"(fr) : "v"(lbase + L_FREE + slot * 4), "i"(INFL - 4) : "memory");
        frs = __builtin_amdgcn_readfirstlane(fr);
      } else VMCNT(INFL - 4);
      lds_st(lbase + L_LANDED, max(nq - LAG, 0));
    };
    unsigned tv = 0;
    auto ticket = [&]() { asm volatile("s_mov_b64 exec, 1\n\tglobal_atomic_add %0, %1, %2, %3 sc0\n\ts_mov_b64 exec, -1" : "=v"(tv) : "v"(0u), "v"(1u), "s"(ctr) : "memory"); };
    size_t voffs = (size_t)b * (1u << 20);
    {
      const char* g = kc + voffs;
      put(g, 0); put(g + 4096, 1);
      wg_barrier();
      for (int p = 1; p < 2 * SSTAT; ++p) { g += PIECE; put(g, 0); settle(0); put(g + 4096, 1); settle(1);
        if (p == 3) STAMP(4);
        if (p == 16) STAMP(7);
      }
      STAMP(5);
      g = vc + voffs;
      for (int p = 0; p < 2 * SSTAT - 6; ++p) { put(g, 0); settle(0); put(g + 4096, 1); settle(1); g += PIECE; }
      while (lds_ld(lbase + L_QFLAG) == 0) __builtin_amdgcn_s_sleep(1);
#pragma unroll
      for (int u = 0; u < 8; ++u) { if (u == 8 - INFL / 4) ticket(); put(g + u * 4096, u); settle(u); }
      STAMP(6);
      voffs += (size_t)(SSTAT - 1) * (2 * PIECE);
    }
    for (;;) {
      asm volatile("" : "+v"(tv));
      const int k = __builtin_amdgcn_readfirstlane(tv);
      constexpr int A = POOL > 24 ? POOL - 24 : 0, N4 = (A + 3) / 4;
      int cur, sz;
      if (k < N4) { cur = 4 * k; sz = min(4, A - cur); } else if (k < N4 + 8) { cur = A + 2 * (k - N4); sz = 2; } else { cur = A + 16 + (k - N4 - 8); sz = 1; }
      if (cur >= POOL) break;
      for (; sz > 0; --sz, ++cur) {
        const size_t off = (size_t)(8 * q + cur / TAILR) * (1u << 20) + (size_t)(SSTAT + cur % TAILR) * (2 * PIECE);
        const char* kp = kc + off; const char* vp = vc + voffs;
#pragma unroll
        for (int u = 0; u < 8; ++u) {
          if (u == 8 - INFL / 4 && sz == 1) ticket();
          put(u < 4 ? kp + u * 4096 : vp + (u - 4) * 4096, u);
          settle(u);
        }
        voffs = off;
      }
    }
    lds_st(lbase + L_LANDED + 4, nq);
    STAMP(1); DBGVAL(15, nq + 4);
    slot += 2; if (slot >= NSLOT) { slot -= NSLOT; ++gen; }
    frs = 0;
#pragma unroll
    for (int u = 0; u < 4; ++u) { put(vc + voffs + u * 4096, u); settle(u); }
#pragma unroll
    for (int i = LAG - 1; i >= 0; --i) { VMCNT(4 * i); lds_st(lbase + L_LANDED, nq - i + (i < 4 ? 4 : 0)); }
    STAMP(2);
  } else {
    const int h = b >> 5;
    LAS float* xn = (LAS float*)(smem + L_XN);
    LAS float* red = (LAS float*)(smem + L_RED);
    u32x2 ev, tag;
    auto arrive = [&](unsigned cnt) -> int {
      unsigned arr;
      asm volatile("s_mov_b64 exec, 1\n\tds_add_rtn_u32 %0, %1, %2\n\ts_mov_b64 exec, -1\n\ts_waitcnt lgkmcnt(0)" : "=&v"(arr) : "v"(lbase + cnt), "v"(1u) : "memory");
      return __builtin_amdgcn_readfirstlane(arr);
    };
    auto make_tag = [&]() {
      const u64 e = (u64)(unsigned)__builtin_amdgcn_readfirstlane(ev[1]) << 32 | (unsigned)__builtin_amdgcn_readfirstlane(ev[0]);
      const u64 z = e * 6364136223846793005ull + 1442695040888963407ull;
      tag = u32x2{(unsigned)z, (unsigned)(z >> 32)};
    };
    if (make_q) {
      const int qi = role >> 1;
      if (wave == 0 && (qi & 4) == 0) asm volatile("global_store_dword %0, %1, %2 sc1" :: "v"(0u), "v"(0u), "s"(ws + WS_CTR + ((qi >> 3) * 4 + (qi & 3)) * 256) : "memory");
      u32x4 xv, wv[14];
      asm volatile("global_load_dwordx4 %0, %1, %2" : "=v"(xv) : "v"((lane < 56 ? lane : 55) * 16u), "s"(x));
      const unsigned o = (unsigned)(tid >> 3) * 8192u + (tid & 7) * 16u;
#pragma unroll
      for (int i = 0; i < 14; ++i) LD_NT(wv[i], o, wq + qi * 128 + i * (32 * 8192), 0);
      asm volatile("global_load_dwordx2 %0, %1, %2 sc1" : "=v"(ev) : "v"(0u), "s"(ws + WS_E) : "memory");
      STAMP(4);
      wg_barrier();
      WAITV(15, xv);
      STAMP(7);
      layer_norm(xv, xn, lane);
      STAMP(1);
#pragma unroll
      for (int i = 0; i < 14; ++i) WAITV(1, wv[i]);
      STAMP(2);
      slice_partials(wv, xn, red + wave * 64, tid);
      WAITV(0, ev);
      make_tag();
      if (arrive(L_QCNT) == NC - 1 && lane < 16) {
        const LAS f32x4* rq = (const LAS f32x4*)red + lane;
        const f32x4 s = (rq[0] + rq[16]) + (rq[32] + rq[48]);
        const u32x4 g = {pack_bf16(s[0], s[1]), pack_bf16(s[2], s[3]), tag[0], tag[1]};
        ST4_SC1((unsigned)(qi * 256 + lane * 16), g, ws + WS_Q);
      }
      STAMP(3);
    } else {
      asm volatile("global_load_dwordx2 %0, %1, %2 sc1" : "=v"(ev) : "v"(0u), "s"(ws + WS_E) : "memory");
      wg_barrier();
      WAITV(0, ev);
      make_tag();
    }
    bool bad = false;
    unsigned o_pd = (unsigned)(b * PD_STRIDE + tid * 16), o_y = (unsigned)(role * Y_STRIDE + (tid < 224 ? tid : 0) * 16);
    unsigned o_g0 = own_o ? (unsigned)((h * 32 + (tid >> 4)) * PD_STRIDE + (role & 15) * 256 + (tid & 15) * 16)
                          : (unsigned)((tid >> 2) * Y_STRIDE + (own_out ? role >> 1 : 0) * 64 + (tid & 3) * 16);
    unsigned o_g1 = o_g0 + (own_o ? 16 * PD_STRIDE : 64 * Y_STRIDE);
    asm volatile("" : "+v"(o_pd), "+v"(o_y), "+v"(o_g0), "+v"(o_g1));
    const char* gbase = ws + (own_o ? WS_PD : WS_Y);
    float m = -INFINITY, macc = -INFINITY, l = 0.f;
    f32x2 a0[4], a1[4];
#pragma unroll
    for (int k = 0; k < 4; ++k) { a0[k] = f32x2{0.f, 0.f}; a1[k] = f32x2{0.f, 0.f}; }
    unsigned landed = 0, endq = 0xffffffffu;
    unsigned pc = wave; int slot = wave, gen = 1;
    u32x4 q0, q1;
    LAS u16* scw = (LAS u16*)(smem + L_REC + wave * 2048) + (lane >> 1);
    auto refresh = [&]() {
      u32x2 c; asm volatile("ds_read_b64 %0, %1\n\ts_waitcnt lgkmcnt(0)" : "=v"(c) : "v"(lbase + L_LANDED) : "memory");
      landed = __builtin_amdgcn_readfirstlane(c[0]); endq = __builtin_amdgcn_readfirstlane(c[1]);
    };
    auto advance = [&](int n) { pc += n; slot += n; if (slot >= NSLOT) { slot -= NSLOT; ++gen; } };
    auto k_read = [&](u32x4 (&R)[8]) {
      const LAS u32x4* kt = (const LAS u32x4*)(smem + slot * PIECE) + lane;
#pragma unroll
      for (int i = 0; i < 8; ++i) R[i] = kt[i * 64];
      lds_st(lbase + L_FREE + slot * 4, gen);
    };
    auto k_scores = [&](const u32x4 (&R)[8], int t) {
      float sc[8];
#pragma unroll
      for (int i = 0; i < 8; ++i) {
        float a = 0.f, c = 0.f;
#pragma unroll
        for (int k = 0; k < 4; ++k) { a = dot2(R[i][k], q0[k], a); c = dot2(R[i][k], q1[k], c); }
        sc[i] = swap16_add(a, c);
      }
      const float s = row_transpose_sum8(sc, 0xccccccccccccccccull);
      const unsigned sb = pack_bf16(s, s);
      scw[t * 32] = (u16)sb;
      m = vmax(m, lo16(sb));
    };
    auto v_step = [&](int t) {
      LAS float* pw = (LAS float*)(smem + L_P) + wave * 32;
      const float p = __builtin_amdgcn_exp2f((lo16(scw[t * 32]) - m) * C2);
      pw[lane >> 1] = p;
      l += p;
      f32x4 pp[4];
#pragma unroll
      for (int k = 0; k < 4; ++k) pp[k] = *(const LAS f32x4*)(pw + (lane >> 5) * 16 + 4 * k);
      const LAS u32x4* vt = (const LAS u32x4*)(smem + slot * PIECE) + lane;
#pragma unroll
      for (int hf = 0; hf < 2; ++hf) {
        while (landed < 2 * pc + 1 + hf) refresh();
        u32x4 R[4];
#pragma unroll
        for (int i = 0; i < 4; ++i) R[i] = vt[(hf * 4 + i) * 64];
        if (hf == 1) lds_st(lbase + L_FREE + slot * 4, gen);
#pragma unroll
        for (int i = 0; i < 4; ++i)
#pragma unroll
          for (int k = 0; k < 4; ++k) { const f32x2 v = {lo16(R[i][k]), hi16(R[i][k])}; a0[k] += v * pp[hf][i]; a1[k] += v * pp[2 + hf][i]; }
        asm volatile("" : "+v"(a0[0]), "+v"(a0[1]), "+v"(a0[2]), "+v"(a0[3]), "+v"(a1[0]), "+v"(a1[1]), "+v"(a1[2]), "+v"(a1[3]));
      }
    };
    {
      u32x4 R0[8], R1[8], R2[8];
      while (landed < 2 * pc + 2) refresh();
      k_read(R0); advance(4);
      while (landed < 2 * pc + 2) refresh();
      k_read(R1); advance(4);
      while (landed < 2 * pc + 2) refresh();
      k_read(R2); advance(4);
      if (wave == 0) {
        u32x4 g0 = {0u, 0u, ~tag[0], 0u}, g1 = g0;
        const unsigned qo = (unsigned)(h * 2048 + lane * 32);
        unsigned spins = 0;
        for (;;) {
          const u64 miss = __ballot(!(g0[2] == tag[0] && g0[3] == tag[1] && g1[2] == tag[0] && g1[3] == tag[1]));
          if (miss == 0) break;
          if (++spins > SPIN_CAP) { bad = true; break; }
          repoll2(g0, g1, qo, qo + 16, ws + WS_Q, miss, miss);
        }
        *(LAS u32x4*)(smem + L_Q + lane * 16) = u32x4{g0[0], g0[1], g1[0], g1[1]};
        lds_st(lbase + L_QFLAG, 1u);
        DBGVAL(15, spins);
      } else {
        while (lds_ld(lbase + L_QFLAG) == 0) __builtin_amdgcn_s_sleep(1);
      }
      q0 = *(const LAS u32x4*)(smem + L_Q + (lane & 31) * 16); q1 = *(const LAS u32x4*)(smem + L_Q + 512 + (lane & 31) * 16);
      STAMP(5);
      k_scores(R0, 0); k_scores(R1, 1); k_scores(R2, 2);
      STAMP(6);
    }
    for (int t = 3; t < SSTAT / 2; ++t) {
      u32x4 R[8];
      while (landed < 2 * pc + 2) refresh();
      k_read(R); advance(4);
      k_scores(R, t);
    }
    m = head_max(m); macc = m;
    f32x2 y = {0.f, 0.f}, wof[32];
    unsigned xo = 0, bo = 0;
    const unsigned tc = (tid < 224 ? tid : 223) * 4u;
    if (has_mlp) {
      const int hg = role >> 2;
      u32x4 xv, wv[14]; unsigned b1w;
      asm volatile("global_load_dwordx4 %0, %1, %2" : "=v"(xv) : "v"((lane < 56 ? lane : 55) * 16u), "s"(x));
      asm volatile("global_load_ushort %0, %1, %2" : "=v"(b1w) : "v"(lane * 2u), "s"(b1 + hg * 64));
      const unsigned o = (unsigned)(tid >> 3) * (F * 2u) + (tid & 7) * 16u;
#pragma unroll
      for (int i = 0; i < 14; ++i) LD_NT(wv[i], o, w1 + hg * 128 + i * (32 * F * 2), 0);
      WAITV(14, xv); WAITV(14, b1w);
      layer_norm(xv, xn, lane);
#pragma unroll
      for (int i = 0; i < 14; ++i) WAITV(0, wv[i]);
      slice_partials(wv, xn, red + wave * 64, tid);
      arrive(L_HCNT);
      while (lds_ld(lbase + L_HCNT) < NC) __builtin_amdgcn_s_sleep(1);
      const float hv = rbf(rbf((red[lane] + red[64 + lane]) + (red[128 + lane] + red[192 + lane])) + lo16(b1w));
      const float u = 0.7978845608028654f * (hv + 0.044715f * hv * hv * hv);
      const float e = __builtin_amdgcn_exp2f(u * 2.885390081777927f);
      const float ge = rbf(hv * (1.f - __builtin_amdgcn_rcpf(e + 1.f)));
#pragma unroll
      for (int c = 0; c < 2; ++c) {
        unsigned w[32];
#pragma unroll
        for (int r = 0; r < 32; ++r) LD1_NT(w[r], tc, w2 + (size_t)(hg * 64 + c * 32 + (r & ~3)) * (D * 2), (r & 3) * (D * 2));
#pragma unroll
        for (int r = 0; r < 32; ++r) WAITV(0, w[r]);
#pragma unroll
        for (int r = 0; r < 32; ++r) y += f32x2{lo16(w[r]), hi16(w[r])} * lane_of(ge, c * 32 + r);
      }
    }
    if (own_o) {
      unsigned w[32];
#pragma unroll
      for (int r = 0; r < 32; ++r) LD1_NT(w[r], tc, wo + (size_t)(role * 32 + (r & ~3)) * (D * 2), (r & 3) * (D * 2));
#pragma unroll
      for (int r = 0; r < 32; ++r) WAITV(0, w[r]);
#pragma unroll
      for (int r = 0; r < 32; ++r) wof[r] = f32x2{lo16(w[r]), hi16(w[r])};
    } else {
#pragma unroll
      for (int r = 0; r < 32; ++r) wof[r] = f32x2{0.f, 0.f};
      asm volatile("global_load_dword %0, %2, %3\n\tglobal_load_dword %1, %2, %4\n\ts_waitcnt vmcnt(0)"
                   : "=&v"(xo), "=&v"(bo) : "v"((lane & 3) * 4u), "s"(x + (own_out ? role >> 1 : 0) * 8), "s"(b2 + (own_out ? role >> 1 : 0) * 8));
    }
#pragma unroll
    for (int r = 0; r < 32; ++r) asm volatile("" : "+v"(wof[r]));
    asm volatile("" : "+v"(y));
    const int nv = SSTAT / 2 - (wave >> 1);
    for (int t = 0; t < nv; ++t) { v_step(t); advance(4); }
    if (wave >= 2) { advance(2); v_step(SSTAT / 2 - 1); advance(2); } else { pc -= 2; slot -= 2; if (slot < 0) { slot += NSLOT; --gen; } }
    for (;;) {
      while (landed < 2 * pc + 2 && endq >= 2 * pc + 2) refresh();
      if (endq < 2 * pc + 2) break;
      u32x4 R[8];
      k_read(R); advance(6);
      k_scores(R, 0);
      m = head_max(m);
      const float alpha = __builtin_amdgcn_exp2f((macc - m) * C2);
      macc = m; l *= alpha;
      const float al0 = lane_of(alpha, 0), al1 = lane_of(alpha, 16);
#pragma unroll
      for (int k = 0; k < 4; ++k) { a0[k] *= al0; a1[k] *= al1; }
      v_step(0); advance(2);
    }
    STAMP(8);
    LAS float* rec = (LAS float*)(smem + L_REC);
    LAS f32x2* mlr = (LAS f32x2*)(smem + L_ML);
    {
      f32x4 o0, o1;
#pragma unroll
      for (int k = 0; k < 2; ++k) {
        o0[2 * k] = swap32_add(a0[k][0], a1[k][0]); o0[2 * k + 1] = swap32_add(a0[k][1], a1[k][1]);
        o1[2 * k] = swap32_add(a0[2 + k][0], a1[2 + k][0]); o1[2 * k + 1] = swap32_add(a0[2 + k][1], a1[2 + k][1]);
      }
      *(LAS f32x4*)(rec + wave * 512 + 8 * lane) = o0; *(LAS f32x4*)(rec + wave * 512 + 8 * lane + 4) = o1;
      l += dpp<ROR8>(l); l += dpp<HMIR>(l); l += dpp<XOR2>(l); l = swap32_add(l, l);
      if ((lane & 47) == 0) mlr[(lane >> 4) * NC + wave] = f32x2{m, l};
    }
    wg_barrier();
    const unsigned tag16 = tag[0] & 0xffffu;
    {
      const LAS f32x4* mp = (const LAS f32x4*)(mlr + (tid >> 7) * NC);
      const f32x4 m01 = mp[0], m23 = mp[1];
      const LAS f32x2* ap = (const LAS f32x2*)rec + tid;
      const f32x2 v0 = ap[0], v1 = ap[256], v2 = ap[512], v3 = ap[768];
      const float M = vmax(vmax(m01[0], m01[2]), vmax(m23[0], m23[2]));
      const float f0 = __builtin_amdgcn_exp2f((m01[0] - M) * C2), f1 = __builtin_amdgcn_exp2f((m01[2] - M) * C2);
      const float f2 = __builtin_amdgcn_exp2f((m23[0] - M) * C2), f3 = __builtin_amdgcn_exp2f((m23[2] - M) * C2);
      const f32x2 o = (v0 * f0 + v1 * f1) + (v2 * f2 + v3 * f3);
      const unsigned L = __float_as_uint((f0 * m01[1] + f1 * m01[3]) + (f2 * m23[1] + f3 * m23[3]));
      const unsigned pay = (lane & 2) ? L << 16 : ((lane & 1) ? L : __float_as_uint(M)) & 0xffff0000u;
      const u32x4 gd = {__float_as_uint(o[0]), __float_as_uint(o[1]), pay | tag16, tag[1]};
      ST4_SC1(o_pd, gd, ws + WS_PD);
    }
    STAMP(9);
    u32x4 g0, g1;
    if (own_o) {
      g0 = u32x4{0u, 0u, 0u, ~tag[1]}; g1 = g0;
      unsigned spins = 0;
      for (;;) {
        const u64 m0 = __ballot(!((g0[2] & 0xffffu) == tag16 && g0[3] == tag[1])), m1 = __ballot(!((g1[2] & 0xffffu) == tag16 && g1[3] == tag[1]));
        if ((m0 | m1) == 0) break;
        if (++spins > SPIN_CAP) { bad = true; break; }
        repoll2(g0, g1, o_g0, o_g1, gbase, m0, m1);
      }
      STAMP(10);
      const float ma = hi16(dppu<0x00>(g0[2])), mb = hi16(dppu<0x00>(g1[2]));
      const float Mw = rows_max(vmax(ma, mb));
      const float fa = __builtin_amdgcn_exp2f((ma - Mw) * C2), fb = __builtin_amdgcn_exp2f((mb - Mw) * C2);
      const float la = __uint_as_float((dppu<0x55>(g0[2]) & 0xffff0000u) | (dppu<0xAA>(g0[2]) >> 16));
      const float lb = __uint_as_float((dppu<0x55>(g1[2]) & 0xffff0000u) | (dppu<0xAA>(g1[2]) >> 16));
      const float oe = fa * __uint_as_float(g0[0]) + fb * __uint_as_float(g1[0]), oo = fa * __uint_as_float(g0[1]) + fb * __uint_as_float(g1[1]);
      const float Lp = fa * la + fb * lb;
      const float t = swap32_add(swap16_add(oe, oo), swap16_add(Lp, Lp));
      LAS float* redo = (LAS float*)(smem + L_REDO);
      LAS float* ml2 = (LAS float*)(smem + L_ML2);
      if (lane < 32) redo[(2 * (lane & 15) + (lane >> 4)) * NC + wave] = t;
      if (lane == 32) { ml2[wave] = Mw; ml2[NC + wave] = t; }
      wg_barrier();
      const f32x4 ov = ((const LAS f32x4*)redo)[lane & 31], Mv = ((const LAS f32x4*)ml2)[0], Lv = ((const LAS f32x4*)ml2)[1];
      const float M = vmax(vmax(Mv[0], Mv[1]), vmax(Mv[2], Mv[3]));
      f32x4 fv;
#pragma unroll
      for (int w = 0; w < NC; ++w) fv[w] = __builtin_amdgcn_exp2f((Mv[w] - M) * C2);
      const f32x4 on4 = ov * fv, L4 = Lv * fv;
      const float on = rbf(((on4[0] + on4[1]) + (on4[2] + on4[3])) * __builtin_amdgcn_rcpf((L4[0] + L4[1]) + (L4[2] + L4[3])));
      f32x2 y1 = {0.f, 0.f}, y2 = y1, y3 = y1;
#pragma unroll
      for (int r = 0; r < 32; r += 4) {
        y += wof[r] * lane_of(on, r); y1 += wof[r + 1] * lane_of(on, r + 1); y2 += wof[r + 2] * lane_of(on, r + 2); y3 += wof[r + 3] * lane_of(on, r + 3);
      }
      y = (y + y1) + (y2 + y3);
      if (bad) y[0] = __uint_as_float(0x7fc00000u);
      if (tid < 224) {
        const u32x4 gy = {__float_as_uint(y[0]), __float_as_uint(y[1]), tag[0], tag[1]};
        ST4_SC1(o_y, gy, ws + WS_Y);
      }
      STAMP(11);
    } else if (own_out) {
      g0 = u32x4{0u, 0u, ~tag[0], 0u}; g1 = g0;
      unsigned spins = 0;
      for (;;) {
        const u64 m0 = __ballot(!(g0[2] == tag[0] && g0[3] == tag[1])), m1 = __ballot(!(g1[2] == tag[0] && g1[3] == tag[1]));
        if ((m0 | m1) == 0) break;
        if (++spins > SPIN_CAP) { bad = true; break; }
        const u64 gate = (spins == 1 || (m0 & m1) != ~0ull) ? ~0ull : 1ull;
        repoll2(g0, g1, o_g0, o_g1, gbase, m0 & gate, m1 & gate);
      }
      STAMP(12);
      float s0 = __uint_as_float(g0[0]) + __uint_as_float(g1[0]), s1 = __uint_as_float(g0[1]) + __uint_as_float(g1[1]);
      if (bad) s0 = __uint_as_float(0x7fc00000u);
      s0 += dpp<ROR8>(s0); s1 += dpp<ROR8>(s1); s0 += dpp<ROR4>(s0); s1 += dpp<ROR4>(s1);
      float ss = swap16_add(s0, s1); ss = swap32_add(ss, ss);
      LAS float* redy = (LAS float*)(smem + L_REDO);
      if ((lane & 44) == 0) redy[wave * 8 + 2 * (lane & 3) + (lane >> 4)] = ss;
      wg_barrier();
      if (tid < 4) {
        const LAS f32x2* ry = (const LAS f32x2*)redy + tid;
        const f32x2 t = (ry[0] + ry[4]) + (ry[8] + ry[12]);
        out[(role >> 1) * 4 + tid] = pack_bf16(lo16(xo) + t[0] + lo16(bo), hi16(xo) + t[1] + hi16(bo));
        if (role == 1 && tid == 0) *(u32x2*)(ws + WS_E) = tag;
      }
      STAMP(13);
    }
  }
  { unsigned xcc = (unsigned)__builtin_amdgcn_s_getreg(20 | (3 << 11)) & 0xfu; unsigned hw = (unsigned)__builtin_amdgcn_s_getreg(63492);
    stamps = (lane == 14) ? (xcc << 16 | ((hw >> 8) & 0xffu)) : stamps;
    if (lane < 16) ((unsigned*)(ws + WS_DBG))[(b * 5 + wave) * 16 + lane] = stamps; }
}

__global__ void probe_kernel(const unsigned* st) {
  __shared__ unsigned sm[4];
  unsigned t0 = (unsigned)__builtin_amdgcn_s_memrealtime();
  if (threadIdx.x == 0) {
    unsigned ref = st[0], mn = 0xffffffffu;
    for (int w = 0; w < 1280; ++w) mn = min(mn, st[w * 16] - ref + 100000u);
    unsigned mx = 0, lo = 0xffffffffu; unsigned long long sum = 0; int n = 0;
    for (int w = 0; w < 1280; ++w) { if (((w % 5) == 4) != (PROBE_LOADER != 0)) continue; unsigned v = st[w * 16 + PROBE_IDX]; if (!v) continue; v = v - ref + 100000u - mn; mx = max(mx, v); lo = min(lo, v); sum += v; ++n; }
    sm[0] = PROBE_STAT == 0 ? lo : PROBE_STAT == 1 ? (unsigned)(sum / n) : mx;
  }
  __syncthreads();
  const unsigned d = 30000u + min(sm[0], 20000u) * 10u;
  while ((unsigned)__builtin_amdgcn_s_memrealtime() - t0 < d) __builtin_amdgcn_s_sleep(4);
}

extern "C" void kernel_launch(void* const* d_in, const int* in_sizes, int n_in, void* d_out, int out_size, void* d_ws, size_t ws_size, hipStream_t stream) {
  fused_kernel<<<NWG, 320, 0, stream>>>((const u16*)d_in[0], (const char*)d_in[1], (const char*)d_in[2], (const char*)d_in[3], (const char*)d_in[4],
                                        (const char*)d_in[6], (const u16*)d_in[7], (const char*)d_in[8], (const u16*)d_in[9], (unsigned*)d_out, (char*)d_ws);
  probe_kernel<<<1, 64, 0, stream>>>((const unsigned*)((char*)d_ws + WS_DBG));
}
```

```cpp
#include <hip/hip_runtime.h>
#include <hip/hip_bf16.h>
#include <cstdint>
#include <type_traits>

typedef unsigned u32x4 __attribute__((ext_vector_type(4)));
typedef unsigned u32x2 __attribute__((ext_vector_type(2)));
typedef float f32x4 __attribute__((ext_vector_type(4)));
typedef float f32x2 __attribute__((ext_vector_type(2)));
typedef __bf16 bf16x2 __attribute__((ext_vector_type(2)));
typedef unsigned short u16;
typedef unsigned long long u64;
#define LAS __attribute__((address_space(3)))

#define DBG 0
#define INFL 28
#define SSTAT 56

constexpr int D = 448, F = 1792, DH = 256;
constexpr int NWG = 256, NC = 4;
constexpr int PIECE = 8192, NSLOT = 18;
constexpr int RPR = 64;
constexpr int TAILR = RPR - SSTAT, POOL = 8 * TAILR;
static_assert(POOL >= 24 && INFL % 4 == 0 && INFL >= 20 && INFL <= 32 && SSTAT % 2 == 0 && SSTAT >= 8 && (SSTAT / 2 + 1) * 64 <= 2048, "loader structure");
constexpr unsigned PD_STRIDE = 33 * 128, Y_STRIDE = 29 * 128;
constexpr size_t WS_E = 0, WS_CTR = 4096, WS_Q = 16384, WS_PD = 65536, WS_Y = WS_PD + (size_t)NWG * PD_STRIDE, WS_DBG = 4u << 20;
constexpr unsigned SPIN_CAP = 1u << 22;
constexpr unsigned L_CTL = NSLOT * PIECE;
constexpr unsigned L_LANDED = L_CTL, L_QFLAG = L_CTL + 8, L_QCNT = L_CTL + 12, L_HCNT = L_CTL + 16, L_FREE = L_CTL + 32;
constexpr unsigned L_Q = L_CTL + 128, L_RED = L_Q + 1024, L_REDO = L_RED + 1024, L_ML2 = L_REDO + 512, L_P = L_ML2 + 64;
constexpr unsigned L_ML = L_P + NC * 128, L_XN = L_ML + 64, L_REC = L_XN + 2048, L_END = L_REC + NC * 2 * DH * 4;
static_assert(L_END <= 160 * 1024 && D <= 2 * DH, "LDS");
constexpr float C2 = 0.0625f * 1.4426950408889634f;

__device__ __forceinline__ float lo16(unsigned w) { return __uint_as_float(w << 16); }
__device__ __forceinline__ float hi16(unsigned w) { return __uint_as_float(w & 0xffff0000u); }
__device__ __forceinline__ unsigned pack_bf16(float a, float b) {
  bf16x2 r = {(__bf16)a, (__bf16)b};
  return __builtin_bit_cast(unsigned, r);
}
__device__ __forceinline__ float rbf(float x) { return lo16(pack_bf16(x, x)); }
__device__ __forceinline__ float dot2(unsigned k, unsigned q, float acc) {
  return __builtin_amdgcn_fdot2_f32_bf16(__builtin_bit_cast(bf16x2, k), __builtin_bit_cast(bf16x2, q), acc, false);
}
template <int CTRL> __device__ __forceinline__ unsigned dppu(unsigned x) { return (unsigned)__builtin_amdgcn_mov_dpp((int)x, CTRL, 0xf, 0xf, true); }
template <int CTRL> __device__ __forceinline__ float dpp(float x) { return __uint_as_float(dppu<CTRL>(__float_as_uint(x))); }
constexpr int XOR1 = 0xB1, XOR2 = 0x4E, HMIR = 0x141, MIR = 0x140, ROR8 = 0x128, ROR4 = 0x124;
__device__ __forceinline__ float swap16_add(float a, float b) {
  auto r = __builtin_amdgcn_permlane16_swap(__float_as_uint(a), __float_as_uint(b), false, false);
  return __uint_as_float(r[0]) + __uint_as_float(r[1]);
}
__device__ __forceinline__ float swap32_add(float a, float b) {
  auto r = __builtin_amdgcn_permlane32_swap(__float_as_uint(a), __float_as_uint(b), false, false);
  return __uint_as_float(r[0]) + __uint_as_float(r[1]);
}
__device__ __forceinline__ float row_sum16(float x) { x += dpp<XOR1>(x); x += dpp<XOR2>(x); x += dpp<HMIR>(x); x += dpp<MIR>(x); return x; }
__device__ __forceinline__ float wave_sum(float x) { x = row_sum16(x); x = swap16_add(x, x); return swap32_add(x, x); }
__device__ __forceinline__ float lane_of(float x, int l) { return __builtin_bit_cast(float, __builtin_amdgcn_readlane(__builtin_bit_cast(int, x), l)); }
__device__ __forceinline__ float row_transpose_sum8(const float (&v)[8], u64 bit1) {
  float t0, t1, t2, t3, r;
  asm volatile("s_nop 1\n\t"
      "v_add_f32_dpp %0, %5, %5 row_ror:8 row_mask:0xf bank_mask:0xf\n\tv_add_f32_dpp %1, %6, %6 row_ror:8 row_mask:0xf bank_mask:0xf\n\t"
      "v_add_f32_dpp %2, %7, %7 row_ror:8 row_mask:0xf bank_mask:0xf\n\tv_add_f32_dpp %3, %8, %8 row_ror:8 row_mask:0xf bank_mask:0xf\n\t"
      "v_add_f32_dpp %0, %9, %9 row_ror:8 row_mask:0xf bank_mask:0xc\n\tv_add_f32_dpp %1, %10, %10 row_ror:8 row_mask:0xf bank_mask:0xc\n\t"
      "v_add_f32_dpp %2, %11, %11 row_ror:8 row_mask:0xf bank_mask:0xc\n\tv_add_f32_dpp %3, %12, %12 row_ror:8 row_mask:0xf bank_mask:0xc\n\t"
      "v_add_f32_dpp %0, %0, %0 row_half_mirror row_mask:0xf bank_mask:0x5\n\tv_add_f32_dpp %1, %1, %1 row_half_mirror row_mask:0xf bank_mask:0x5\n\t"
      "v_add_f32_dpp %0, %2, %2 row_half_mirror row_mask:0xf bank_mask:0xa\n\tv_add_f32_dpp %1, %3, %3 row_half_mirror row_mask:0xf bank_mask:0xa\n\t"
      "s_nop 1\n\t"
      "v_add_f32_dpp %2, %0, %0 quad_perm:[2,3,0,1] row_mask:0xf bank_mask:0xf\n\tv_add_f32_dpp %3, %1, %1 quad_perm:[2,3,0,1] row_mask:0xf bank_mask:0xf\n\t"
      "v_cndmask_b32 %2, %2, %3, %13\n\ts_nop 1\n\t"
      "v_add_f32_dpp %4, %2, %2 quad_perm:[1,0,3,2] row_mask:0xf bank_mask:0xf"
      : "=&v"(t0), "=&v"(t1), "=&v"(t2), "=&v"(t3), "=&v"(r)
      : "v"(v[0]), "v"(v[1]), "v"(v[2]), "v"(v[3]), "v"(v[4]), "v"(v[5]), "v"(v[6]), "v"(v[7]), "s"(bit1));
  return r;
}
__device__ __forceinline__ float head_max(float x) {
  float y;
  asm volatile("s_nop 1\n\tv_max_f32_dpp %0, %0, %0 row_ror:8 row_mask:0xf bank_mask:0xf\n\ts_nop 1\n\tv_max_f32_dpp %0, %0, %0 row_half_mirror row_mask:0xf bank_mask:0xf\n\t"
               "s_nop 1\n\tv_max_f32_dpp %0, %0, %0 quad_perm:[2,3,0,1] row_mask:0xf bank_mask:0xf\n\tv_mov_b32 %1, %0\n\ts_nop 1\n\tv_permlane32_swap_b32 %0, %1\n\tv_max_f32 %0, %0, %1"
               : "+v"(x), "=&v"(y));
  return x;
}
__device__ __forceinline__ float rows_max(float x) {
  float y;
  asm volatile("v_mov_b32 %1, %0\n\ts_nop 1\n\tv_permlane16_swap_b32 %0, %1\n\tv_max_f32 %0, %0, %1\n\tv_mov_b32 %1, %0\n\ts_nop 1\n\tv_permlane32_swap_b32 %0, %1\n\tv_max_f32 %0, %0, %1"
               : "+v"(x), "=&v"(y));
  return x;
}
__device__ __forceinline__ float vmax(float a, float b) { float r; asm("v_max_f32 %0, %1, %2" : "=v"(r) : "v"(a), "v"(b)); return r; }
__device__ __forceinline__ void layer_norm(const u32x4& xv, LAS float* xn, int lane) {
  float xf[8];
#pragma unroll
  for (int k = 0; k < 4; ++k) { xf[2 * k] = lo16(xv[k]); xf[2 * k + 1] = hi16(xv[k]); }
  const float live = lane < 56 ? 1.f : 0.f;
  const float mean = wave_sum((((xf[0] + xf[1]) + (xf[2] + xf[3])) + ((xf[4] + xf[5]) + (xf[6] + xf[7]))) * live) * (1.f / D);
  float ss = 0.f;
#pragma unroll
  for (int k = 0; k < 8; ++k) { const float d = xf[k] - mean; ss += d * d; }
  const float var = wave_sum(ss * live) * (1.f / D);
  const float rden = __builtin_amdgcn_rcpf(rbf(__builtin_amdgcn_sqrtf(rbf(rbf(var) + 1e-5f)))) * live, mb = rbf(mean);
#pragma unroll
  for (int k = 0; k < 8; k += 2) {
    const unsigned c = pack_bf16(xf[k] - mb, xf[k + 1] - mb), n = pack_bf16(lo16(c) * rden, hi16(c) * rden);
    xf[k] = lo16(n); xf[k + 1] = hi16(n);
  }
  *(LAS f32x4*)(xn + 8 * lane) = f32x4{xf[0], xf[1], xf[2], xf[3]}; *(LAS f32x4*)(xn + 8 * lane + 4) = f32x4{xf[4], xf[5], xf[6], xf[7]};
}
__device__ __forceinline__ void slice_partials(const u32x4 (&w)[14], const LAS float* xn, LAS float* red, int tid) {
  f32x2 v[4] = {{0.f, 0.f}, {0.f, 0.f}, {0.f, 0.f}, {0.f, 0.f}};
#pragma unroll
  for (int i = 0; i < 14; ++i) {
    const float xr = xn[(tid >> 3) + 32 * i];
#pragma unroll
    for (int k = 0; k < 4; ++k) v[k] += f32x2{lo16(w[i][k]), hi16(w[i][k])} * xr;
  }
  const float a0 = swap32_add(v[0][0], v[2][0]), a1 = swap32_add(v[0][1], v[2][1]), a2 = swap32_add(v[1][0], v[3][0]), a3 = swap32_add(v[1][1], v[3][1]);
  const float b0 = swap16_add(a0, a2), b1 = swap16_add(a1, a3);
  const float t1 = b0 + dpp<ROR8>(b0), t2 = b1 + dpp<ROR8>(b1);
  red[(tid & 7) * 8 + ((tid >> 3) & 7)] = (tid & 8) ? t2 : t1;
}

#define LD_NT(dst, voff, sbase, imm) asm volatile("global_load_dwordx4 %0, %1, %2 offset:%3 nt" : "=v"(dst) : "v"(voff), "s"(sbase), "i"(imm))
#define LD1_NT(dst, voff, sbase, imm) asm volatile("global_load_dword %0, %1, %2 offset:%3 nt" : "=v"(dst) : "v"(voff), "s"(sbase), "i"(imm))
#define LD4_SC1(dst, voff, sbase, imm) asm volatile("global_load_dwordx4 %0, %1, %2 offset:%3 sc1" : "=v"(dst) : "v"(voff), "s"(sbase), "i"(imm) : "memory")
#define ST4_SC1(voff, val, sbase) asm volatile("global_store_dwordx4 %0, %1, %2 sc1\n\ts_nop 1" :: "v"(voff), "v"(val), "s"(sbase) : "memory")
#define WAITV(n, reg) asm volatile("s_waitcnt vmcnt(%1)" : "+v"(reg) : "i"(n))
#define VMCNT(n) asm volatile("s_waitcnt vmcnt(%0)" :: "i"(n) : "memory")
__device__ __forceinline__ void repoll2(u32x4& g0, u32x4& g1, unsigned o0, unsigned o1, const char* base, u64 m0, u64 m1) {
  asm volatile("s_mov_b64 exec, %5\n\tglobal_load_dwordx4 %0, %2, %4 sc1\n\ts_mov_b64 exec, %6\n\tglobal_load_dwordx4 %1, %3, %4 sc1\n\ts_mov_b64 exec, -1\n\ts_waitcnt vmcnt(0)"
               : "+v"(g0), "+v"(g1) : "v"(o0), "v"(o1), "s"(base), "s"(m0), "s"(m1) : "memory");
}
__device__ __forceinline__ void wg_barrier() { asm volatile("s_waitcnt lgkmcnt(0)\n\ts_barrier" ::: "memory"); }
__device__ __forceinline__ void lds_st(unsigned addr, unsigned v) { asm volatile("ds_write_b32 %0, %1" :: "v"(addr), "v"(v) : "memory"); }
__device__ __forceinline__ unsigned lds_ld(unsigned addr) {
  unsigned v; asm volatile("ds_read_b32 %0, %1\n\ts_waitcnt lgkmcnt(0)" : "=v"(v) : "v"(addr) : "memory"); return __builtin_amdgcn_readfirstlane(v); }
template <int N> __device__ __forceinline__ void dma(const char* g, unsigned voff, unsigned lds) {
  if constexpr (N == 4)
    asm volatile("s_mov_b32 m0, %2\n\ts_nop 0\n\tglobal_load_lds_dwordx4 %0, %1 nt\n\tglobal_load_lds_dwordx4 %0, %1 offset:1024 nt\n\t"
                 "global_load_lds_dwordx4 %0, %1 offset:2048 nt\n\tglobal_load_lds_dwordx4 %0, %1 offset:3072 nt" :: "v"(voff), "s"(g), "s"(lds) : "memory");
  else
    asm volatile("s_mov_b32 m0, %2\n\ts_nop 0\n\tglobal_load_lds_dwordx4 %0, %1 nt\n\tglobal_load_lds_dwordx4 %0, %1 offset:1024 nt" :: "v"(voff), "s"(g), "s"(lds) : "memory");
}

#define STAMP(i) do {} while (0)
#define DBGVAL(i, v) do {} while (0)

__global__ __launch_bounds__(320) void fused_kernel(const u16* __restrict__ x, const char* __restrict__ kc, const char* __restrict__ vc,
                                                    const char* __restrict__ wq, const char* __restrict__ wo, const char* __restrict__ w1,
                                                    const u16* __restrict__ b1, const char* __restrict__ w2, const u16* __restrict__ b2,
                                                    unsigned* __restrict__ out, char* __restrict__ ws) {
  __shared__ __attribute__((aligned(1024))) char smem[L_END];
  const unsigned lbase = (unsigned)(uintptr_t)(LAS char*)smem;
  const int b = blockIdx.x, tid = threadIdx.x, lane = tid & 63, wave = __builtin_amdgcn_readfirstlane(tid >> 6);
  const int role = b >> 1;
  const bool own_o = ((b ^ (b >> 3)) & 1) == 0, has_mlp = own_o && (role & 3) == 0 && role < 112;
  const bool make_q = !own_o && (role & 1) == 0, own_out = !own_o && (role & 1) == 1 && role < 112;
  STAMP(0);
  if (wave == NC) {
    __builtin_amdgcn_s_setprio(3);
    if (lane < 8 + NSLOT) lds_st(lbase + L_CTL + lane * 4, lane == 1 ? 0xffffffffu : 0u);
    const unsigned voff = lane * 16;
    const int q = b >> 3;
    unsigned* ctr = (unsigned*)(ws + WS_CTR) + q * 64;
    int nq = 0, slot = 0, gen = 0;
    constexpr int LAG = INFL / 4 - 1;
    int frs = 0;
    auto put = [&](const char* g, int u) {
      if ((u & 1) == 0 && frs < gen) { while ((int)lds_ld(lbase + L_FREE + slot * 4) < gen) __builtin_amdgcn_s_sleep(1); }
      dma<4>(g, voff, lbase + slot * PIECE + (u & 1) * 4096);
      ++nq;
      if (u & 1) { if (++slot == NSLOT) { slot = 0; ++gen; } }
    };
    auto settle = [&](int u) {
      if (u & 1) {
        unsigned fr;
        asm volatile("ds_read_b32 %0, %1\n\ts_waitcnt vmcnt(%2) lgkmcnt(0)" : "=&v"(fr) : "v"(lbase + L_FREE + slot * 4), "i"(INFL - 4) : "memory");
        frs = __builtin_amdgcn_readfirstlane(fr);
      } else VMCNT(INFL - 4);
      lds_st(lbase + L_LANDED, max(nq - LAG, 0));
    };
    unsigned tv = 0;
    auto ticket = [&]() { asm volatile("s_mov_b64 exec, 1\n\tglobal_atomic_add %0, %1, %2, %3 sc0\n\ts_mov_b64 exec, -1" : "=v"(tv) : "v"(0u), "v"(1u), "s"(ctr) : "memory"); };
    size_t voffs = (size_t)b * (1u << 20);
    {
      const char* g = kc + voffs;
      put(g, 0); put(g + 4096, 1);
      wg_barrier();
      for (int p = 1; p < 2 * SSTAT; ++p) { g += PIECE; put(g, 0); settle(0); put(g + 4096, 1); settle(1);
      }
      STAMP(5);
      g = vc + voffs;
      for (int p = 0; p < 2 * SSTAT - 6; ++p) { put(g, 0); settle(0); put(g + 4096, 1); settle(1); g += PIECE; }
      while (lds_ld(lbase + L_QFLAG) == 0) __builtin_amdgcn_s_sleep(1);
#pragma unroll
      for (int u = 0; u < 8; ++u) { if (u == 8 - INFL / 4) ticket(); put(g + u * 4096, u); settle(u); }
      STAMP(6);
      voffs += (size_t)(SSTAT - 1) * (2 * PIECE);
    }
    for (;;) {
      asm volatile("" : "+v"(tv));
      const int k = __builtin_amdgcn_readfirstlane(tv);
      constexpr int A = POOL > 24 ? POOL - 24 : 0, N4 = (A + 3) / 4;
      int cur, sz;
      if (k < N4) { cur = 4 * k; sz = min(4, A - cur); } else if (k < N4 + 8) { cur = A + 2 * (k - N4); sz = 2; } else { cur = A + 16 + (k - N4 - 8); sz = 1; }
      if (cur >= POOL) break;
      for (; sz > 0; --sz, ++cur) {
        const size_t off = (size_t)(8 * q + cur / TAILR) * (1u << 20) + (size_t)(SSTAT + cur % TAILR) * (2 * PIECE);
        const char* kp = kc + off; const char* vp = vc + voffs;
#pragma unroll
        for (int u = 0; u < 8; ++u) {
          if (u == 8 - INFL / 4 && sz == 1) ticket();
          put(u < 4 ? kp + u * 4096 : vp + (u - 4) * 4096, u);
          settle(u);
        }
        voffs = off;
      }
    }
    lds_st(lbase + L_LANDED + 4, nq);
    STAMP(1); DBGVAL(15, nq + 4);
    slot += 2; if (slot >= NSLOT) { slot -= NSLOT; ++gen; }
    frs = 0;
#pragma unroll
    for (int u = 0; u < 4; ++u) { put(vc + voffs + u * 4096, u); settle(u); }
#pragma unroll
    for (int i = LAG - 1; i >= 0; --i) { VMCNT(4 * i); lds_st(lbase + L_LANDED, nq - i + (i < 4 ? 4 : 0)); }
    STAMP(2);
  } else {
    const int h = b >> 5;
    LAS float* xn = (LAS float*)(smem + L_XN);
    LAS float* red = (LAS float*)(smem + L_RED);
    u32x2 ev, tag;
    auto arrive = [&](unsigned cnt) -> int {
      unsigned arr;
      asm volatile("s_mov_b64 exec, 1\n\tds_add_rtn_u32 %0, %1, %2\n\ts_mov_b64 exec, -1\n\ts_waitcnt lgkmcnt(0)" : "=&v"(arr) : "v"(lbase + cnt), "v"(1u) : "memory");
      return __builtin_amdgcn_readfirstlane(arr);
    };
    auto make_tag = [&]() {
      const u64 e = (u64)(unsigned)__builtin_amdgcn_readfirstlane(ev[1]) << 32 | (unsigned)__builtin_amdgcn_readfirstlane(ev[0]);
      const u64 z = e * 6364136223846793005ull + 1442695040888963407ull;
      tag = u32x2{(unsigned)z, (unsigned)(z >> 32)};
    };
    if (make_q) {
      const int qi = role >> 1;
      if (wave == 0 && (qi & 4) == 0) asm volatile("global_store_dword %0, %1, %2 sc1" :: "v"(0u), "v"(0u), "s"(ws + WS_CTR + ((qi >> 3) * 4 + (qi & 3)) * 256) : "memory");
      u32x4 xv, wv[14];
      asm volatile("global_load_dwordx4 %0, %1, %2" : "=v"(xv) : "v"((lane < 56 ? lane : 55) * 16u), "s"(x));
      const unsigned o = (unsigned)(tid >> 3) * 8192u + (tid & 7) * 16u;
#pragma unroll
      for (int i = 0; i < 14; ++i) LD_NT(wv[i], o, wq + qi * 128 + i * (32 * 8192), 0);
      asm volatile("global_load_dwordx2 %0, %1, %2 sc1" : "=v"(ev) : "v"(0u), "s"(ws + WS_E) : "memory");
      STAMP(4);
      wg_barrier();
      WAITV(15, xv);
      STAMP(7);
      layer_norm(xv, xn, lane);
      STAMP(1);
#pragma unroll
      for (int i = 0; i < 14; ++i) WAITV(1, wv[i]);
      STAMP(2);
      slice_partials(wv, xn, red + wave * 64, tid);
      WAITV(0, ev);
      make_tag();
      if (arrive(L_QCNT) == NC - 1 && lane < 16) {
        const LAS f32x4* rq = (const LAS f32x4*)red + lane;
        const f32x4 s = (rq[0] + rq[16]) + (rq[32] + rq[48]);
        const u32x4 g = {pack_bf16(s[0], s[1]), pack_bf16(s[2], s[3]), tag[0], tag[1]};
        ST4_SC1((unsigned)(qi * 256 + lane * 16), g, ws + WS_Q);
      }
      STAMP(3);
    } else {
      asm volatile("global_load_dwordx2 %0, %1, %2 sc1" : "=v"(ev) : "v"(0u), "s"(ws + WS_E) : "memory");
      wg_barrier();
      WAITV(0, ev);
      make_tag();
    }
    bool bad = false;
    unsigned o_pd = (unsigned)(b * PD_STRIDE + tid * 16), o_y = (unsigned)(role * Y_STRIDE + (tid < 224 ? tid : 0) * 16);
    unsigned o_g0 = own_o ? (unsigned)((h * 32 + (tid >> 4)) * PD_STRIDE + (role & 15) * 256 + (tid & 15) * 16)
                          : (unsigned)((tid >> 2) * Y_STRIDE + (own_out ? role >> 1 : 0) * 64 + (tid & 3) * 16);
    unsigned o_g1 = o_g0 + (own_o ? 16 * PD_STRIDE : 64 * Y_STRIDE);
    asm volatile("" : "+v"(o_pd), "+v"(o_y), "+v"(o_g0), "+v"(o_g1));
    const char* gbase = ws + (own_o ? WS_PD : WS_Y);
    float m = -INFINITY, macc = -INFINITY, l = 0.f;
    f32x2 a0[4], a1[4];
#pragma unroll
    for (int k = 0; k < 4; ++k) { a0[k] = f32x2{0.f, 0.f}; a1[k] = f32x2{0.f, 0.f}; }
    unsigned landed = 0, endq = 0xffffffffu;
    unsigned pc = wave; int slot = wave, gen = 1;
    u32x4 q0, q1;
    LAS u16* scw = (LAS u16*)(smem + L_REC + wave * 2048) + (lane >> 1);
    auto refresh = [&]() {
      u32x2 c; asm volatile("ds_read_b64 %0, %1\n\ts_waitcnt lgkmcnt(0)" : "=v"(c) : "v"(lbase + L_LANDED) : "memory");
      landed = __builtin_amdgcn_readfirstlane(c[0]); endq = __builtin_amdgcn_readfirstlane(c[1]);
    };
    auto advance = [&](int n) { pc += n; slot += n; if (slot >= NSLOT) { slot -= NSLOT; ++gen; } };
    auto k_read = [&](u32x4 (&R)[8]) {
      const LAS u32x4* kt = (const LAS u32x4*)(smem + slot * PIECE) + lane;
#pragma unroll
      for (int i = 0; i < 8; ++i) R[i] = kt[i * 64];
      lds_st(lbase + L_FREE + slot * 4, gen);
    };
    auto k_scores = [&](const u32x4 (&R)[8], int t) {
      float sc[8];
#pragma unroll
      for (int i = 0; i < 8; ++i) {
        float a = 0.f, c = 0.f;
#pragma unroll
        for (int k = 0; k < 4; ++k) { a = dot2(R[i][k], q0[k], a); c = dot2(R[i][k], q1[k], c); }
        sc[i] = swap16_add(a, c);
      }
      const float s = row_transpose_sum8(sc, 0xccccccccccccccccull);
      const unsigned sb = pack_bf16(s, s);
      scw[t * 32] = (u16)sb;
      m = vmax(m, lo16(sb));
    };
    auto v_step = [&](int t) {
      LAS float* pw = (LAS float*)(smem + L_P) + wave * 32;
      const float p = __builtin_amdgcn_exp2f((lo16(scw[t * 32]) - m) * C2);
      pw[lane >> 1] = p;
      l += p;
      f32x4 pp[4];
#pragma unroll
      for (int k = 0; k < 4; ++k) pp[k] = *(const LAS f32x4*)(pw + (lane >> 5) * 16 + 4 * k);
      const LAS u32x4* vt = (const LAS u32x4*)(smem + slot * PIECE) + lane;
#pragma unroll
      for (int hf = 0; hf < 2; ++hf) {
        while (landed < 2 * pc + 1 + hf) refresh();
        u32x4 R[4];
#pragma unroll
        for (int i = 0; i < 4; ++i) R[i] = vt[(hf * 4 + i) * 64];
        if (hf == 1) lds_st(lbase + L_FREE + slot * 4, gen);
#pragma unroll
        for (int i = 0; i < 4; ++i)
#pragma unroll
          for (int k = 0; k < 4; ++k) { const f32x2 v = {lo16(R[i][k]), hi16(R[i][k])}; a0[k] += v * pp[hf][i]; a1[k] += v * pp[2 + hf][i]; }
        asm volatile("" : "+v"(a0[0]), "+v"(a0[1]), "+v"(a0[2]), "+v"(a0[3]), "+v"(a1[0]), "+v"(a1[1]), "+v"(a1[2]), "+v"(a1[3]));
      }
    };
    {
      u32x4 R0[8], R1[8], R2[8];
      while (landed < 2 * pc + 2) refresh();
      k_read(R0); advance(4);
      while (landed < 2 * pc + 2) refresh();
      k_read(R1); advance(4);
      while (landed < 2 * pc + 2) refresh();
      k_read(R2); advance(4);
      if (wave == 0) {
        u32x4 g0 = {0u, 0u, ~tag[0], 0u}, g1 = g0;
        const unsigned qo = (unsigned)(h * 2048 + lane * 32);
        unsigned spins = 0;
        for (;;) {
          const u64 miss = __ballot(!(g0[2] == tag[0] && g0[3] == tag[1] && g1[2] == tag[0] && g1[3] == tag[1]));
          if (miss == 0) break;
          if (++spins > SPIN_CAP) { bad = true; break; }
          repoll2(g0, g1, qo, qo + 16, ws + WS_Q, miss, miss);
        }
        *(LAS u32x4*)(smem + L_Q + lane * 16) = u32x4{g0[0], g0[1], g1[0], g1[1]};
        lds_st(lbase + L_QFLAG, 1u);
        DBGVAL(15, spins);
      } else {
        while (lds_ld(lbase + L_QFLAG) == 0) __builtin_amdgcn_s_sleep(1);
      }
      q0 = *(const LAS u32x4*)(smem + L_Q + (lane & 31) * 16); q1 = *(const LAS u32x4*)(smem + L_Q + 512 + (lane & 31) * 16);
      STAMP(5);
      k_scores(R0, 0); k_scores(R1, 1); k_scores(R2, 2);
      STAMP(6);
    }
    for (int t = 3; t < SSTAT / 2; ++t) {
      u32x4 R[8];
      while (landed < 2 * pc + 2) refresh();
      k_read(R); advance(4);
      k_scores(R, t);
    }
    m = head_max(m); macc = m;
    f32x2 y = {0.f, 0.f}, wof[32];
    unsigned xo = 0, bo = 0;
    const unsigned tc = (tid < 224 ? tid : 223) * 4u;
    if (has_mlp) {
      const int hg = role >> 2;
      u32x4 xv, wv[14]; unsigned b1w;
      asm volatile("global_load_dwordx4 %0, %1, %2" : "=v"(xv) : "v"((lane < 56 ? lane : 55) * 16u), "s"(x));
      asm volatile("global_load_ushort %0, %1, %2" : "=v"(b1w) : "v"(lane * 2u), "s"(b1 + hg * 64));
      const unsigned o = (unsigned)(tid >> 3) * (F * 2u) + (tid & 7) * 16u;
#pragma unroll
      for (int i = 0; i < 14; ++i) LD_NT(wv[i], o, w1 + hg * 128 + i * (32 * F * 2), 0);
      WAITV(14, xv); WAITV(14, b1w);
      layer_norm(xv, xn, lane);
#pragma unroll
      for (int i = 0; i < 14; ++i) WAITV(0, wv[i]);
      slice_partials(wv, xn, red + wave * 64, tid);
      arrive(L_HCNT);
      while (lds_ld(lbase + L_HCNT) < NC) __builtin_amdgcn_s_sleep(1);
      const float hv = rbf(rbf((red[lane] + red[64 + lane]) + (red[128 + lane] + red[192 + lane])) + lo16(b1w));
      const float u = 0.7978845608028654f * (hv + 0.044715f * hv * hv * hv);
      const float e = __builtin_amdgcn_exp2f(u * 2.885390081777927f);
      const float ge = rbf(hv * (1.f - __builtin_amdgcn_rcpf(e + 1.f)));
#pragma unroll
      for (int c = 0; c < 2; ++c) {
        unsigned w[32];
#pragma unroll
        for (int r = 0; r < 32; ++r) LD1_NT(w[r], tc, w2 + (size_t)(hg * 64 + c * 32 + (r & ~3)) * (D * 2), (r & 3) * (D * 2));
#pragma unroll
        for (int r = 0; r < 32; ++r) WAITV(0, w[r]);
#pragma unroll
        for (int r = 0; r < 32; ++r) y += f32x2{lo16(w[r]), hi16(w[r])} * lane_of(ge, c * 32 + r);
      }
    }
    if (own_o) {
      unsigned w[32];
#pragma unroll
      for (int r = 0; r < 32; ++r) LD1_NT(w[r], tc, wo + (size_t)(role * 32 + (r & ~3)) * (D * 2), (r & 3) * (D * 2));
#pragma unroll
      for (int r = 0; r < 32; ++r) WAITV(0, w[r]);
#pragma unroll
      for (int r = 0; r < 32; ++r) wof[r] = f32x2{lo16(w[r]), hi16(w[r])};
    } else {
#pragma unroll
      for (int r = 0; r < 32; ++r) wof[r] = f32x2{0.f, 0.f};
      asm volatile("global_load_dword %0, %2, %3\n\tglobal_load_dword %1, %2, %4\n\ts_waitcnt vmcnt(0)"
                   : "=&v"(xo), "=&v"(bo) : "v"((lane & 3) * 4u), "s"(x + (own_out ? role >> 1 : 0) * 8), "s"(b2 + (own_out ? role >> 1 : 0) * 8));
    }
#pragma unroll
    for (int r = 0; r < 32; ++r) asm volatile("" : "+v"(wof[r]));
    asm volatile("" : "+v"(y));
    const int nv = SSTAT / 2 - (wave >> 1);
    for (int t = 0; t < nv; ++t) { v_step(t); advance(4); }
    if (wave >= 2) { advance(2); v_step(SSTAT / 2 - 1); advance(2); } else { pc -= 2; slot -= 2; if (slot < 0) { slot += NSLOT; --gen; } }
    for (;;) {
      while (landed < 2 * pc + 2 && endq >= 2 * pc + 2) refresh();
      if (endq < 2 * pc + 2) break;
      u32x4 R[8];
      k_read(R); advance(6);
      k_scores(R, 0);
      m = head_max(m);
      const float alpha = __builtin_amdgcn_exp2f((macc - m) * C2);
      macc = m; l *= alpha;
      const float al0 = lane_of(alpha, 0), al1 = lane_of(alpha, 16);
#pragma unroll
      for (int k = 0; k < 4; ++k) { a0[k] *= al0; a1[k] *= al1; }
      v_step(0); advance(2);
    }
    STAMP(8);
    LAS float* rec = (LAS float*)(smem + L_REC);
    LAS f32x2* mlr = (LAS f32x2*)(smem + L_ML);
    {
      f32x4 o0, o1;
#pragma unroll
      for (int k = 0; k < 2; ++k) {
        o0[2 * k] = swap32_add(a0[k][0], a1[k][0]); o0[2 * k + 1] = swap32_add(a0[k][1], a1[k][1]);
        o1[2 * k] = swap32_add(a0[2 + k][0], a1[2 + k][0]); o1[2 * k + 1] = swap32_add(a0[2 + k][1], a1[2 + k][1]);
      }
      *(LAS f32x4*)(rec + wave * 512 + 8 * lane) = o0; *(LAS f32x4*)(rec + wave * 512 + 8 * lane + 4) = o1;
      l += dpp<ROR8>(l); l += dpp<HMIR>(l); l += dpp<XOR2>(l); l = swap32_add(l, l);
      if ((lane & 47) == 0) mlr[(lane >> 4) * NC + wave] = f32x2{m, l};
    }
    wg_barrier();
    const unsigned tag16 = tag[0] & 0xffffu;
    {
      const LAS f32x4* mp = (const LAS f32x4*)(mlr + (tid >> 7) * NC);
      const f32x4 m01 = mp[0], m23 = mp[1];
      const LAS f32x2* ap = (const LAS f32x2*)rec + tid;
      const f32x2 v0 = ap[0], v1 = ap[256], v2 = ap[512], v3 = ap[768];
      const float M = vmax(vmax(m01[0], m01[2]), vmax(m23[0], m23[2]));
      const float f0 = __builtin_amdgcn_exp2f((m01[0] - M) * C2), f1 = __builtin_amdgcn_exp2f((m01[2] - M) * C2);
      const float f2 = __builtin_amdgcn_exp2f((m23[0] - M) * C2), f3 = __builtin_amdgcn_exp2f((m23[2] - M) * C2);
      const f32x2 o = (v0 * f0 + v1 * f1) + (v2 * f2 + v3 * f3);
      const unsigned L = __float_as_uint((f0 * m01[1] + f1 * m01[3]) + (f2 * m23[1] + f3 * m23[3]));
      const unsigned pay = (lane & 2) ? L << 16 : ((lane & 1) ? L : __float_as_uint(M)) & 0xffff0000u;
      const u32x4 gd = {__float_as_uint(o[0]), __float_as_uint(o[1]), pay | tag16, tag[1]};
      ST4_SC1(o_pd, gd, ws + WS_PD);
    }
    STAMP(9);
    u32x4 g0, g1;
    if (own_o) {
      g0 = u32x4{0u, 0u, 0u, ~tag[1]}; g1 = g0;
      unsigned spins = 0;
      for (;;) {
        const u64 m0 = __ballot(!((g0[2] & 0xffffu) == tag16 && g0[3] == tag[1])), m1 = __ballot(!((g1[2] & 0xffffu) == tag16 && g1[3] == tag[1]));
        if ((m0 | m1) == 0) break;
        if (++spins > SPIN_CAP) { bad = true; break; }
        repoll2(g0, g1, o_g0, o_g1, gbase, m0, m1);
      }
      STAMP(10);
      const float ma = hi16(dppu<0x00>(g0[2])), mb = hi16(dppu<0x00>(g1[2]));
      const float Mw = rows_max(vmax(ma, mb));
      const float fa = __builtin_amdgcn_exp2f((ma - Mw) * C2), fb = __builtin_amdgcn_exp2f((mb - Mw) * C2);
      const float la = __uint_as_float((dppu<0x55>(g0[2]) & 0xffff0000u) | (dppu<0xAA>(g0[2]) >> 16));
      const float lb = __uint_as_float((dppu<0x55>(g1[2]) & 0xffff0000u) | (dppu<0xAA>(g1[2]) >> 16));
      const float oe = fa * __uint_as_float(g0[0]) + fb * __uint_as_float(g1[0]), oo = fa * __uint_as_float(g0[1]) + fb * __uint_as_float(g1[1]);
      const float Lp = fa * la + fb * lb;
      const float t = swap32_add(swap16_add(oe, oo), swap16_add(Lp, Lp));
      LAS float* redo = (LAS float*)(smem + L_REDO);
      LAS float* ml2 = (LAS float*)(smem + L_ML2);
      if (lane < 32) redo[(2 * (lane & 15) + (lane >> 4)) * NC + wave] = t;
      if (lane == 32) { ml2[wave] = Mw; ml2[NC + wave] = t; }
      wg_barrier();
      const f32x4 ov = ((const LAS f32x4*)redo)[lane & 31], Mv = ((const LAS f32x4*)ml2)[0], Lv = ((const LAS f32x4*)ml2)[1];
      const float M = vmax(vmax(Mv[0], Mv[1]), vmax(Mv[2], Mv[3]));
      f32x4 fv;
#pragma unroll
      for (int w = 0; w < NC; ++w) fv[w] = __builtin_amdgcn_exp2f((Mv[w] - M) * C2);
      const f32x4 on4 = ov * fv, L4 = Lv * fv;
      const float on = rbf(((on4[0] + on4[1]) + (on4[2] + on4[3])) * __builtin_amdgcn_rcpf((L4[0] + L4[1]) + (L4[2] + L4[3])));
      f32x2 y1 = {0.f, 0.f}, y2 = y1, y3 = y1;
#pragma unroll
      for (int r = 0; r < 32; r += 4) {
        y += wof[r] * lane_of(on, r); y1 += wof[r + 1] * lane_of(on, r + 1); y2 += wof[r + 2] * lane_of(on, r + 2); y3 += wof[r + 3] * lane_of(on, r + 3);
      }
      y = (y + y1) + (y2 + y3);
      if (bad) y[0] = __uint_as_float(0x7fc00000u);
      if (tid < 224) {
        const u32x4 gy = {__float_as_uint(y[0]), __float_as_uint(y[1]), tag[0], tag[1]};
        ST4_SC1(o_y, gy, ws + WS_Y);
      }
      STAMP(11);
    } else if (own_out) {
      g0 = u32x4{0u, 0u, ~tag[0], 0u}; g1 = g0;
      unsigned spins = 0;
      for (;;) {
        const u64 m0 = __ballot(!(g0[2] == tag[0] && g0[3] == tag[1])), m1 = __ballot(!(g1[2] == tag[0] && g1[3] == tag[1]));
        if ((m0 | m1) == 0) break;
        if (++spins > SPIN_CAP) { bad = true; break; }
        const u64 gate = (spins == 1 || (m0 & m1) != ~0ull) ? ~0ull : 1ull;
        repoll2(g0, g1, o_g0, o_g1, gbase, m0 & gate, m1 & gate);
      }
      STAMP(12);
      float s0 = __uint_as_float(g0[0]) + __uint_as_float(g1[0]), s1 = __uint_as_float(g0[1]) + __uint_as_float(g1[1]);
      if (bad) s0 = __uint_as_float(0x7fc00000u);
      s0 += dpp<ROR8>(s0); s1 += dpp<ROR8>(s1); s0 += dpp<ROR4>(s0); s1 += dpp<ROR4>(s1);
      float ss = swap16_add(s0, s1); ss = swap32_add(ss, ss);
      LAS float* redy = (LAS float*)(smem + L_REDO);
      if ((lane & 44) == 0) redy[wave * 8 + 2 * (lane & 3) + (lane >> 4)] = ss;
      wg_barrier();
      if (tid < 4) {
        const LAS f32x2* ry = (const LAS f32x2*)redy + tid;
        const f32x2 t = (ry[0] + ry[4]) + (ry[8] + ry[12]);
        out[(role >> 1) * 4 + tid] = pack_bf16(lo16(xo) + t[0] + lo16(bo), hi16(xo) + t[1] + hi16(bo));
        if (role == 1 && tid == 0) *(u32x2*)(ws + WS_E) = tag;
      }
      STAMP(13);
    }
  }
}

__global__ void nop_kernel(int* p) { if (p == nullptr) __builtin_trap(); }
extern "C" void kernel_launch(void* const* d_in, const int* in_sizes, int n_in, void* d_out, int out_size, void* d_ws, size_t ws_size, hipStream_t stream) {
  fused_kernel<<<NWG, 320, 0, stream>>>((const u16*)d_in[0], (const char*)d_in[1], (const char*)d_in[2], (const char*)d_in[3], (const char*)d_in[4],
                                        (const char*)d_in[6], (const u16*)d_in[7], (const char*)d_in[8], (const u16*)d_in[9], (unsigned*)d_out, (char*)d_ws);
  nop_kernel<<<1, 64, 0, stream>>>((int*)d_ws);
}
```

```cpp
#define DBG 1
#include <hip/hip_runtime.h>
#include <hip/hip_bf16.h>
#include <cstdint>
#include <type_traits>

typedef unsigned u32x4 __attribute__((ext_vector_type(4)));
typedef unsigned u32x2 __attribute__((ext_vector_type(2)));
typedef float f32x4 __attribute__((ext_vector_type(4)));
typedef float f32x2 __attribute__((ext_vector_type(2)));
typedef __bf16 bf16x2 __attribute__((ext_vector_type(2)));
typedef unsigned short u16;
typedef unsigned long long u64;
#define LAS __attribute__((address_space(3)))

#define INFL 28
#define SSTAT 56

constexpr int D = 448, F = 1792, DH = 256;
constexpr int NWG = 256, NC = 4;
constexpr int PIECE = 8192, NSLOT = 18;
constexpr int RPR = 64;
constexpr int TAILR = RPR - SSTAT, POOL = 8 * TAILR;
static_assert(POOL >= 24 && INFL % 4 == 0 && INFL >= 20 && INFL <= 32 && SSTAT % 2 == 0 && SSTAT >= 8 && (SSTAT / 2 + 1) * 64 <= 2048, "loader structure");
constexpr unsigned PD_STRIDE = 33 * 128, Y_STRIDE = 29 * 128;
constexpr size_t WS_E = 0, WS_CTR = 4096, WS_Q = 16384, WS_PD = 65536, WS_Y = WS_PD + (size_t)NWG * PD_STRIDE, WS_DBG = 4u << 20;
constexpr unsigned SPIN_CAP = 1u << 22;
constexpr unsigned L_CTL = NSLOT * PIECE;
constexpr unsigned L_LANDED = L_CTL, L_QFLAG = L_CTL + 8, L_QCNT = L_CTL + 12, L_HCNT = L_CTL + 16, L_FREE = L_CTL + 32;
constexpr unsigned L_Q = L_CTL + 128, L_RED = L_Q + 1024, L_REDO = L_RED + 1024, L_ML2 = L_REDO + 512, L_P = L_ML2 + 64;
constexpr unsigned L_ML = L_P + NC * 128, L_XN = L_ML + 64, L_REC = L_XN + 2048, L_END = L_REC + NC * 2 * DH * 4;
static_assert(L_END <= 160 * 1024 && D <= 2 * DH, "LDS");
constexpr float C2 = 0.0625f * 1.4426950408889634f;

__device__ __forceinline__ float lo16(unsigned w) { return __uint_as_float(w << 16); }
__device__ __forceinline__ float hi16(unsigned w) { return __uint_as_float(w & 0xffff0000u); }
__device__ __forceinline__ unsigned pack_bf16(float a, float b) {
  bf16x2 r = {(__bf16)a, (__bf16)b};
  return __builtin_bit_cast(unsigned, r);
}
__device__ __forceinline__ float rbf(float x) { return lo16(pack_bf16(x, x)); }
__device__ __forceinline__ float dot2(unsigned k, unsigned q, float acc) {
  return __builtin_amdgcn_fdot2_f32_bf16(__builtin_bit_cast(bf16x2, k), __builtin_bit_cast(bf16x2, q), acc, false);
}
template <int CTRL> __device__ __forceinline__ unsigned dppu(unsigned x) { return (unsigned)__builtin_amdgcn_mov_dpp((int)x, CTRL, 0xf, 0xf, true); }
template <int CTRL> __device__ __forceinline__ float dpp(float x) { return __uint_as_float(dppu<CTRL>(__float_as_uint(x))); }
constexpr int XOR1 = 0xB1, XOR2 = 0x4E, HMIR = 0x141, MIR = 0x140, ROR8 = 0x128, ROR4 = 0x124;
__device__ __forceinline__ float swap16_add(float a, float b) {
  auto r = __builtin_amdgcn_permlane16_swap(__float_as_uint(a), __float_as_uint(b), false, false);
  return __uint_as_float(r[0]) + __uint_as_float(r[1]);
}
__device__ __forceinline__ float swap32_add(float a, float b) {
  auto r = __builtin_amdgcn_permlane32_swap(__float_as_uint(a), __float_as_uint(b), false, false);
  return __uint_as_float(r[0]) + __uint_as_float(r[1]);
}
__device__ __forceinline__ float row_sum16(float x) { x += dpp<XOR1>(x); x += dpp<XOR2>(x); x += dpp<HMIR>(x); x += dpp<MIR>(x); return x; }
__device__ __forceinline__ float wave_sum(float x) { x = row_sum16(x); x = swap16_add(x, x); return swap32_add(x, x); }
__device__ __forceinline__ float lane_of(float x, int l) { return __builtin_bit_cast(float, __builtin_amdgcn_readlane(__builtin_bit_cast(int, x), l)); }
__device__ __forceinline__ float row_transpose_sum8(const float (&v)[8], u64 bit1) {
  float t0, t1, t2, t3, r;
  asm volatile("s_nop 1\n\t"
      "v_add_f32_dpp %0, %5, %5 row_ror:8 row_mask:0xf bank_mask:0xf\n\tv_add_f32_dpp %1, %6, %6 row_ror:8 row_mask:0xf bank_mask:0xf\n\t"
      "v_add_f32_dpp %2, %7, %7 row_ror:8 row_mask:0xf bank_mask:0xf\n\tv_add_f32_dpp %3, %8, %8 row_ror:8 row_mask:0xf bank_mask:0xf\n\t"
      "v_add_f32_dpp %0, %9, %9 row_ror:8 row_mask:0xf bank_mask:0xc\n\tv_add_f32_dpp %1, %10, %10 row_ror:8 row_mask:0xf bank_mask:0xc\n\t"
      "v_add_f32_dpp %2, %11, %11 row_ror:8 row_mask:0xf bank_mask:0xc\n\tv_add_f32_dpp %3, %12, %12 row_ror:8 row_mask:0xf bank_mask:0xc\n\t"
      "v_add_f32_dpp %0, %0, %0 row_half_mirror row_mask:0xf bank_mask:0x5\n\tv_add_f32_dpp %1, %1, %1 row_half_mirror row_mask:0xf bank_mask:0x5\n\t"
      "v_add_f32_dpp %0, %2, %2 row_half_mirror row_mask:0xf bank_mask:0xa\n\tv_add_f32_dpp %1, %3, %3 row_half_mirror row_mask:0xf bank_mask:0xa\n\t"
      "s_nop 1\n\t"
      "v_add_f32_dpp %2, %0, %0 quad_perm:[2,3,0,1] row_mask:0xf bank_mask:0xf\n\tv_add_f32_dpp %3, %1, %1 quad_perm:[2,3,0,1] row_mask:0xf bank_mask:0xf\n\t"
      "v_cndmask_b32 %2, %2, %3, %13\n\ts_nop 1\n\t"
      "v_add_f32_dpp %4, %2, %2 quad_perm:[1,0,3,2] row_mask:0xf bank_mask:0xf"
      : "=&v"(t0), "=&v"(t1), "=&v"(t2), "=&v"(t3), "=&v"(r)
      : "v"(v[0]), "v"(v[1]), "v"(v[2]), "v"(v[3]), "v"(v[4]), "v"(v[5]), "v"(v[6]), "v"(v[7]), "s"(bit1));
  return r;
}
__device__ __forceinline__ float head_max(float x) {
  float y;
  asm volatile("s_nop 1\n\tv_max_f32_dpp %0, %0, %0 row_ror:8 row_mask:0xf bank_mask:0xf\n\ts_nop 1\n\tv_max_f32_dpp %0, %0, %0 row_half_mirror row_mask:0xf bank_mask:0xf\n\t"
               "s_nop 1\n\tv_max_f32_dpp %0, %0, %0 quad_perm:[2,3,0,1] row_mask:0xf bank_mask:0xf\n\tv_mov_b32 %1, %0\n\ts_nop 1\n\tv_permlane32_swap_b32 %0, %1\n\tv_max_f32 %0, %0, %1"
               : "+v"(x), "=&v"(y));
  return x;
}
__device__ __forceinline__ float rows_max(float x) {
  float y;
  asm volatile("v_mov_b32 %1, %0\n\ts_nop 1\n\tv_permlane16_swap_b32 %0, %1\n\tv_max_f32 %0, %0, %1\n\tv_mov_b32 %1, %0\n\ts_nop 1\n\tv_permlane32_swap_b32 %0, %1\n\tv_max_f32 %0, %0, %1"
               : "+v"(x), "=&v"(y));
  return x;
}
__device__ __forceinline__ float vmax(float a, float b) { float r; asm("v_max_f32 %0, %1, %2" : "=v"(r) : "v"(a), "v"(b)); return r; }
__device__ __forceinline__ void layer_norm(const u32x4& xv, LAS float* xn, int lane) {
  float xf[8];
#pragma unroll
  for (int k = 0; k < 4; ++k) { xf[2 * k] = lo16(xv[k]); xf[2 * k + 1] = hi16(xv[k]); }
  const float live = lane < 56 ? 1.f : 0.f;
  const float mean = wave_sum((((xf[0] + xf[1]) + (xf[2] + xf[3])) + ((xf[4] + xf[5]) + (xf[6] + xf[7]))) * live) * (1.f / D);
  float ss = 0.f;
#pragma unroll
  for (int k = 0; k < 8; ++k) { const float d = xf[k] - mean; ss += d * d; }
  const float var = wave_sum(ss * live) * (1.f / D);
  const float rden = __builtin_amdgcn_rcpf(rbf(__builtin_amdgcn_sqrtf(rbf(rbf(var) + 1e-5f)))) * live, mb = rbf(mean);
#pragma unroll
  for (int k = 0; k < 8; k += 2) {
    const unsigned c = pack_bf16(xf[k] - mb, xf[k + 1] - mb), n = pack_bf16(lo16(c) * rden, hi16(c) * rden);
    xf[k] = lo16(n); xf[k + 1] = hi16(n);
  }
  *(LAS f32x4*)(xn + 8 * lane) = f32x4{xf[0], xf[1], xf[2], xf[3]}; *(LAS f32x4*)(xn + 8 * lane + 4) = f32x4{xf[4], xf[5], xf[6], xf[7]};
}
__device__ __forceinline__ void slice_partials(const u32x4 (&w)[14], const LAS float* xn, LAS float* red, int tid) {
  f32x2 v[4] = {{0.f, 0.f}, {0.f, 0.f}, {0.f, 0.f}, {0.f, 0.f}};
#pragma unroll
  for (int i = 0; i < 14; ++i) {
    const float xr = xn[(tid >> 3) + 32 * i];
#pragma unroll
    for (int k = 0; k < 4; ++k) v[k] += f32x2{lo16(w[i][k]), hi16(w[i][k])} * xr;
  }
  const float a0 = swap32_add(v[0][0], v[2][0]), a1 = swap32_add(v[0][1], v[2][1]), a2 = swap32_add(v[1][0], v[3][0]), a3 = swap32_add(v[1][1], v[3][1]);
  const float b0 = swap16_add(a0, a2), b1 = swap16_add(a1, a3);
  const float t1 = b0 + dpp<ROR8>(b0), t2 = b1 + dpp<ROR8>(b1);
  red[(tid & 7) * 8 + ((tid >> 3) & 7)] = (tid & 8) ? t2 : t1;
}

#define LD_NT(dst, voff, sbase, imm) asm volatile("global_load_dwordx4 %0, %1, %2 offset:%3 nt" : "=v"(dst) : "v"(voff), "s"(sbase), "i"(imm))
#define LD1_NT(dst, voff, sbase, imm) asm volatile("global_load_dword %0, %1, %2 offset:%3 nt" : "=v"(dst) : "v"(voff), "s"(sbase), "i"(imm))
#define LD4_SC1(dst, voff, sbase, imm) asm volatile("global_load_dwordx4 %0, %1, %2 offset:%3 sc1" : "=v"(dst) : "v"(voff), "s"(sbase), "i"(imm) : "memory")
#define ST4_SC1(voff, val, sbase) asm volatile("global_store_dwordx4 %0, %1, %2 sc1\n\ts_nop 1" :: "v"(voff), "v"(val), "s"(sbase) : "memory")
#define WAITV(n, reg) asm volatile("s_waitcnt vmcnt(%1)" : "+v"(reg) : "i"(n))
#define VMCNT(n) asm volatile("s_waitcnt vmcnt(%0)" :: "i"(n) : "memory")
__device__ __forceinline__ void repoll2(u32x4& g0, u32x4& g1, unsigned o0, unsigned o1, const char* base, u64 m0, u64 m1) {
  asm volatile("s_mov_b64 exec, %5\n\tglobal_load_dwordx4 %0, %2, %4 sc1\n\ts_mov_b64 exec, %6\n\tglobal_load_dwordx4 %1, %3, %4 sc1\n\ts_mov_b64 exec, -1\n\ts_waitcnt vmcnt(0)"
               : "+v"(g0), "+v"(g1) : "v"(o0), "v"(o1), "s"(base), "s"(m0), "s"(m1) : "memory");
}
__device__ __forceinline__ void wg_barrier() { asm volatile("s_waitcnt lgkmcnt(0)\n\ts_barrier" ::: "memory"); }
__device__ __forceinline__ void lds_st(unsigned addr, unsigned v) { asm volatile("ds_write_b32 %0, %1" :: "v"(addr), "v"(v) : "memory"); }
__device__ __forceinline__ unsigned lds_ld(unsigned addr) {
  unsigned v; asm volatile("ds_read_b32 %0, %1\n\ts_waitcnt lgkmcnt(0)" : "=v"(v) : "v"(addr) : "memory"); return __builtin_amdgcn_readfirstlane(v); }
template <int N> __device__ __forceinline__ void dma(const char* g, unsigned voff, unsigned lds) {
  if constexpr (N == 4)
    asm volatile("s_mov_b32 m0, %2\n\ts_nop 0\n\tglobal_load_lds_dwordx4 %0, %1 nt\n\tglobal_load_lds_dwordx4 %0, %1 offset:1024 nt\n\t"
                 "global_load_lds_dwordx4 %0, %1 offset:2048 nt\n\tglobal_load_lds_dwordx4 %0, %1 offset:3072 nt" :: "v"(voff), "s"(g), "s"(lds) : "memory");
  else
    asm volatile("s_mov_b32 m0, %2\n\ts_nop 0\n\tglobal_load_lds_dwordx4 %0, %1 nt\n\tglobal_load_lds_dwordx4 %0, %1 offset:1024 nt" :: "v"(voff), "s"(g), "s"(lds) : "memory");
}

#define STAMP(i) do { unsigned t_ = (unsigned)__builtin_amdgcn_s_memrealtime(); stamps = (lane == (i)) ? t_ : stamps; } while (0)
#define DBGVAL(i, v) do { stamps = (lane == (i)) ? (unsigned)(v) : stamps; } while (0)

__global__ __launch_bounds__(320) void fused_kernel(const u16* __restrict__ x, const char* __restrict__ kc, const char* __restrict__ vc,
                                                    const char* __restrict__ wq, const char* __restrict__ wo, const char* __restrict__ w1,
                                                    const u16* __restrict__ b1, const char* __restrict__ w2, const u16* __restrict__ b2,
                                                    unsigned* __restrict__ out, char* __restrict__ ws) {
  __shared__ __attribute__((aligned(1024))) char smem[L_END];
  const unsigned lbase = (unsigned)(uintptr_t)(LAS char*)smem;
  const int b = blockIdx.x, tid = threadIdx.x, lane = tid & 63, wave = __builtin_amdgcn_readfirstlane(tid >> 6);
  const int role = b >> 1;
  const bool own_o = ((b ^ (b >> 3)) & 1) == 0, has_mlp = own_o && (role & 3) == 0 && role < 112;
  const bool make_q = !own_o && (role & 1) == 0, own_out = !own_o && (role & 1) == 1 && role < 112;
  unsigned stamps = 0;
  STAMP(0);
  if (wave == NC) {
    __builtin_amdgcn_s_setprio(3);
    if (lane < 8 + NSLOT) lds_st(lbase + L_CTL + lane * 4, lane == 1 ? 0xffffffffu : 0u);
    const unsigned voff = lane * 16;
    const int q = b >> 3;
    unsigned* ctr = (unsigned*)(ws + WS_CTR) + q * 64;
    int nq = 0, slot = 0, gen = 0;
    constexpr int LAG = INFL / 4 - 1;
    int frs = 0;
    auto put = [&](const char* g, int u) {
      if ((u & 1) == 0 && frs < gen) { while ((int)lds_ld(lbase + L_FREE + slot * 4) < gen) __builtin_amdgcn_s_sleep(1); }
      dma<4>(g, voff, lbase + slot * PIECE + (u & 1) * 4096);
      ++nq;
      if (u & 1) { if (++slot == NSLOT) { slot = 0; ++gen; } }
    };
    auto settle = [&](int u) {
      if (u & 1) {
        unsigned fr;
        asm volatile("ds_read_b32 %0, %1\n\ts_waitcnt vmcnt(%2) lgkmcnt(0)" : "=&v"(fr) : "v"(lbase + L_FREE + slot * 4), "i"(INFL - 4) : "memory");
        frs = __builtin_amdgcn_readfirstlane(fr);
      } else VMCNT(INFL - 4);
      lds_st(lbase + L_LANDED, max(nq - LAG, 0));
    };
    unsigned tv = 0;
    auto ticket = [&]() { asm volatile("s_mov_b64 exec, 1\n\tglobal_atomic_add %0, %1, %2, %3 sc0\n\ts_mov_b64 exec, -1" : "=v"(tv) : "v"(0u), "v"(1u), "s"(ctr) : "memory"); };
    size_t voffs = (size_t)b * (1u << 20);
    {
      const char* g = kc + voffs;
      put(g, 0); put(g + 4096, 1);
      wg_barrier();
      for (int p = 1; p < 2 * SSTAT; ++p) { g += PIECE; put(g, 0); settle(0); put(g + 4096, 1); settle(1);
        if (p == 3) STAMP(4);
        if (p == 16) STAMP(7);
      }
      STAMP(5);
      g = vc + voffs;
      for (int p = 0; p < 2 * SSTAT - 6; ++p) { put(g, 0); settle(0); put(g + 4096, 1); settle(1); g += PIECE; }
      while (lds_ld(lbase + L_QFLAG) == 0) __builtin_amdgcn_s_sleep(1);
#pragma unroll
      for (int u = 0; u < 8; ++u) { if (u == 8 - INFL / 4) ticket(); put(g + u * 4096, u); settle(u); }
      STAMP(6);
      voffs += (size_t)(SSTAT - 1) * (2 * PIECE);
    }
    for (;;) {
      asm volatile("" : "+v"(tv));
      const int k = __builtin_amdgcn_readfirstlane(tv);
      constexpr int A = POOL > 24 ? POOL - 24 : 0, N4 = (A + 3) / 4;
      int cur, sz;
      if (k < N4) { cur = 4 * k; sz = min(4, A - cur); } else if (k < N4 + 8) { cur = A + 2 * (k - N4); sz = 2; } else { cur = A + 16 + (k - N4 - 8); sz = 1; }
      if (cur >= POOL) break;
      for (; sz > 0; --sz, ++cur) {
        const size_t off = (size_t)(8 * q + cur / TAILR) * (1u << 20) + (size_t)(SSTAT + cur % TAILR) * (2 * PIECE);
        const char* kp = kc + off; const char* vp = vc + voffs;
#pragma unroll
        for (int u = 0; u < 8; ++u) {
          if (u == 8 - INFL / 4 && sz == 1) ticket();
          put(u < 4 ? kp + u * 4096 : vp + (u - 4) * 4096, u);
          settle(u);
        }
        voffs = off;
      }
    }
    lds_st(lbase + L_LANDED + 4, nq);
    STAMP(1); DBGVAL(15, nq + 4);
    slot += 2; if (slot >= NSLOT) { slot -= NSLOT; ++gen; }
    frs = 0;
#pragma unroll
    for (int u = 0; u < 4; ++u) { put(vc + voffs + u * 4096, u); settle(u); }
#pragma unroll
    for (int i = LAG - 1; i >= 0; --i) { VMCNT(4 * i); lds_st(lbase + L_LANDED, nq - i + (i < 4 ? 4 : 0)); }
    STAMP(2);
  } else {
    const int h = b >> 5;
    LAS float* xn = (LAS float*)(smem + L_XN);
    LAS float* red = (LAS float*)(smem + L_RED);
    u32x2 ev, tag;
    auto arrive = [&](unsigned cnt) -> int {
      unsigned arr;
      asm volatile("s_mov_b64 exec, 1\n\tds_add_rtn_u32 %0, %1, %2\n\ts_mov_b64 exec, -1\n\ts_waitcnt lgkmcnt(0)" : "=&v"(arr) : "v"(lbase + cnt), "v"(1u) : "memory");
      return __builtin_amdgcn_readfirstlane(arr);
    };
    auto make_tag = [&]() {
      const u64 e = (u64)(unsigned)__builtin_amdgcn_readfirstlane(ev[1]) << 32 | (unsigned)__builtin_amdgcn_readfirstlane(ev[0]);
      const u64 z = e * 6364136223846793005ull + 1442695040888963407ull;
      tag = u32x2{(unsigned)z, (unsigned)(z >> 32)};
    };
    if (make_q) {
      const int qi = role >> 1;
      if (wave == 0 && (qi & 4) == 0) asm volatile("global_store_dword %0, %1, %2 sc1" :: "v"(0u), "v"(0u), "s"(ws + WS_CTR + ((qi >> 3) * 4 + (qi & 3)) * 256) : "memory");
      u32x4 xv, wv[14];
      asm volatile("global_load_dwordx4 %0, %1, %2" : "=v"(xv) : "v"((lane < 56 ? lane : 55) * 16u), "s"(x));
      const unsigned o = (unsigned)(tid >> 3) * 8192u + (tid & 7) * 16u;
#pragma unroll
      for (int i = 0; i < 14; ++i) LD_NT(wv[i], o, wq + qi * 128 + i * (32 * 8192), 0);
      asm volatile("global_load_dwordx2 %0, %1, %2 sc1" : "=v"(ev) : "v"(0u), "s"(ws + WS_E) : "memory");
      STAMP(4);
      wg_barrier();
      WAITV(15, xv);
      STAMP(7);
      layer_norm(xv, xn, lane);
      STAMP(1);
#pragma unroll
      for (int i = 0; i < 14; ++i) WAITV(1, wv[i]);
      STAMP(2);
      slice_partials(wv, xn, red + wave * 64, tid);
      WAITV(0, ev);
      make_tag();
      if (arrive(L_QCNT) == NC - 1 && lane < 16) {
        const LAS f32x4* rq = (const LAS f32x4*)red + lane;
        const f32x4 s = (rq[0] + rq[16]) + (rq[32] + rq[48]);
        const u32x4 g = {pack_bf16(s[0], s[1]), pack_bf16(s[2], s[3]), tag[0], tag[1]};
        ST4_SC1((unsigned)(qi * 256 + lane * 16), g, ws + WS_Q);
      }
      STAMP(3);
    } else {
      asm volatile("global_load_dwordx2 %0, %1, %2 sc1" : "=v"(ev) : "v"(0u), "s"(ws + WS_E) : "memory");
      wg_barrier();
      WAITV(0, ev);
      make_tag();
    }
    bool bad = false;
    unsigned o_pd = (unsigned)(b * PD_STRIDE + tid * 16), o_y = (unsigned)(role * Y_STRIDE + (tid < 224 ? tid : 0) * 16);
    unsigned o_g0 = own_o ? (unsigned)((h * 32 + (tid >> 4)) * PD_STRIDE + (role & 15) * 256 + (tid & 15) * 16)
                          : (unsigned)((tid >> 2) * Y_STRIDE + (own_out ? role >> 1 : 0) * 64 + (tid & 3) * 16);
    unsigned o_g1 = o_g0 + (own_o ? 16 * PD_STRIDE : 64 * Y_STRIDE);
    asm volatile("" : "+v"(o_pd), "+v"(o_y), "+v"(o_g0), "+v"(o_g1));
    const char* gbase = ws + (own_o ? WS_PD : WS_Y);
    float m = -INFINITY, macc = -INFINITY, l = 0.f;
    f32x2 a0[4], a1[4];
#pragma unroll
    for (int k = 0; k < 4; ++k) { a0[k] = f32x2{0.f, 0.f}; a1[k] = f32x2{0.f, 0.f}; }
    unsigned landed = 0, endq = 0xffffffffu;
    unsigned pc = wave; int slot = wave, gen = 1;
    u32x4 q0, q1;
    LAS u16* scw = (LAS u16*)(smem + L_REC + wave * 2048) + (lane >> 1);
    auto refresh = [&]() {
      u32x2 c; asm volatile("ds_read_b64 %0, %1\n\ts_waitcnt lgkmcnt(0)" : "=v"(c) : "v"(lbase + L_LANDED) : "memory");
      landed = __builtin_amdgcn_readfirstlane(c[0]); endq = __builtin_amdgcn_readfirstlane(c[1]);
    };
    auto advance = [&](int n) { pc += n; slot += n; if (slot >= NSLOT) { slot -= NSLOT; ++gen; } };
    auto k_read = [&](u32x4 (&R)[8]) {
      const LAS u32x4* kt = (const LAS u32x4*)(smem + slot * PIECE) + lane;
#pragma unroll
      for (int i = 0; i < 8; ++i) R[i] = kt[i * 64];
      lds_st(lbase + L_FREE + slot * 4, gen);
    };
    auto k_scores = [&](const u32x4 (&R)[8], int t) {
      float sc[8];
#pragma unroll
      for (int i = 0; i < 8; ++i) {
        float a = 0.f, c = 0.f;
#pragma unroll
        for (int k = 0; k < 4; ++k) { a = dot2(R[i][k], q0[k], a); c = dot2(R[i][k], q1[k], c); }
        sc[i] = swap16_add(a, c);
      }
      const float s = row_transpose_sum8(sc, 0xccccccccccccccccull);
      const unsigned sb = pack_bf16(s, s);
      scw[t * 32] = (u16)sb;
      m = vmax(m, lo16(sb));
    };
    auto v_step = [&](int t) {
      LAS float* pw = (LAS float*)(smem + L_P) + wave * 32;
      const float p = __builtin_amdgcn_exp2f((lo16(scw[t * 32]) - m) * C2);
      pw[lane >> 1] = p;
      l += p;
      f32x4 pp[4];
#pragma unroll
      for (int k = 0; k < 4; ++k) pp[k] = *(const LAS f32x4*)(pw + (lane >> 5) * 16 + 4 * k);
      const LAS u32x4* vt = (const LAS u32x4*)(smem + slot * PIECE) + lane;
#pragma unroll
      for (int hf = 0; hf < 2; ++hf) {
        while (landed < 2 * pc + 1 + hf) refresh();
        u32x4 R[4];
#pragma unroll
        for (int i = 0; i < 4; ++i) R[i] = vt[(hf * 4 + i) * 64];
        if (hf == 1) lds_st(lbase + L_FREE + slot * 4, gen);
#pragma unroll
        for (int i = 0; i < 4; ++i)
#pragma unroll
          for (int k = 0; k < 4; ++k) { const f32x2 v = {lo16(R[i][k]), hi16(R[i][k])}; a0[k] += v * pp[hf][i]; a1[k] += v * pp[2 + hf][i]; }
        asm volatile("" : "+v"(a0[0]), "+v"(a0[1]), "+v"(a0[2]), "+v"(a0[3]), "+v"(a1[0]), "+v"(a1[1]), "+v"(a1[2]), "+v"(a1[3]));
      }
    };
    {
      u32x4 R0[8], R1[8], R2[8];
      while (landed < 2 * pc + 2) refresh();
      k_read(R0); advance(4);
      while (landed < 2 * pc + 2) refresh();
      k_read(R1); advance(4);
      while (landed < 2 * pc + 2) refresh();
      k_read(R2); advance(4);
      if (wave == 0) {
        u32x4 g0 = {0u, 0u, ~tag[0], 0u}, g1 = g0;
        const unsigned qo = (unsigned)(h * 2048 + lane * 32);
        unsigned spins = 0;
        for (;;) {
          const u64 miss = __ballot(!(g0[2] == tag[0] && g0[3] == tag[1] && g1[2] == tag[0] && g1[3] == tag[1]));
          if (miss == 0) break;
          if (++spins > SPIN_CAP) { bad = true; break; }
          repoll2(g0, g1, qo, qo + 16, ws + WS_Q, miss, miss);
        }
        *(LAS u32x4*)(smem + L_Q + lane * 16) = u32x4{g0[0], g0[1], g1[0], g1[1]};
        lds_st(lbase + L_QFLAG, 1u);
        DBGVAL(15, spins);
      } else {
        while (lds_ld(lbase + L_QFLAG) == 0) __builtin_amdgcn_s_sleep(1);
      }
      q0 = *(const LAS u32x4*)(smem + L_Q + (lane & 31) * 16); q1 = *(const LAS u32x4*)(smem + L_Q + 512 + (lane & 31) * 16);
      STAMP(5);
      k_scores(R0, 0); k_scores(R1, 1); k_scores(R2, 2);
      STAMP(6);
    }
    for (int t = 3; t < SSTAT / 2; ++t) {
      u32x4 R[8];
      while (landed < 2 * pc + 2) refresh();
      k_read(R); advance(4);
      k_scores(R, t);
    }
    m = head_max(m); macc = m;
    f32x2 y = {0.f, 0.f}, wof[32];
    unsigned xo = 0, bo = 0;
    const unsigned tc = (tid < 224 ? tid : 223) * 4u;
    if (has_mlp) {
      const int hg = role >> 2;
      u32x4 xv, wv[14]; unsigned b1w;
      asm volatile("global_load_dwordx4 %0, %1, %2" : "=v"(xv) : "v"((lane < 56 ? lane : 55) * 16u), "s"(x));
      asm volatile("global_load_ushort %0, %1, %2" : "=v"(b1w) : "v"(lane * 2u), "s"(b1 + hg * 64));
      const unsigned o = (unsigned)(tid >> 3) * (F * 2u) + (tid & 7) * 16u;
#pragma unroll
      for (int i = 0; i < 14; ++i) LD_NT(wv[i], o, w1 + hg * 128 + i * (32 * F * 2), 0);
      WAITV(14, xv); WAITV(14, b1w);
      layer_norm(xv, xn, lane);
#pragma unroll
      for (int i = 0; i < 14; ++i) WAITV(0, wv[i]);
      slice_partials(wv, xn, red + wave * 64, tid);
      arrive(L_HCNT);
      while (lds_ld(lbase + L_HCNT) < NC) __builtin_amdgcn_s_sleep(1);
      const float hv = rbf(rbf((red[lane] + red[64 + lane]) + (red[128 + lane] + red[192 + lane])) + lo16(b1w));
      const float u = 0.7978845608028654f * (hv + 0.044715f * hv * hv * hv);
      const float e = __builtin_amdgcn_exp2f(u * 2.885390081777927f);
      const float ge = rbf(hv * (1.f - __builtin_amdgcn_rcpf(e + 1.f)));
#pragma unroll
      for (int c = 0; c < 2; ++c) {
        unsigned w[32];
#pragma unroll
        for (int r = 0; r < 32; ++r) LD1_NT(w[r], tc, w2 + (size_t)(hg * 64 + c * 32 + (r & ~3)) * (D * 2), (r & 3) * (D * 2));
#pragma unroll
        for (int r = 0; r < 32; ++r) WAITV(0, w[r]);
#pragma unroll
        for (int r = 0; r < 32; ++r) y += f32x2{lo16(w[r]), hi16(w[r])} * lane_of(ge, c * 32 + r);
      }
    }
    if (own_o) {
      unsigned w[32];
#pragma unroll
      for (int r = 0; r < 32; ++r) LD1_NT(w[r], tc, wo + (size_t)(role * 32 + (r & ~3)) * (D * 2), (r & 3) * (D * 2));
#pragma unroll
      for (int r = 0; r < 32; ++r) WAITV(0, w[r]);
#pragma unroll
      for (int r = 0; r < 32; ++r) wof[r] = f32x2{lo16(w[r]), hi16(w[r])};
    } else {
#pragma unroll
      for (int r = 0; r < 32; ++r) wof[r] = f32x2{0.f, 0.f};
      asm volatile("global_load_dword %0, %2, %3\n\tglobal_load_dword %1, %2, %4\n\ts_waitcnt vmcnt(0)"
                   : "=&v"(xo), "=&v"(bo) : "v"((lane & 3) * 4u), "s"(x + (own_out ? role >> 1 : 0) * 8), "s"(b2 + (own_out ? role >> 1 : 0) * 8));
    }
#pragma unroll
    for (int r = 0; r < 32; ++r) asm volatile("" : "+v"(wof[r]));
    asm volatile("" : "+v"(y));
    const int nv = SSTAT / 2 - (wave >> 1);
    for (int t = 0; t < nv; ++t) { v_step(t); advance(4); }
    if (wave >= 2) { advance(2); v_step(SSTAT / 2 - 1); advance(2); } else { pc -= 2; slot -= 2; if (slot < 0) { slot += NSLOT; --gen; } }
    for (;;) {
      while (landed < 2 * pc + 2 && endq >= 2 * pc + 2) refresh();
      if (endq < 2 * pc + 2) break;
      u32x4 R[8];
      k_read(R); advance(6);
      k_scores(R, 0);
      m = head_max(m);
      const float alpha = __builtin_amdgcn_exp2f((macc - m) * C2);
      macc = m; l *= alpha;
      const float al0 = lane_of(alpha, 0), al1 = lane_of(alpha, 16);
#pragma unroll
      for (int k = 0; k < 4; ++k) { a0[k] *= al0; a1[k] *= al1; }
      v_step(0); advance(2);
    }
    STAMP(8);
    LAS float* rec = (LAS float*)(smem + L_REC);
    LAS f32x2* mlr = (LAS f32x2*)(smem + L_ML);
    {
      f32x4 o0, o1;
#pragma unroll
      for (int k = 0; k < 2; ++k) {
        o0[2 * k] = swap32_add(a0[k][0], a1[k][0]); o0[2 * k + 1] = swap32_add(a0[k][1], a1[k][1]);
        o1[2 * k] = swap32_add(a0[2 + k][0], a1[2 + k][0]); o1[2 * k + 1] = swap32_add(a0[2 + k][1], a1[2 + k][1]);
      }
      *(LAS f32x4*)(rec + wave * 512 + 8 * lane) = o0; *(LAS f32x4*)(rec + wave * 512 + 8 * lane + 4) = o1;
      l += dpp<ROR8>(l); l += dpp<HMIR>(l); l += dpp<XOR2>(l); l = swap32_add(l, l);
      if ((lane & 47) == 0) mlr[(lane >> 4) * NC + wave] = f32x2{m, l};
    }
    wg_barrier();
    const unsigned tag16 = tag[0] & 0xffffu;
    {
      const LAS f32x4* mp = (const LAS f32x4*)(mlr + (tid >> 7) * NC);
      const f32x4 m01 = mp[0], m23 = mp[1];
      const LAS f32x2* ap = (const LAS f32x2*)rec + tid;
      const f32x2 v0 = ap[0], v1 = ap[256], v2 = ap[512], v3 = ap[768];
      const float M = vmax(vmax(m01[0], m01[2]), vmax(m23[0], m23[2]));
      const float f0 = __builtin_amdgcn_exp2f((m01[0] - M) * C2), f1 = __builtin_amdgcn_exp2f((m01[2] - M) * C2);
      const float f2 = __builtin_amdgcn_exp2f((m23[0] - M) * C2), f3 = __builtin_amdgcn_exp2f((m23[2] - M) * C2);
      const f32x2 o = (v0 * f0 + v1 * f1) + (v2 * f2 + v3 * f3);
      const unsigned L = __float_as_uint((f0 * m01[1] + f1 * m01[3]) + (f2 * m23[1] + f3 * m23[3]));
      const unsigned pay = (lane & 2) ? L << 16 : ((lane & 1) ? L : __float_as_uint(M)) & 0xffff0000u;
      const u32x4 gd = {__float_as_uint(o[0]), __float_as_uint(o[1]), pay | tag16, tag[1]};
      ST4_SC1(o_pd, gd, ws + WS_PD);
    }
    STAMP(9);
    u32x4 g0, g1;
    if (own_o) {
      g0 = u32x4{0u, 0u, 0u, ~tag[1]}; g1 = g0;
      unsigned spins = 0;
      for (;;) {
        const u64 m0 = __ballot(!((g0[2] & 0xffffu) == tag16 && g0[3] == tag[1])), m1 = __ballot(!((g1[2] & 0xffffu) == tag16 && g1[3] == tag[1]));
        if ((m0 | m1) == 0) break;
        if (++spins > SPIN_CAP) { bad = true; break; }
        repoll2(g0, g1, o_g0, o_g1, gbase, m0, m1);
      }
      STAMP(10);
      const float ma = hi16(dppu<0x00>(g0[2])), mb = hi16(dppu<0x00>(g1[2]));
      const float Mw = rows_max(vmax(ma, mb));
      const float fa = __builtin_amdgcn_exp2f((ma - Mw) * C2), fb = __builtin_amdgcn_exp2f((mb - Mw) * C2);
      const float la = __uint_as_float((dppu<0x55>(g0[2]) & 0xffff0000u) | (dppu<0xAA>(g0[2]) >> 16));
      const float lb = __uint_as_float((dppu<0x55>(g1[2]) & 0xffff0000u) | (dppu<0xAA>(g1[2]) >> 16));
      const float oe = fa * __uint_as_float(g0[0]) + fb * __uint_as_float(g1[0]), oo = fa * __uint_as_float(g0[1]) + fb * __uint_as_float(g1[1]);
      const float Lp = fa * la + fb * lb;
      const float t = swap32_add(swap16_add(oe, oo), swap16_add(Lp, Lp));
      LAS float* redo = (LAS float*)(smem + L_REDO);
      LAS float* ml2 = (LAS float*)(smem + L_ML2);
      if (lane < 32) redo[(2 * (lane & 15) + (lane >> 4)) * NC + wave] = t;
      if (lane == 32) { ml2[wave] = Mw; ml2[NC + wave] = t; }
      wg_barrier();
      const f32x4 ov = ((const LAS f32x4*)redo)[lane & 31], Mv = ((const LAS f32x4*)ml2)[0], Lv = ((const LAS f32x4*)ml2)[1];
      const float M = vmax(vmax(Mv[0], Mv[1]), vmax(Mv[2], Mv[3]));
      f32x4 fv;
#pragma unroll
      for (int w = 0; w < NC; ++w) fv[w] = __builtin_amdgcn_exp2f((Mv[w] - M) * C2);
      const f32x4 on4 = ov * fv, L4 = Lv * fv;
      const float on = rbf(((on4[0] + on4[1]) + (on4[2] + on4[3])) * __builtin_amdgcn_rcpf((L4[0] + L4[1]) + (L4[2] + L4[3])));
      f32x2 y1 = {0.f, 0.f}, y2 = y1, y3 = y1;
#pragma unroll
      for (int r = 0; r < 32; r += 4) {
        y += wof[r] * lane_of(on, r); y1 += wof[r + 1] * lane_of(on, r + 1); y2 += wof[r + 2] * lane_of(on, r + 2); y3 += wof[r + 3] * lane_of(on, r + 3);
      }
      y = (y + y1) + (y2 + y3);
      if (bad) y[0] = __uint_as_float(0x7fc00000u);
      if (tid < 224) {
        const u32x4 gy = {__float_as_uint(y[0]), __float_as_uint(y[1]), tag[0], tag[1]};
        ST4_SC1(o_y, gy, ws + WS_Y);
      }
      STAMP(11);
    } else if (own_out) {
      g0 = u32x4{0u, 0u, ~tag[0], 0u}; g1 = g0;
      unsigned spins = 0;
      for (;;) {
        const u64 m0 = __ballot(!(g0[2] == tag[0] && g0[3] == tag[1])), m1 = __ballot(!(g1[2] == tag[0] && g1[3] == tag[1]));
        if ((m0 | m1) == 0) break;
        if (++spins > SPIN_CAP) { bad = true; break; }
        const u64 gate = (spins == 1 || (m0 & m1) != ~0ull) ? ~0ull : 1ull;
        repoll2(g0, g1, o_g0, o_g1, gbase, m0 & gate, m1 & gate);
      }
      STAMP(12);
      float s0 = __uint_as_float(g0[0]) + __uint_as_float(g1[0]), s1 = __uint_as_float(g0[1]) + __uint_as_float(g1[1]);
      if (bad) s0 = __uint_as_float(0x7fc00000u);
      s0 += dpp<ROR8>(s0); s1 += dpp<ROR8>(s1); s0 += dpp<ROR4>(s0); s1 += dpp<ROR4>(s1);
      float ss = swap16_add(s0, s1); ss = swap32_add(ss, ss);
      LAS float* redy = (LAS float*)(smem + L_REDO);
      if ((lane & 44) == 0) redy[wave * 8 + 2 * (lane & 3) + (lane >> 4)] = ss;
      wg_barrier();
      if (tid < 4) {
        const LAS f32x2* ry = (const LAS f32x2*)redy + tid;
        const f32x2 t = (ry[0] + ry[4]) + (ry[8] + ry[12]);
        out[(role >> 1) * 4 + tid] = pack_bf16(lo16(xo) + t[0] + lo16(bo), hi16(xo) + t[1] + hi16(bo));
        if (role == 1 && tid == 0) *(u32x2*)(ws + WS_E) = tag;
      }
      STAMP(13);
    }
  }
  { unsigned xcc = (unsigned)__builtin_amdgcn_s_getreg(20 | (3 << 11)) & 0xfu; unsigned hw = (unsigned)__builtin_amdgcn_s_getreg(63492);
    stamps = (lane == 14) ? (xcc << 16 | ((hw >> 8) & 0xffu)) : stamps;
    if (lane < 16) ((unsigned*)(ws + WS_DBG))[(b * 5 + wave) * 16 + lane] = stamps; }
}

__global__ void lead_kernel(unsigned* st) { if (threadIdx.x == 0) st[blockIdx.x] = (unsigned)__builtin_amdgcn_s_memrealtime(); }
__global__ void probe_kernel(const unsigned* st, const unsigned* lead) {
  __shared__ unsigned sm[4];
  unsigned t0 = (unsigned)__builtin_amdgcn_s_memrealtime();
  if (threadIdx.x == 0) {
    unsigned x;
    if (0 == 0) { unsigned mn = 0xffffffffu; for (int w = 0; w < 256 * 5; ++w) { unsigned v = st[w * 16 + 13]; if (v) mn = min(mn, t0 - v); } x = mn; }
    else { unsigned mn = 0xffffffffu; for (int w = 0; w < 256 * 5; ++w) mn = min(mn, st[w * 16] - lead[0]); x = mn; }
    sm[0] = x;
  }
  __syncthreads();
  const unsigned d = 30000u + min(sm[0], 20000u) * 20u;
  while ((unsigned)__builtin_amdgcn_s_memrealtime() - t0 < d) __builtin_amdgcn_s_sleep(4);
}

extern "C" void kernel_launch(void* const* d_in, const int* in_sizes, int n_in, void* d_out, int out_size, void* d_ws, size_t ws_size, hipStream_t stream) {
  lead_kernel<<<1, 64, 0, stream>>>((unsigned*)((char*)d_ws + (8u << 20)));
  fused_kernel<<<NWG, 320, 0, stream>>>((const u16*)d_in[0], (const char*)d_in[1], (const char*)d_in[2], (const char*)d_in[3], (const char*)d_in[4],
                                        (const char*)d_in[6], (const u16*)d_in[7], (const char*)d_in[8], (const u16*)d_in[9], (unsigned*)d_out, (char*)d_ws);
  probe_kernel<<<1, 64, 0, stream>>>((const unsigned*)((char*)d_ws + WS_DBG), (const unsigned*)((char*)d_ws + (8u << 20)));
}
```

```cpp
#include <hip/hip_runtime.h>
#include <hip/hip_bf16.h>
#include <cstdint>
#include <type_traits>

typedef unsigned u32x4 __attribute__((ext_vector_type(4)));
typedef unsigned u32x2 __attribute__((ext_vector_type(2)));
typedef float f32x4 __attribute__((ext_vector_type(4)));
typedef float f32x2 __attribute__((ext_vector_type(2)));
typedef __bf16 bf16x2 __attribute__((ext_vector_type(2)));
typedef unsigned short u16;
typedef unsigned long long u64;
#define LAS __attribute__((address_space(3)))

#define DBG 0
#define INFL 28
#define SSTAT 56

constexpr int D = 448, F = 1792, DH = 256;
constexpr int NWG = 256, NC = 4;
constexpr int PIECE = 8192, NSLOT = 18;
constexpr int RPR = 64;
constexpr int TAILR = RPR - SSTAT, POOL = 8 * TAILR;
static_assert(POOL >= 24 && INFL % 4 == 0 && INFL >= 20 && INFL <= 32 && SSTAT % 2 == 0 && SSTAT >= 8 && (SSTAT / 2 + 1) * 64 <= 2048, "loader structure");
constexpr unsigned PD_STRIDE = 33 * 128, Y_STRIDE = 29 * 128;
constexpr size_t WS_E = 0, WS_CTR = 4096, WS_Q = 16384, WS_PD = 65536, WS_Y = WS_PD + (size_t)NWG * PD_STRIDE, WS_DBG = 4u << 20;
constexpr unsigned SPIN_CAP = 1u << 22;
constexpr unsigned L_CTL = NSLOT * PIECE;
constexpr unsigned L_LANDED = L_CTL, L_QFLAG = L_CTL + 8, L_QCNT = L_CTL + 12, L_HCNT = L_CTL + 16, L_FREE = L_CTL + 32;
constexpr unsigned L_Q = L_CTL + 128, L_RED = L_Q + 1024, L_REDO = L_RED + 1024, L_ML2 = L_REDO + 512, L_P = L_ML2 + 64;
constexpr unsigned L_ML = L_P + NC * 128, L_XN = L_ML + 64, L_REC = L_XN + 2048, L_END = L_REC + NC * 2 * DH * 4;
static_assert(L_END <= 160 * 1024 && D <= 2 * DH, "LDS");
constexpr float C2 = 0.0625f * 1.4426950408889634f;

__device__ __forceinline__ float lo16(unsigned w) { return __uint_as_float(w << 16); }
__device__ __forceinline__ float hi16(unsigned w) { return __uint_as_float(w & 0xffff0000u); }
__device__ __forceinline__ unsigned pack_bf16(float a, float b) {
  bf16x2 r = {(__bf16)a, (__bf16)b};
  return __builtin_bit_cast(unsigned, r);
}
__device__ __forceinline__ float rbf(float x) { return lo16(pack_bf16(x, x)); }
__device__ __forceinline__ float dot2(unsigned k, unsigned q, float acc) {
  return __builtin_amdgcn_fdot2_f32_bf16(__builtin_bit_cast(bf16x2, k), __builtin_bit_cast(bf16x2, q), acc, false);
}
template <int CTRL> __device__ __forceinline__ unsigned dppu(unsigned x) { return (unsigned)__builtin_amdgcn_mov_dpp((int)x, CTRL, 0xf, 0xf, true); }
template <int CTRL> __device__ __forceinline__ float dpp(float x) { return __uint_as_float(dppu<CTRL>(__float_as_uint(x))); }
constexpr int XOR1 = 0xB1, XOR2 = 0x4E, HMIR = 0x141, MIR = 0x140, ROR8 = 0x128, ROR4 = 0x124;
__device__ __forceinline__ float swap16_add(float a, float b) {
  auto r = __builtin_amdgcn_permlane16_swap(__float_as_uint(a), __float_as_uint(b), false, false);
  return __uint_as_float(r[0]) + __uint_as_float(r[1]);
}
__device__ __forceinline__ float swap32_add(float a, float b) {
  auto r = __builtin_amdgcn_permlane32_swap(__float_as_uint(a), __float_as_uint(b), false, false);
  return __uint_as_float(r[0]) + __uint_as_float(r[1]);
}
__device__ __forceinline__ float row_sum16(float x) { x += dpp<XOR1>(x); x += dpp<XOR2>(x); x += dpp<HMIR>(x); x += dpp<MIR>(x); return x; }
__device__ __forceinline__ float wave_sum(float x) { x = row_sum16(x); x = swap16_add(x, x); return swap32_add(x, x); }
__device__ __forceinline__ float lane_of(float x, int l) { return __builtin_bit_cast(float, __builtin_amdgcn_readlane(__builtin_bit_cast(int, x), l)); }
__device__ __forceinline__ float row_transpose_sum8(const float (&v)[8], u64 bit1) {
  float t0, t1, t2, t3, r;
  asm volatile("s_nop 1\n\t"
      "v_add_f32_dpp %0, %5, %5 row_ror:8 row_mask:0xf bank_mask:0xf\n\tv_add_f32_dpp %1, %6, %6 row_ror:8 row_mask:0xf bank_mask:0xf\n\t"
      "v_add_f32_dpp %2, %7, %7 row_ror:8 row_mask:0xf bank_mask:0xf\n\tv_add_f32_dpp %3, %8, %8 row_ror:8 row_mask:0xf bank_mask:0xf\n\t"
      "v_add_f32_dpp %0, %9, %9 row_ror:8 row_mask:0xf bank_mask:0xc\n\tv_add_f32_dpp %1, %10, %10 row_ror:8 row_mask:0xf bank_mask:0xc\n\t"
      "v_add_f32_dpp %2, %11, %11 row_ror:8 row_mask:0xf bank_mask:0xc\n\tv_add_f32_dpp %3, %12, %12 row_ror:8 row_mask:0xf bank_mask:0xc\n\t"
      "v_add_f32_dpp %0, %0, %0 row_half_mirror row_mask:0xf bank_mask:0x5\n\tv_add_f32_dpp %1, %1, %1 row_half_mirror row_mask:0xf bank_mask:0x5\n\t"
      "v_add_f32_dpp %0, %2, %2 row_half_mirror row_mask:0xf bank_mask:0xa\n\tv_add_f32_dpp %1, %3, %3 row_half_mirror row_mask:0xf bank_mask:0xa\n\t"
      "s_nop 1\n\t"
      "v_add_f32_dpp %2, %0, %0 quad_perm:[2,3,0,1] row_mask:0xf bank_mask:0xf\n\tv_add_f32_dpp %3, %1, %1 quad_perm:[2,3,0,1] row_mask:0xf bank_mask:0xf\n\t"
      "v_cndmask_b32 %2, %2, %3, %13\n\ts_nop 1\n\t"
      "v_add_f32_dpp %4, %2, %2 quad_perm:[1,0,3,2] row_mask:0xf bank_mask:0xf"
      : "=&v"(t0), "=&v"(t1), "=&v"(t2), "=&v"(t3), "=&v"(r)
      : "v"(v[0]), "v"(v[1]), "v"(v[2]), "v"(v[3]), "v"(v[4]), "v"(v[5]), "v"(v[6]), "v"(v[7]), "s"(bit1));
  return r;
}
__device__ __forceinline__ float head_max(float x) {
  float y;
  asm volatile("s_nop 1\n\tv_max_f32_dpp %0, %0, %0 row_ror:8 row_mask:0xf bank_mask:0xf\n\ts_nop 1\n\tv_max_f32_dpp %0, %0, %0 row_half_mirror row_mask:0xf bank_mask:0xf\n\t"
               "s_nop 1\n\tv_max_f32_dpp %0, %0, %0 quad_perm:[2,3,0,1] row_mask:0xf bank_mask:0xf\n\tv_mov_b32 %1, %0\n\ts_nop 1\n\tv_permlane32_swap_b32 %0, %1\n\tv_max_f32 %0, %0, %1"
               : "+v"(x), "=&v"(y));
  return x;
}
__device__ __forceinline__ float rows_max(float x) {
  float y;
  asm volatile("v_mov_b32 %1, %0\n\ts_nop 1\n\tv_permlane16_swap_b32 %0, %1\n\tv_max_f32 %0, %0, %1\n\tv_mov_b32 %1, %0\n\ts_nop 1\n\tv_permlane32_swap_b32 %0, %1\n\tv_max_f32 %0, %0, %1"
               : "+v"(x), "=&v"(y));
  return x;
}
__device__ __forceinline__ float vmax(float a, float b) { float r; asm("v_max_f32 %0, %1, %2" : "=v"(r) : "v"(a), "v"(b)); return r; }
__device__ __forceinline__ void layer_norm(const u32x4& xv, LAS float* xn, int lane) {
  float xf[8];
#pragma unroll
  for (int k = 0; k < 4; ++k) { xf[2 * k] = lo16(xv[k]); xf[2 * k + 1] = hi16(xv[k]); }
  const float live = lane < 56 ? 1.f : 0.f;
  const float mean = wave_sum((((xf[0] + xf[1]) + (xf[2] + xf[3])) + ((xf[4] + xf[5]) + (xf[6] + xf[7]))) * live) * (1.f / D);
  float ss = 0.f;
#pragma unroll
  for (int k = 0; k < 8; ++k) { const float d = xf[k] - mean; ss += d * d; }
  const float var = wave_sum(ss * live) * (1.f / D);
  const float rden = __builtin_amdgcn_rcpf(rbf(__builtin_amdgcn_sqrtf(rbf(rbf(var) + 1e-5f)))) * live, mb = rbf(mean);
#pragma unroll
  for (int k = 0; k < 8; k += 2) {
    const unsigned c = pack_bf16(xf[k] - mb, xf[k + 1] - mb), n = pack_bf16(lo16(c) * rden, hi16(c) * rden);
    xf[k] = lo16(n); xf[k + 1] = hi16(n);
  }
  *(LAS f32x4*)(xn + 8 * lane) = f32x4{xf[0], xf[1], xf[2], xf[3]}; *(LAS f32x4*)(xn + 8 * lane + 4) = f32x4{xf[4], xf[5], xf[6], xf[7]};
}
__device__ __forceinline__ void slice_partials(const u32x4 (&w)[14], const LAS float* xn, LAS float* red, int tid) {
  f32x2 v[4] = {{0.f, 0.f}, {0.f, 0.f}, {0.f, 0.f}, {0.f, 0.f}};
#pragma unroll
  for (int i = 0; i < 14; ++i) {
    const float xr = xn[(tid >> 3) + 32 * i];
#pragma unroll
    for (int k = 0; k < 4; ++k) v[k] += f32x2{lo16(w[i][k]), hi16(w[i][k])} * xr;
  }
  const float a0 = swap32_add(v[0][0], v[2][0]), a1 = swap32_add(v[0][1], v[2][1]), a2 = swap32_add(v[1][0], v[3][0]), a3 = swap32_add(v[1][1], v[3][1]);
  const float b0 = swap16_add(a0, a2), b1 = swap16_add(a1, a3);
  const float t1 = b0 + dpp<ROR8>(b0), t2 = b1 + dpp<ROR8>(b1);
  red[(tid & 7) * 8 + ((tid >> 3) & 7)] = (tid & 8) ? t2 : t1;
}

#define LD_NT(dst, voff, sbase, imm) asm volatile("global_load_dwordx4 %0, %1, %2 offset:%3 nt" : "=v"(dst) : "v"(voff), "s"(sbase), "i"(imm))
#define LD1_NT(dst, voff, sbase, imm) asm volatile("global_load_dword %0, %1, %2 offset:%3 nt" : "=v"(dst) : "v"(voff), "s"(sbase), "i"(imm))
#define LD4_SC1(dst, voff, sbase, imm) asm volatile("global_load_dwordx4 %0, %1, %2 offset:%3 sc1" : "=v"(dst) : "v"(voff), "s"(sbase), "i"(imm) : "memory")
#define ST4_SC1(voff, val, sbase) asm volatile("global_store_dwordx4 %0, %1, %2 sc1\n\ts_nop 1" :: "v"(voff), "v"(val), "s"(sbase) : "memory")
#define WAITV(n, reg) asm volatile("s_waitcnt vmcnt(%1)" : "+v"(reg) : "i"(n))
#define VMCNT(n) asm volatile("s_waitcnt vmcnt(%0)" :: "i"(n) : "memory")
__device__ __forceinline__ void repoll2(u32x4& g0, u32x4& g1, unsigned o0, unsigned o1, const char* base, u64 m0, u64 m1) {
  asm volatile("s_mov_b64 exec, %5\n\tglobal_load_dwordx4 %0, %2, %4 sc1\n\ts_mov_b64 exec, %6\n\tglobal_load_dwordx4 %1, %3, %4 sc1\n\ts_mov_b64 exec, -1\n\ts_waitcnt vmcnt(0)"
               : "+v"(g0), "+v"(g1) : "v"(o0), "v"(o1), "s"(base), "s"(m0), "s"(m1) : "memory");
}
__device__ __forceinline__ void wg_barrier() { asm volatile("s_waitcnt lgkmcnt(0)\n\ts_barrier" ::: "memory"); }
__device__ __forceinline__ void lds_st(unsigned addr, unsigned v) { asm volatile("ds_write_b32 %0, %1" :: "v"(addr), "v"(v) : "memory"); }
__device__ __forceinline__ unsigned lds_ld(unsigned addr) {
  unsigned v; asm volatile("ds_read_b32 %0, %1\n\ts_waitcnt lgkmcnt(0)" : "=v"(v) : "v"(addr) : "memory"); return __builtin_amdgcn_readfirstlane(v); }
template <int N> __device__ __forceinline__ void dma(const char* g, unsigned voff, unsigned lds) {
  if constexpr (N == 4)
    asm volatile("s_mov_b32 m0, %2\n\ts_nop 0\n\tglobal_load_lds_dwordx4 %0, %1 nt\n\tglobal_load_lds_dwordx4 %0, %1 offset:1024 nt\n\t"
                 "global_load_lds_dwordx4 %0, %1 offset:2048 nt\n\tglobal_load_lds_dwordx4 %0, %1 offset:3072 nt" :: "v"(voff), "s"(g), "s"(lds) : "memory");
  else
    asm volatile("s_mov_b32 m0, %2\n\ts_nop 0\n\tglobal_load_lds_dwordx4 %0, %1 nt\n\tglobal_load_lds_dwordx4 %0, %1 offset:1024 nt" :: "v"(voff), "s"(g), "s"(lds) : "memory");
}

#define STAMP(i) do {} while (0)
#define DBGVAL(i, v) do {} while (0)

__global__ __launch_bounds__(320) void fused_kernel(const u16* __restrict__ x, const char* __restrict__ kc, const char* __restrict__ vc,
                                                    const char* __restrict__ wq, const char* __restrict__ wo, const char* __restrict__ w1,
                                                    const u16* __restrict__ b1, const char* __restrict__ w2, const u16* __restrict__ b2,
                                                    unsigned* __restrict__ out, char* __restrict__ ws) {
  __shared__ __attribute__((aligned(1024))) char smem[L_END];
  const unsigned lbase = (unsigned)(uintptr_t)(LAS char*)smem;
  const int b = blockIdx.x, tid = threadIdx.x, lane = tid & 63, wave = __builtin_amdgcn_readfirstlane(tid >> 6);
  const int role = b >> 1;
  const bool own_o = ((b ^ (b >> 3)) & 1) == 0, has_mlp = own_o && (role & 3) == 0 && role < 112;
  const bool make_q = !own_o && (role & 1) == 0, own_out = !own_o && (role & 1) == 1 && role < 112;
  STAMP(0);
  if (wave == NC) {
    __builtin_amdgcn_s_setprio(3);
    if (lane < 8 + NSLOT) lds_st(lbase + L_CTL + lane * 4, lane == 1 ? 0xffffffffu : 0u);
    const unsigned voff = lane * 16;
    const int q = b >> 3;
    unsigned* ctr = (unsigned*)(ws + WS_CTR) + q * 64;
    int nq = 0, slot = 0, gen = 0;
    constexpr int LAG = INFL / 4 - 1;
    int frs = 0;
    auto put = [&](const char* g, int u) {
      if ((u & 1) == 0 && frs < gen) { while ((int)lds_ld(lbase + L_FREE + slot * 4) < gen) __builtin_amdgcn_s_sleep(1); }
      dma<4>(g, voff, lbase + slot * PIECE + (u & 1) * 4096);
      ++nq;
      if (u & 1) { if (++slot == NSLOT) { slot = 0; ++gen; } }
    };
    auto settle = [&](int u) {
      if (u & 1) {
        unsigned fr;
        asm volatile("ds_read_b32 %0, %1\n\ts_waitcnt vmcnt(%2) lgkmcnt(0)" : "=&v"(fr) : "v"(lbase + L_FREE + slot * 4), "i"(INFL - 4) : "memory");
        frs = __builtin_amdgcn_readfirstlane(fr);
      } else VMCNT(INFL - 4);
      lds_st(lbase + L_LANDED, max(nq - LAG, 0));
    };
    unsigned tv = 0;
    auto ticket = [&]() { asm volatile("s_mov_b64 exec, 1\n\tglobal_atomic_add %0, %1, %2, %3 sc0\n\ts_mov_b64 exec, -1" : "=v"(tv) : "v"(0u), "v"(1u), "s"(ctr) : "memory"); };
    size_t voffs = (size_t)b * (1u << 20);
    {
      const char* g = kc + voffs;
      put(g, 0); put(g + 4096, 1);
      wg_barrier();
      for (int p = 1; p < 2 * SSTAT; ++p) { g += PIECE; put(g, 0); settle(0); put(g + 4096, 1); settle(1);
      }
      STAMP(5);
      g = vc + voffs;
      for (int p = 0; p < 2 * SSTAT - 6; ++p) { put(g, 0); settle(0); put(g + 4096, 1); settle(1); g += PIECE; }
      while (lds_ld(lbase + L_QFLAG) == 0) __builtin_amdgcn_s_sleep(1);
#pragma unroll
      for (int u = 0; u < 8; ++u) { if (u == 8 - INFL / 4) ticket(); put(g + u * 4096, u); settle(u); }
      STAMP(6);
      voffs += (size_t)(SSTAT - 1) * (2 * PIECE);
    }
    for (;;) {
      asm volatile("" : "+v"(tv));
      const int k = __builtin_amdgcn_readfirstlane(tv);
      constexpr int A = POOL > 24 ? POOL - 24 : 0, N4 = (A + 3) / 4;
      int cur, sz;
      if (k < N4) { cur = 4 * k; sz = min(4, A - cur); } else if (k < N4 + 8) { cur = A + 2 * (k - N4); sz = 2; } else { cur = A + 16 + (k - N4 - 8); sz = 1; }
      if (cur >= POOL) break;
      for (; sz > 0; --sz, ++cur) {
        const size_t off = (size_t)(8 * q + cur / TAILR) * (1u << 20) + (size_t)(SSTAT + cur % TAILR) * (2 * PIECE);
        const char* kp = kc + off; const char* vp = vc + voffs;
#pragma unroll
        for (int u = 0; u < 8; ++u) {
          if (u == 8 - INFL / 4 && sz == 1) ticket();
          put(u < 4 ? kp + u * 4096 : vp + (u - 4) * 4096, u);
          settle(u);
        }
        voffs = off;
      }
    }
    lds_st(lbase + L_LANDED + 4, nq);
    STAMP(1); DBGVAL(15, nq + 4);
    slot += 2; if (slot >= NSLOT) { slot -= NSLOT; ++gen; }
    frs = 0;
#pragma unroll
    for (int u = 0; u < 4; ++u) { put(vc + voffs + u * 4096, u); settle(u); }
#pragma unroll
    for (int i = LAG - 1; i >= 0; --i) { VMCNT(4 * i); lds_st(lbase + L_LANDED, nq - i + (i < 4 ? 4 : 0)); }
    STAMP(2);
  } else {
    const int h = b >> 5;
    LAS float* xn = (LAS float*)(smem + L_XN);
    LAS float* red = (LAS float*)(smem + L_RED);
    u32x2 ev, tag;
    auto arrive = [&](unsigned cnt) -> int {
      unsigned arr;
      asm volatile("s_mov_b64 exec, 1\n\tds_add_rtn_u32 %0, %1, %2\n\ts_mov_b64 exec, -1\n\ts_waitcnt lgkmcnt(0)" : "=&v"(arr) : "v"(lbase + cnt), "v"(1u) : "memory");
      return __builtin_amdgcn_readfirstlane(arr);
    };
    auto make_tag = [&]() {
      const u64 e = (u64)(unsigned)__builtin_amdgcn_readfirstlane(ev[1]) << 32 | (unsigned)__builtin_amdgcn_readfirstlane(ev[0]);
      const u64 z = e * 6364136223846793005ull + 1442695040888963407ull;
      tag = u32x2{(unsigned)z, (unsigned)(z >> 32)};
    };
    if (make_q) {
      const int qi = role >> 1;
      if (wave == 0 && (qi & 4) == 0) asm volatile("global_store_dword %0, %1, %2 sc1" :: "v"(0u), "v"(0u), "s"(ws + WS_CTR + ((qi >> 3) * 4 + (qi & 3)) * 256) : "memory");
      u32x4 xv, wv[14];
      asm volatile("global_load_dwordx4 %0, %1, %2" : "=v"(xv) : "v"((lane < 56 ? lane : 55) * 16u), "s"(x));
      const unsigned o = (unsigned)(tid >> 3) * 8192u + (tid & 7) * 16u;
#pragma unroll
      for (int i = 0; i < 14; ++i) LD_NT(wv[i], o, wq + qi * 128 + i * (32 * 8192), 0);
      asm volatile("global_load_dwordx2 %0, %1, %2 sc1" : "=v"(ev) : "v"(0u), "s"(ws + WS_E) : "memory");
      STAMP(4);
      wg_barrier();
      WAITV(15, xv);
      STAMP(7);
      layer_norm(xv, xn, lane);
      STAMP(1);
#pragma unroll
      for (int i = 0; i < 14; ++i) WAITV(1, wv[i]);
      STAMP(2);
      slice_partials(wv, xn, red + wave * 64, tid);
      WAITV(0, ev);
      make_tag();
      if (arrive(L_QCNT) == NC - 1 && lane < 16) {
        const LAS f32x4* rq = (const LAS f32x4*)red + lane;
        const f32x4 s = (rq[0] + rq[16]) + (rq[32] + rq[48]);
        const u32x4 g = {pack_bf16(s[0], s[1]), pack_bf16(s[2], s[3]), tag[0], tag[1]};
        ST4_SC1((unsigned)(qi * 256 + lane * 16), g, ws + WS_Q);
      }
      STAMP(3);
    } else {
      asm volatile("global_load_dwordx2 %0, %1, %2 sc1" : "=v"(ev) : "v"(0u), "s"(ws + WS_E) : "memory");
      wg_barrier();
      WAITV(0, ev);
      make_tag();
    }
    bool bad = false;
    unsigned o_pd = (unsigned)(b * PD_STRIDE + tid * 16), o_y = (unsigned)(role * Y_STRIDE + (tid < 224 ? tid : 0) * 16);
    unsigned o_g0 = own_o ? (unsigned)((h * 32 + (tid >> 4)) * PD_STRIDE + (role & 15) * 256 + (tid & 15) * 16)
                          : (unsigned)((tid >> 2) * Y_STRIDE + (own_out ? role >> 1 : 0) * 64 + (tid & 3) * 16);
    unsigned o_g1 = o_g0 + (own_o ? 16 * PD_STRIDE : 64 * Y_STRIDE);
    asm volatile("" : "+v"(o_pd), "+v"(o_y), "+v"(o_g0), "+v"(o_g1));
    const char* gbase = ws + (own_o ? WS_PD : WS_Y);
    float m = -INFINITY, macc = -INFINITY, l = 0.f;
    f32x2 a0[4], a1[4];
#pragma unroll
    for (int k = 0; k < 4; ++k) { a0[k] = f32x2{0.f, 0.f}; a1[k] = f32x2{0.f, 0.f}; }
    unsigned landed = 0, endq = 0xffffffffu;
    unsigned pc = wave; int slot = wave, gen = 1;
    u32x4 q0, q1;
    LAS u16* scw = (LAS u16*)(smem + L_REC + wave * 2048) + (lane >> 1);
    auto refresh = [&]() {
      u32x2 c; asm volatile("ds_read_b64 %0, %1\n\ts_waitcnt lgkmcnt(0)" : "=v"(c) : "v"(lbase + L_LANDED) : "memory");
      landed = __builtin_amdgcn_readfirstlane(c[0]); endq = __builtin_amdgcn_readfirstlane(c[1]);
    };
    auto advance = [&](int n) { pc += n; slot += n; if (slot >= NSLOT) { slot -= NSLOT; ++gen; } };
    auto k_read = [&](u32x4 (&R)[8]) {
      const LAS u32x4* kt = (const LAS u32x4*)(smem + slot * PIECE) + lane;
#pragma unroll
      for (int i = 0; i < 8; ++i) R[i] = kt[i * 64];
      lds_st(lbase + L_FREE + slot * 4, gen);
    };
    auto k_scores = [&](const u32x4 (&R)[8], int t) {
      float sc[8];
#pragma unroll
      for (int i = 0; i < 8; ++i) {
        float a = 0.f, c = 0.f;
#pragma unroll
        for (int k = 0; k < 4; ++k) { a = dot2(R[i][k], q0[k], a); c = dot2(R[i][k], q1[k], c); }
        sc[i] = swap16_add(a, c);
      }
      const float s = row_transpose_sum8(sc, 0xccccccccccccccccull);
      const unsigned sb = pack_bf16(s, s);
      scw[t * 32] = (u16)sb;
      m = vmax(m, lo16(sb));
    };
    auto v_step = [&](int t) {
      LAS float* pw = (LAS float*)(smem + L_P) + wave * 32;
      const float p = __builtin_amdgcn_exp2f((lo16(scw[t * 32]) - m) * C2);
      pw[lane >> 1] = p;
      l += p;
      f32x4 pp[4];
#pragma unroll
      for (int k = 0; k < 4; ++k) pp[k] = *(const LAS f32x4*)(pw + (lane >> 5) * 16 + 4 * k);
      const LAS u32x4* vt = (const LAS u32x4*)(smem + slot * PIECE) + lane;
#pragma unroll
      for (int hf = 0; hf < 2; ++hf) {
        while (landed < 2 * pc + 1 + hf) refresh();
        u32x4 R[4];
#pragma unroll
        for (int i = 0; i < 4; ++i) R[i] = vt[(hf * 4 + i) * 64];
        if (hf == 1) lds_st(lbase + L_FREE + slot * 4, gen);
#pragma unroll
        for (int i = 0; i < 4; ++i)
#pragma unroll
          for (int k = 0; k < 4; ++k) { const f32x2 v = {lo16(R[i][k]), hi16(R[i][k])}; a0[k] += v * pp[hf][i]; a1[k] += v * pp[2 + hf][i]; }
        asm volatile("" : "+v"(a0[0]), "+v"(a0[1]), "+v"(a0[2]), "+v"(a0[3]), "+v"(a1[0]), "+v"(a1[1]), "+v"(a1[2]), "+v"(a1[3]));
      }
    };
    {
      u32x4 R0[8], R1[8], R2[8];
      while (landed < 2 * pc + 2) refresh();
      k_read(R0); advance(4);
      while (landed < 2 * pc + 2) refresh();
      k_read(R1); advance(4);
      while (landed < 2 * pc + 2) refresh();
      k_read(R2); advance(4);
      if (wave == 0) {
        u32x4 g0 = {0u, 0u, ~tag[0], 0u}, g1 = g0;
        const unsigned qo = (unsigned)(h * 2048 + lane * 32);
        unsigned spins = 0;
        for (;;) {
          const u64 miss = __ballot(!(g0[2] == tag[0] && g0[3] == tag[1] && g1[2] == tag[0] && g1[3] == tag[1]));
          if (miss == 0) break;
          if (++spins > SPIN_CAP) { bad = true; break; }
          repoll2(g0, g1, qo, qo + 16, ws + WS_Q, miss, miss);
        }
        *(LAS u32x4*)(smem + L_Q + lane * 16) = u32x4{g0[0], g0[1], g1[0], g1[1]};
        lds_st(lbase + L_QFLAG, 1u);
        DBGVAL(15, spins);
      } else {
        while (lds_ld(lbase + L_QFLAG) == 0) __builtin_amdgcn_s_sleep(1);
      }
      q0 = *(const LAS u32x4*)(smem + L_Q + (lane & 31) * 16); q1 = *(const LAS u32x4*)(smem + L_Q + 512 + (lane & 31) * 16);
      STAMP(5);
      k_scores(R0, 0); k_scores(R1, 1); k_scores(R2, 2);
      STAMP(6);
    }
    for (int t = 3; t < SSTAT / 2; ++t) {
      u32x4 R[8];
      while (landed < 2 * pc + 2) refresh();
      k_read(R); advance(4);
      k_scores(R, t);
    }
    m = head_max(m); macc = m;
    f32x2 y = {0.f, 0.f}, wof[32];
    unsigned xo = 0, bo = 0;
    const unsigned tc = (tid < 224 ? tid : 223) * 4u;
    if (has_mlp) {
      const int hg = role >> 2;
      u32x4 xv, wv[14]; unsigned b1w;
      asm volatile("global_load_dwordx4 %0, %1, %2" : "=v"(xv) : "v"((lane < 56 ? lane : 55) * 16u), "s"(x));
      asm volatile("global_load_ushort %0, %1, %2" : "=v"(b1w) : "v"(lane * 2u), "s"(b1 + hg * 64));
      const unsigned o = (unsigned)(tid >> 3) * (F * 2u) + (tid & 7) * 16u;
#pragma unroll
      for (int i = 0; i < 14; ++i) LD_NT(wv[i], o, w1 + hg * 128 + i * (32 * F * 2), 0);
      WAITV(14, xv); WAITV(14, b1w);
      layer_norm(xv, xn, lane);
#pragma unroll
      for (int i = 0; i < 14; ++i) WAITV(0, wv[i]);
      slice_partials(wv, xn, red + wave * 64, tid);
      arrive(L_HCNT);
      while (lds_ld(lbase + L_HCNT) < NC) __builtin_amdgcn_s_sleep(1);
      const float hv = rbf(rbf((red[lane] + red[64 + lane]) + (red[128 + lane] + red[192 + lane])) + lo16(b1w));
      const float u = 0.7978845608028654f * (hv + 0.044715f * hv * hv * hv);
      const float e = __builtin_amdgcn_exp2f(u * 2.885390081777927f);
      const float ge = rbf(hv * (1.f - __builtin_amdgcn_rcpf(e + 1.f)));
#pragma unroll
      for (int c = 0; c < 2; ++c) {
        unsigned w[32];
#pragma unroll
        for (int r = 0; r < 32; ++r) LD1_NT(w[r], tc, w2 + (size_t)(hg * 64 + c * 32 + (r & ~3)) * (D * 2), (r & 3) * (D * 2));
#pragma unroll
        for (int r = 0; r < 32; ++r) WAITV(0, w[r]);
#pragma unroll
        for (int r = 0; r < 32; ++r) y += f32x2{lo16(w[r]), hi16(w[r])} * lane_of(ge, c * 32 + r);
      }
    }
    if (own_o) {
      unsigned w[32];
#pragma unroll
      for (int r = 0; r < 32; ++r) LD1_NT(w[r], tc, wo + (size_t)(role * 32 + (r & ~3)) * (D * 2), (r & 3) * (D * 2));
#pragma unroll
      for (int r = 0; r < 32; ++r) WAITV(0, w[r]);
#pragma unroll
      for (int r = 0; r < 32; ++r) wof[r] = f32x2{lo16(w[r]), hi16(w[r])};
    } else {
#pragma unroll
      for (int r = 0; r < 32; ++r) wof[r] = f32x2{0.f, 0.f};
      asm volatile("global_load_dword %0, %2, %3\n\tglobal_load_dword %1, %2, %4\n\ts_waitcnt vmcnt(0)"
                   : "=&v"(xo), "=&v"(bo) : "v"((lane & 3) * 4u), "s"(x + (own_out ? role >> 1 : 0) * 8), "s"(b2 + (own_out ? role >> 1 : 0) * 8));
    }
#pragma unroll
    for (int r = 0; r < 32; ++r) asm volatile("" : "+v"(wof[r]));
    asm volatile("" : "+v"(y));
    const int nv = SSTAT / 2 - (wave >> 1);
    for (int t = 0; t < nv; ++t) { v_step(t); advance(4); }
    if (wave >= 2) { advance(2); v_step(SSTAT / 2 - 1); advance(2); } else { pc -= 2; slot -= 2; if (slot < 0) { slot += NSLOT; --gen; } }
    for (;;) {
      while (landed < 2 * pc + 2 && endq >= 2 * pc + 2) refresh();
      if (endq < 2 * pc + 2) break;
      u32x4 R[8];
      k_read(R); advance(6);
      k_scores(R, 0);
      m = head_max(m);
      const float alpha = __builtin_amdgcn_exp2f((macc - m) * C2);
      macc = m; l *= alpha;
      const float al0 = lane_of(alpha, 0), al1 = lane_of(alpha, 16);
#pragma unroll
      for (int k = 0; k < 4; ++k) { a0[k] *= al0; a1[k] *= al1; }
      v_step(0); advance(2);
    }
    STAMP(8);
    LAS float* rec = (LAS float*)(smem + L_REC);
    LAS f32x2* mlr = (LAS f32x2*)(smem + L_ML);
    {
      f32x4 o0, o1;
#pragma unroll
      for (int k = 0; k < 2; ++k) {
        o0[2 * k] = swap32_add(a0[k][0], a1[k][0]); o0[2 * k + 1] = swap32_add(a0[k][1], a1[k][1]);
        o1[2 * k] = swap32_add(a0[2 + k][0], a1[2 + k][0]); o1[2 * k + 1] = swap32_add(a0[2 + k][1], a1[2 + k][1]);
      }
      *(LAS f32x4*)(rec + wave * 512 + 8 * lane) = o0; *(LAS f32x4*)(rec + wave * 512 + 8 * lane + 4) = o1;
      l += dpp<ROR8>(l); l += dpp<HMIR>(l); l += dpp<XOR2>(l); l = swap32_add(l, l);
      if ((lane & 47) == 0) mlr[(lane >> 4) * NC + wave] = f32x2{m, l};
    }
    wg_barrier();
    const unsigned tag16 = tag[0] & 0xffffu;
    {
      const LAS f32x4* mp = (const LAS f32x4*)(mlr + (tid >> 7) * NC);
      const f32x4 m01 = mp[0], m23 = mp[1];
      const LAS f32x2* ap = (const LAS f32x2*)rec + tid;
      const f32x2 v0 = ap[0], v1 = ap[256], v2 = ap[512], v3 = ap[768];
      const float M = vmax(vmax(m01[0], m01[2]), vmax(m23[0], m23[2]));
      const float f0 = __builtin_amdgcn_exp2f((m01[0] - M) * C2), f1 = __builtin_amdgcn_exp2f((m01[2] - M) * C2);
      const float f2 = __builtin_amdgcn_exp2f((m23[0] - M) * C2), f3 = __builtin_amdgcn_exp2f((m23[2] - M) * C2);
      const f32x2 o = (v0 * f0 + v1 * f1) + (v2 * f2 + v3 * f3);
      const unsigned L = __float_as_uint((f0 * m01[1] + f1 * m01[3]) + (f2 * m23[1] + f3 * m23[3]));
      const unsigned pay = (lane & 2) ? L << 16 : ((lane & 1) ? L : __float_as_uint(M)) & 0xffff0000u;
      const u32x4 gd = {__float_as_uint(o[0]), __float_as_uint(o[1]), pay | tag16, tag[1]};
      ST4_SC1(o_pd, gd, ws + WS_PD);
    }
    STAMP(9);
    u32x4 g0, g1;
    if (own_o) {
      g0 = u32x4{0u, 0u, 0u, ~tag[1]}; g1 = g0;
      unsigned spins = 0;
      for (;;) {
        const u64 m0 = __ballot(!((g0[2] & 0xffffu) == tag16 && g0[3] == tag[1])), m1 = __ballot(!((g1[2] & 0xffffu) == tag16 && g1[3] == tag[1]));
        if ((m0 | m1) == 0) break;
        if (++spins > SPIN_CAP) { bad = true; break; }
        repoll2(g0, g1, o_g0, o_g1, gbase, m0, m1);
      }
      STAMP(10);
      const float ma = hi16(dppu<0x00>(g0[2])), mb = hi16(dppu<0x00>(g1[2]));
      const float Mw = rows_max(vmax(ma, mb));
      const float fa = __builtin_amdgcn_exp2f((ma - Mw) * C2), fb = __builtin_amdgcn_exp2f((mb - Mw) * C2);
      const float la = __uint_as_float((dppu<0x55>(g0[2]) & 0xffff0000u) | (dppu<0xAA>(g0[2]) >> 16));
      const float lb = __uint_as_float((dppu<0x55>(g1[2]) & 0xffff0000u) | (dppu<0xAA>(g1[2]) >> 16));
      const float oe = fa * __uint_as_float(g0[0]) + fb * __uint_as_float(g1[0]), oo = fa * __uint_as_float(g0[1]) + fb * __uint_as_float(g1[1]);
      const float Lp = fa * la + fb * lb;
      const float t = swap32_add(swap16_add(oe, oo), swap16_add(Lp, Lp));
      LAS float* redo = (LAS float*)(smem + L_REDO);
      LAS float* ml2 = (LAS float*)(smem + L_ML2);
      if (lane < 32) redo[(2 * (lane & 15) + (lane >> 4)) * NC + wave] = t;
      if (lane == 32) { ml2[wave] = Mw; ml2[NC + wave] = t; }
      wg_barrier();
      const f32x4 ov = ((const LAS f32x4*)redo)[lane & 31], Mv = ((const LAS f32x4*)ml2)[0], Lv = ((const LAS f32x4*)ml2)[1];
      const float M = vmax(vmax(Mv[0], Mv[1]), vmax(Mv[2], Mv[3]));
      f32x4 fv;
#pragma unroll
      for (int w = 0; w < NC; ++w) fv[w] = __builtin_amdgcn_exp2f((Mv[w] - M) * C2);
      const f32x4 on4 = ov * fv, L4 = Lv * fv;
      const float on = rbf(((on4[0] + on4[1]) + (on4[2] + on4[3])) * __builtin_amdgcn_rcpf((L4[0] + L4[1]) + (L4[2] + L4[3])));
      f32x2 y1 = {0.f, 0.f}, y2 = y1, y3 = y1;
#pragma unroll
      for (int r = 0; r < 32; r += 4) {
        y += wof[r] * lane_of(on, r); y1 += wof[r + 1] * lane_of(on, r + 1); y2 += wof[r + 2] * lane_of(on, r + 2); y3 += wof[r + 3] * lane_of(on, r + 3);
      }
      y = (y + y1) + (y2 + y3);
      if (bad) y[0] = __uint_as_float(0x7fc00000u);
      if (tid < 224) {
        const u32x4 gy = {__float_as_uint(y[0]), __float_as_uint(y[1]), tag[0], tag[1]};
        ST4_SC1(o_y, gy, ws + WS_Y);
      }
      STAMP(11);
    } else if (own_out) {
      g0 = u32x4{0u, 0u, ~tag[0], 0u}; g1 = g0;
      unsigned spins = 0;
      for (;;) {
        const u64 m0 = __ballot(!(g0[2] == tag[0] && g0[3] == tag[1])), m1 = __ballot(!(g1[2] == tag[0] && g1[3] == tag[1]));
        if ((m0 | m1) == 0) break;
        if (++spins > SPIN_CAP) { bad = true; break; }
        const u64 gate = (spins == 1 || (m0 & m1) != ~0ull) ? ~0ull : 1ull;
        repoll2(g0, g1, o_g0, o_g1, gbase, m0 & gate, m1 & gate);
      }
      STAMP(12);
      float s0 = __uint_as_float(g0[0]) + __uint_as_float(g1[0]), s1 = __uint_as_float(g0[1]) + __uint_as_float(g1[1]);
      if (bad) s0 = __uint_as_float(0x7fc00000u);
      s0 += dpp<ROR8>(s0); s1 += dpp<ROR8>(s1); s0 += dpp<ROR4>(s0); s1 += dpp<ROR4>(s1);
      float ss = swap16_add(s0, s1); ss = swap32_add(ss, ss);
      LAS float* redy = (LAS float*)(smem + L_REDO);
      if ((lane & 44) == 0) redy[wave * 8 + 2 * (lane & 3) + (lane >> 4)] = ss;
      wg_barrier();
      if (tid < 4) {
        const LAS f32x2* ry = (const LAS f32x2*)redy + tid;
        const f32x2 t = (ry[0] + ry[4]) + (ry[8] + ry[12]);
        out[(role >> 1) * 4 + tid] = pack_bf16(lo16(xo) + t[0] + lo16(bo), hi16(xo) + t[1] + hi16(bo));
        if (role == 1 && tid == 0) *(u32x2*)(ws + WS_E) = tag;
      }
      STAMP(13);
    }
  }
}

__global__ void lead_kernel(unsigned* st) { if (threadIdx.x == 0) st[blockIdx.x] = (unsigned)__builtin_amdgcn_s_memrealtime(); }
__global__ __launch_bounds__(64) void x_kernel(unsigned* st) {
  const unsigned t = (unsigned)__builtin_amdgcn_s_memrealtime();
  __shared__ unsigned sm[64];
  sm[(threadIdx.x * 37 + t) % 64] = t;
  __syncthreads();
  if (threadIdx.x == 0) st[blockIdx.x] = t + (sm[t % 64] == 0x12345u);
}
__global__ void probe_kernel(const unsigned* st, const unsigned* lead) {
  __shared__ unsigned sm[4];
  unsigned t0 = (unsigned)__builtin_amdgcn_s_memrealtime();
  if (threadIdx.x == 0) { unsigned mn = 0xffffffffu; for (int w = 0; w < 8; ++w) mn = min(mn, st[w] - lead[0]); sm[0] = mn; }
  __syncthreads();
  const unsigned d = 30000u + min(sm[0], 20000u) * 20u;
  while ((unsigned)__builtin_amdgcn_s_memrealtime() - t0 < d) __builtin_amdgcn_s_sleep(4);
}

extern "C" void kernel_launch(void* const* d_in, const int* in_sizes, int n_in, void* d_out, int out_size, void* d_ws, size_t ws_size, hipStream_t stream) {
  lead_kernel<<<1, 64, 0, stream>>>((unsigned*)((char*)d_ws + (8u << 20)));
  x_kernel<<<8, 64, 0, stream>>>((unsigned*)((char*)d_ws + (9u << 20)));
  fused_kernel<<<NWG, 320, 0, stream>>>((const u16*)d_in[0], (const char*)d_in[1], (const char*)d_in[2], (const char*)d_in[3], (const char*)d_in[4],
                                        (const char*)d_in[6], (const u16*)d_in[7], (const char*)d_in[8], (const u16*)d_in[9], (unsigned*)d_out, (char*)d_ws);
  probe_kernel<<<1, 64, 0, stream>>>((const unsigned*)((char*)d_ws + (9u << 20)), (const unsigned*)((char*)d_ws + (8u << 20)));
}
```

```cpp
#include <hip/hip_runtime.h>
#include <hip/hip_bf16.h>
#include <cstdint>
#include <type_traits>

typedef unsigned u32x4 __attribute__((ext_vector_type(4)));
typedef unsigned u32x2 __attribute__((ext_vector_type(2)));
typedef float f32x4 __attribute__((ext_vector_type(4)));
typedef float f32x2 __attribute__((ext_vector_type(2)));
typedef __bf16 bf16x2 __attribute__((ext_vector_type(2)));
typedef unsigned short u16;
typedef unsigned long long u64;
#define LAS __attribute__((address_space(3)))

#define DBG 0
#define INFL 24
#define SSTAT 56

constexpr int D = 448, F = 1792, DH = 256;
constexpr int NWG = 256, NC = 4;
constexpr int PIECE = 8192, NSLOT = 18;
constexpr int RPR = 64;
constexpr int TAILR = RPR - SSTAT, POOL = 8 * TAILR;
static_assert(POOL >= 24 && INFL % 4 == 0 && INFL >= 20 && INFL <= 32 && SSTAT % 2 == 0 && SSTAT >= 8 && (SSTAT / 2 + 1) * 64 <= 2048, "loader structure");
constexpr unsigned PD_STRIDE = 33 * 128, Y_STRIDE = 29 * 128;
constexpr size_t WS_E = 0, WS_CTR = 4096, WS_Q = 16384, WS_PD = 65536, WS_Y = WS_PD + (size_t)NWG * PD_STRIDE, WS_DBG = 4u << 20;
constexpr unsigned SPIN_CAP = 1u << 22;
constexpr unsigned L_CTL = NSLOT * PIECE;
constexpr unsigned L_LANDED = L_CTL, L_QFLAG = L_CTL + 8, L_QCNT = L_CTL + 12, L_HCNT = L_CTL + 16, L_FREE = L_CTL + 32;
constexpr unsigned L_Q = L_CTL + 128, L_RED = L_Q + 1024, L_REDO = L_RED + 1024, L_ML2 = L_REDO + 512, L_P = L_ML2 + 64;
constexpr unsigned L_ML = L_P + NC * 128, L_XN = L_ML + 64, L_REC = L_XN + 2048, L_END = L_REC + NC * 2 * DH * 4;
static_assert(L_END <= 160 * 1024 && D <= 2 * DH, "LDS");
constexpr float C2 = 0.0625f * 1.4426950408889634f;

__device__ __forceinline__ float lo16(unsigned w) { return __uint_as_float(w << 16); }
__device__ __forceinline__ float hi16(unsigned w) { return __uint_as_float(w & 0xffff0000u); }
__device__ __forceinline__ unsigned pack_bf16(float a, float b) {
  bf16x2 r = {(__bf16)a, (__bf16)b};
  return __builtin_bit_cast(unsigned, r);
}
__device__ __forceinline__ float rbf(float x) { return lo16(pack_bf16(x, x)); }
__device__ __forceinline__ float dot2(unsigned k, unsigned q, float acc) {
  return __builtin_amdgcn_fdot2_f32_bf16(__builtin_bit_cast(bf16x2, k), __builtin_bit_cast(bf16x2, q), acc, false);
}
template <int CTRL> __device__ __forceinline__ unsigned dppu(unsigned x) { return (unsigned)__builtin_amdgcn_mov_dpp((int)x, CTRL, 0xf, 0xf, true); }
template <int CTRL> __device__ __forceinline__ float dpp(float x) { return __uint_as_float(dppu<CTRL>(__float_as_uint(x))); }
constexpr int XOR1 = 0xB1, XOR2 = 0x4E, HMIR = 0x141, MIR = 0x140, ROR8 = 0x128, ROR4 = 0x124;
__device__ __forceinline__ float swap16_add(float a, float b) {
  auto r = __builtin_amdgcn_permlane16_swap(__float_as_uint(a), __float_as_uint(b), false, false);
  return __uint_as_float(r[0]) + __uint_as_float(r[1]);
}
__device__ __forceinline__ float swap32_add(float a, float b) {
  auto r = __builtin_amdgcn_permlane32_swap(__float_as_uint(a), __float_as_uint(b), false, false);
  return __uint_as_float(r[0]) + __uint_as_float(r[1]);
}
__device__ __forceinline__ float row_sum16(float x) { x += dpp<XOR1>(x); x += dpp<XOR2>(x); x += dpp<HMIR>(x); x += dpp<MIR>(x); return x; }
__device__ __forceinline__ float wave_sum(float x) { x = row_sum16(x); x = swap16_add(x, x); return swap32_add(x, x); }
__device__ __forceinline__ float lane_of(float x, int l) { return __builtin_bit_cast(float, __builtin_amdgcn_readlane(__builtin_bit_cast(int, x), l)); }
__device__ __forceinline__ float row_transpose_sum8(const float (&v)[8], u64 bit1) {
  float t0, t1, t2, t3, r;
  asm volatile("s_nop 1\n\t"
      "v_add_f32_dpp %0, %5, %5 row_ror:8 row_mask:0xf bank_mask:0xf\n\tv_add_f32_dpp %1, %6, %6 row_ror:8 row_mask:0xf bank_mask:0xf\n\t"
      "v_add_f32_dpp %2, %7, %7 row_ror:8 row_mask:0xf bank_mask:0xf\n\tv_add_f32_dpp %3, %8, %8 row_ror:8 row_mask:0xf bank_mask:0xf\n\t"
      "v_add_f32_dpp %0, %9, %9 row_ror:8 row_mask:0xf bank_mask:0xc\n\tv_add_f32_dpp %1, %10, %10 row_ror:8 row_mask:0xf bank_mask:0xc\n\t"
      "v_add_f32_dpp %2, %11, %11 row_ror:8 row_mask:0xf bank_mask:0xc\n\tv_add_f32_dpp %3, %12, %12 row_ror:8 row_mask:0xf bank_mask:0xc\n\t"
      "v_add_f32_dpp %0, %0, %0 row_half_mirror row_mask:0xf bank_mask:0x5\n\tv_add_f32_dpp %1, %1, %1 row_half_mirror row_mask:0xf bank_mask:0x5\n\t"
      "v_add_f32_dpp %0, %2, %2 row_half_mirror row_mask:0xf bank_mask:0xa\n\tv_add_f32_dpp %1, %3, %3 row_half_mirror row_mask:0xf bank_mask:0xa\n\t"
      "s_nop 1\n\t"
      "v_add_f32_dpp %2, %0, %0 quad_perm:[2,3,0,1] row_mask:0xf bank_mask:0xf\n\tv_add_f32_dpp %3, %1, %1 quad_perm:[2,3,0,1] row_mask:0xf bank_mask:0xf\n\t"
      "v_cndmask_b32 %2, %2, %3, %13\n\ts_nop 1\n\t"
      "v_add_f32_dpp %4, %2, %2 quad_perm:[1,0,3,2] row_mask:0xf bank_mask:0xf"
      : "=&v"(t0), "=&v"(t1), "=&v"(t2), "=&v"(t3), "=&v"(r)
      : "v"(v[0]), "v"(v[1]), "v"(v[2]), "v"(v[3]), "v"(v[4]), "v"(v[5]), "v"(v[6]), "v"(v[7]), "s"(bit1));
  return r;
}
__device__ __forceinline__ float head_max(float x) {
  float y;
  asm volatile("s_nop 1\n\tv_max_f32_dpp %0, %0, %0 row_ror:8 row_mask:0xf bank_mask:0xf\n\ts_nop 1\n\tv_max_f32_dpp %0, %0, %0 row_half_mirror row_mask:0xf bank_mask:0xf\n\t"
               "s_nop 1\n\tv_max_f32_dpp %0, %0, %0 quad_perm:[2,3,0,1] row_mask:0xf bank_mask:0xf\n\tv_mov_b32 %1, %0\n\ts_nop 1\n\tv_permlane32_swap_b32 %0, %1\n\tv_max_f32 %0, %0, %1"
               : "+v"(x), "=&v"(y));
  return x;
}
__device__ __forceinline__ float rows_max(float x) {
  float y;
  asm volatile("v_mov_b32 %1, %0\n\ts_nop 1\n\tv_permlane16_swap_b32 %0, %1\n\tv_max_f32 %0, %0, %1\n\tv_mov_b32 %1, %0\n\ts_nop 1\n\tv_permlane32_swap_b32 %0, %1\n\tv_max_f32 %0, %0, %1"
               : "+v"(x), "=&v"(y));
  return x;
}
__device__ __forceinline__ float vmax(float a, float b) { float r; asm("v_max_f32 %0, %1, %2" : "=v"(r) : "v"(a), "v"(b)); return r; }
__device__ __forceinline__ void layer_norm(const u32x4& xv, LAS float* xn, int lane) {
  float xf[8];
#pragma unroll
  for (int k = 0; k < 4; ++k) { xf[2 * k] = lo16(xv[k]); xf[2 * k + 1] = hi16(xv[k]); }
  const float live = lane < 56 ? 1.f : 0.f;
  const float mean = wave_sum((((xf[0] + xf[1]) + (xf[2] + xf[3])) + ((xf[4] + xf[5]) + (xf[6] + xf[7]))) * live) * (1.f / D);
  float ss = 0.f;
#pragma unroll
  for (int k = 0; k < 8; ++k) { const float d = xf[k] - mean; ss += d * d; }
  const float var = wave_sum(ss * live) * (1.f / D);
  const float rden = __builtin_amdgcn_rcpf(rbf(__builtin_amdgcn_sqrtf(rbf(rbf(var) + 1e-5f)))) * live, mb = rbf(mean);
#pragma unroll
  for (int k = 0; k < 8; k += 2) {
    const unsigned c = pack_bf16(xf[k] - mb, xf[k + 1] - mb), n = pack_bf16(lo16(c) * rden, hi16(c) * rden);
    xf[k] = lo16(n); xf[k + 1] = hi16(n);
  }
  *(LAS f32x4*)(xn + 8 * lane) = f32x4{xf[0], xf[1], xf[2], xf[3]}; *(LAS f32x4*)(xn + 8 * lane + 4) = f32x4{xf[4], xf[5], xf[6], xf[7]};
}
template <int NY> __device__ __forceinline__ void slice_partials(u32x4 (&w)[14], const LAS float* xn, LAS float* red, int tid) {
  f32x2 v[4] = {{0.f, 0.f}, {0.f, 0.f}, {0.f, 0.f}, {0.f, 0.f}};
  float xr[14];
#pragma unroll
  for (int i = 0; i < 14; ++i) xr[i] = xn[(tid >> 3) + 32 * i];
#pragma unroll
  for (int i = 0; i < 14; ++i) {
    asm volatile("s_waitcnt vmcnt(%1)" : "+v"(w[i]) : "i"(13 - i + NY));
#pragma unroll
    for (int k = 0; k < 4; ++k) v[k] += f32x2{lo16(w[i][k]), hi16(w[i][k])} * xr[i];
    asm volatile("" : "+v"(v[0]), "+v"(v[1]), "+v"(v[2]), "+v"(v[3]));
  }
  const float a0 = swap32_add(v[0][0], v[2][0]), a1 = swap32_add(v[0][1], v[2][1]), a2 = swap32_add(v[1][0], v[3][0]), a3 = swap32_add(v[1][1], v[3][1]);
  const float b0 = swap16_add(a0, a2), b1 = swap16_add(a1, a3);
  const float t1 = b0 + dpp<ROR8>(b0), t2 = b1 + dpp<ROR8>(b1);
  red[(tid & 7) * 8 + ((tid >> 3) & 7)] = (tid & 8) ? t2 : t1;
}

#define LD_NT(dst, voff, sbase, imm) asm volatile("global_load_dwordx4 %0, %1, %2 offset:%3 nt" : "=v"(dst) : "v"(voff), "s"(sbase), "i"(imm))
#define LD1_NT(dst, voff, sbase, imm) asm volatile("global_load_dword %0, %1, %2 offset:%3 nt" : "=v"(dst) : "v"(voff), "s"(sbase), "i"(imm))
#define LD4_SC1(dst, voff, sbase, imm) asm volatile("global_load_dwordx4 %0, %1, %2 offset:%3 sc1" : "=v"(dst) : "v"(voff), "s"(sbase), "i"(imm) : "memory")
#define ST4_SC1(voff, val, sbase) asm volatile("global_store_dwordx4 %0, %1, %2 sc1\n\ts_nop 1" :: "v"(voff), "v"(val), "s"(sbase) : "memory")
#define WAITV(n, reg) asm volatile("s_waitcnt vmcnt(%1)" : "+v"(reg) : "i"(n))
#define VMCNT(n) asm volatile("s_waitcnt vmcnt(%0)" :: "i"(n) : "memory")
__device__ __forceinline__ void repoll2(u32x4& g0, u32x4& g1, unsigned o0, unsigned o1, const char* base, u64 m0, u64 m1) {
  asm volatile("s_mov_b64 exec, %5\n\tglobal_load_dwordx4 %0, %2, %4 sc1\n\ts_mov_b64 exec, %6\n\tglobal_load_dwordx4 %1, %3, %4 sc1\n\ts_mov_b64 exec, -1\n\ts_waitcnt vmcnt(0)"
               : "+v"(g0), "+v"(g1) : "v"(o0), "v"(o1), "s"(base), "s"(m0), "s"(m1) : "memory");
}
__device__ __forceinline__ void wg_barrier() { asm volatile("s_waitcnt lgkmcnt(0)\n\ts_barrier" ::: "memory"); }
__device__ __forceinline__ void lds_st(unsigned addr, unsigned v) { asm volatile("ds_write_b32 %0, %1" :: "v"(addr), "v"(v) : "memory"); }
__device__ __forceinline__ unsigned lds_ld(unsigned addr) {
  unsigned v; asm volatile("ds_read_b32 %0, %1\n\ts_waitcnt lgkmcnt(0)" : "=v"(v) : "v"(addr) : "memory"); return __builtin_amdgcn_readfirstlane(v); }
template <int N> __device__ __forceinline__ void dma(const char* g, unsigned voff, unsigned lds) {
  if constexpr (N == 4)
    asm volatile("s_mov_b32 m0, %2\n\ts_nop 0\n\tglobal_load_lds_dwordx4 %0, %1 nt\n\tglobal_load_lds_dwordx4 %0, %1 offset:1024 nt\n\t"
                 "global_load_lds_dwordx4 %0, %1 offset:2048 nt\n\tglobal_load_lds_dwordx4 %0, %1 offset:3072 nt" :: "v"(voff), "s"(g), "s"(lds) : "memory");
  else
    asm volatile("s_mov_b32 m0, %2\n\ts_nop 0\n\tglobal_load_lds_dwordx4 %0, %1 nt\n\tglobal_load_lds_dwordx4 %0, %1 offset:1024 nt" :: "v"(voff), "s"(g), "s"(lds) : "memory");
}

#define STAMP(i) do {} while (0)
#define DBGVAL(i, v) do {} while (0)

__global__ __launch_bounds__(320) void fused_kernel(const char* __restrict__ kc, const char* __restrict__ vc, char* __restrict__ ws,
                                                    const u16* __restrict__ x, const char* __restrict__ wq, const char* __restrict__ wo, const char* __restrict__ w1,
                                                    const u16* __restrict__ b1, const char* __restrict__ w2, const u16* __restrict__ b2,
                                                    unsigned* __restrict__ out) {
  __shared__ __attribute__((aligned(1024))) char smem[L_END];
  const unsigned lbase = (unsigned)(uintptr_t)(LAS char*)smem;
  const int b = blockIdx.x, tid = threadIdx.x, lane = tid & 63, wave = __builtin_amdgcn_readfirstlane(tid >> 6);
  const int role = b >> 1;
  const bool own_o = ((b ^ (b >> 3)) & 1) == 0, has_mlp = own_o && (role & 3) == 0 && role < 112;
  const bool make_q = !own_o && (role & 1) == 0, own_out = !own_o && (role & 1) == 1 && role < 112;
  STAMP(0);
  if (__builtin_expect(wave == NC, 1)) {
    __builtin_amdgcn_s_setprio(3);
    const unsigned voff = lane * 16;
    const int q = b >> 3;
    unsigned* ctr = (unsigned*)(ws + WS_CTR) + q * 64;
    int nq = 0, slot = 0, gen = 0;
    constexpr int LAG = INFL / 4 - 1;
    int frs = 0;
    auto put = [&](const char* g, int u) {
      if ((u & 1) == 0 && frs < gen) { while ((int)lds_ld(lbase + L_FREE + slot * 4) < gen) __builtin_amdgcn_s_sleep(1); }
      dma<4>(g, voff, lbase + slot * PIECE + (u & 1) * 4096);
      ++nq;
      if (u & 1) { if (++slot == NSLOT) { slot = 0; ++gen; } }
    };
    auto settle_w = [&](int u, auto w) {
      constexpr int W = decltype(w)::value;
      if (u & 1) {
        unsigned fr;
        asm volatile("ds_read_b32 %0, %1\n\ts_waitcnt vmcnt(%2) lgkmcnt(0)" : "=&v"(fr) : "v"(lbase + L_FREE + slot * 4), "i"(W - 4) : "memory");
        frs = __builtin_amdgcn_readfirstlane(fr);
      } else VMCNT(W - 4);
      lds_st(lbase + L_LANDED, max(nq - (W / 4 - 1), 0));
    };
    auto settle = [&](int u) { settle_w(u, std::integral_constant<int, INFL>{}); };
    unsigned tv = 0;
    auto ticket = [&]() { asm volatile("s_mov_b64 exec, 1\n\tglobal_atomic_add %0, %1, %2, %3 sc0\n\ts_mov_b64 exec, -1" : "=v"(tv) : "v"(0u), "v"(1u), "s"(ctr) : "memory"); };
    size_t voffs = (size_t)b * (1u << 20);
    {
      const char* g = kc + voffs;
      put(g, 0); put(g + 4096, 1);
      STAMP(4);
      if (lane < 8 + NSLOT) lds_st(lbase + L_CTL + lane * 4, lane == 1 ? 0xffffffffu : 0u);
      wg_barrier();
      for (int p = 1; p < 2 * SSTAT; ++p) { g += PIECE; put(g, 0); settle(0); put(g + 4096, 1); settle(1); }
      STAMP(5);
      g = vc + voffs;
      for (int p = 0; p < 2 * SSTAT - 6; ++p) { put(g, 0); settle(0); put(g + 4096, 1); settle(1); g += PIECE; }
      while (lds_ld(lbase + L_QFLAG) == 0) __builtin_amdgcn_s_sleep(1);
#pragma unroll
      for (int u = 0; u < 8; ++u) { if (u == 8 - INFL / 4) ticket(); put(g + u * 4096, u); settle(u); }
      STAMP(6);
      voffs += (size_t)(SSTAT - 1) * (2 * PIECE);
    }
    for (;;) {
      asm volatile("" : "+v"(tv));
      const int k = __builtin_amdgcn_readfirstlane(tv);
      constexpr int A = POOL > 24 ? POOL - 24 : 0, N4 = (A + 3) / 4;
      int cur, sz;
      if (k < N4) { cur = 4 * k; sz = min(4, A - cur); } else if (k < N4 + 8) { cur = A + 2 * (k - N4); sz = 2; } else { cur = A + 16 + (k - N4 - 8); sz = 1; }
      if (cur >= POOL) break;
      for (; sz > 0; --sz, ++cur) {
        const size_t off = (size_t)(8 * q + cur / TAILR) * (1u << 20) + (size_t)(SSTAT + cur % TAILR) * (2 * PIECE);
        const char* kp = kc + off; const char* vp = vc + voffs;
#pragma unroll
        for (int u = 0; u < 8; ++u) {
          if (u == 8 - INFL / 4 && sz == 1) ticket();
          put(u < 4 ? kp + u * 4096 : vp + (u - 4) * 4096, u);
          settle(u);
        }
        voffs = off;
      }
    }
    lds_st(lbase + L_LANDED + 4, nq);
    STAMP(1); DBGVAL(15, nq + 4);
    slot += 2; if (slot >= NSLOT) { slot -= NSLOT; ++gen; }
    frs = 0;
#pragma unroll
    for (int u = 0; u < 4; ++u) { put(vc + voffs + u * 4096, u); settle(u); }
#pragma unroll
    for (int i = LAG - 1; i >= 0; --i) { VMCNT(4 * i); lds_st(lbase + L_LANDED, nq - i + (i < 4 ? 4 : 0)); }
    STAMP(2);
  } else {
    const int h = b >> 5;
    LAS float* xn = (LAS float*)(smem + L_XN);
    LAS float* red = (LAS float*)(smem + L_RED);
    u32x2 ev, tag;
    auto arrive = [&](unsigned cnt) -> int {
      unsigned arr;
      asm volatile("s_mov_b64 exec, 1\n\tds_add_rtn_u32 %0, %1, %2\n\ts_mov_b64 exec, -1\n\ts_waitcnt lgkmcnt(0)" : "=&v"(arr) : "v"(lbase + cnt), "v"(1u) : "memory");
      return __builtin_amdgcn_readfirstlane(arr);
    };
    auto make_tag = [&]() {
      WAITV(0, ev);
      const u64 e = (u64)(unsigned)__builtin_amdgcn_readfirstlane(ev[1]) << 32 | (unsigned)__builtin_amdgcn_readfirstlane(ev[0]);
      const u64 z = e * 6364136223846793005ull + 1442695040888963407ull;
      tag = u32x2{(unsigned)z, (unsigned)(z >> 32)};
    };
    asm volatile("global_load_dwordx2 %0, %1, %2 sc1" : "=v"(ev) : "v"(0u), "s"(ws + WS_E) : "memory");
    if (make_q) {
      const int qi = role >> 1;
      if (wave == 0 && (qi & 4) == 0) asm volatile("global_store_dword %0, %1, %2 sc1" :: "v"(0u), "v"(0u), "s"(ws + WS_CTR + ((qi >> 3) * 4 + (qi & 3)) * 256) : "memory");
      u32x4 xv, wv[14];
      asm volatile("global_load_dwordx4 %0, %1, %2" : "=v"(xv) : "v"((lane < 56 ? lane : 55) * 16u), "s"(x));
      const unsigned o = (unsigned)(tid >> 3) * 8192u + (tid & 7) * 16u;
#pragma unroll
      for (int i = 0; i < 14; ++i) LD_NT(wv[i], o, wq + qi * 128 + i * (32 * 8192), 0);
      STAMP(4);
      wg_barrier();
      WAITV(14, xv);
      STAMP(7);
      layer_norm(xv, xn, lane);
      STAMP(1);
      slice_partials<0>(wv, xn, red + wave * 64, tid);
      STAMP(2);
      make_tag();
      if (arrive(L_QCNT) == NC - 1 && lane < 16) {
        const LAS f32x4* rq = (const LAS f32x4*)red + lane;
        const f32x4 s = (rq[0] + rq[16]) + (rq[32] + rq[48]);
        const u32x4 g = {pack_bf16(s[0], s[1]), pack_bf16(s[2], s[3]), tag[0], tag[1]};
        ST4_SC1((unsigned)(qi * 256 + lane * 16), g, ws + WS_Q);
      }
      STAMP(3);
    } else {
      wg_barrier();
    }
    bool bad = false;
    unsigned o_pd = (unsigned)(b * PD_STRIDE + tid * 16), o_y = (unsigned)(role * Y_STRIDE + (tid < 224 ? tid : 0) * 16);
    unsigned o_g0 = own_o ? (unsigned)((h * 32 + (tid >> 4)) * PD_STRIDE + (role & 15) * 256 + (tid & 15) * 16)
                          : (unsigned)((tid >> 2) * Y_STRIDE + (own_out ? role >> 1 : 0) * 64 + (tid & 3) * 16);
    unsigned o_g1 = o_g0 + (own_o ? 16 * PD_STRIDE : 64 * Y_STRIDE);
    asm volatile("" : "+v"(o_pd), "+v"(o_y), "+v"(o_g0), "+v"(o_g1));
    const char* gbase = ws + (own_o ? WS_PD : WS_Y);
    f32x2 y = {0.f, 0.f};
    unsigned wow[32];
    unsigned xo = 0, bo = 0;
    const unsigned tc = (tid < 224 ? tid : 223) * 4u;
    if (has_mlp) {
      const int hg = role >> 2;
      u32x4 xv, wv[14]; unsigned b1w, wa[32], wb[32];
      asm volatile("global_load_dwordx4 %0, %1, %2" : "=v"(xv) : "v"((lane < 56 ? lane : 55) * 16u), "s"(x));
      asm volatile("global_load_ushort %0, %1, %2" : "=v"(b1w) : "v"(lane * 2u), "s"(b1 + hg * 64));
      const unsigned o = (unsigned)(tid >> 3) * (F * 2u) + (tid & 7) * 16u;
#pragma unroll
      for (int i = 0; i < 14; ++i) LD_NT(wv[i], o, w1 + hg * 128 + i * (32 * F * 2), 0);
#pragma unroll
      for (int r = 0; r < 32; ++r) LD1_NT(wa[r], tc, w2 + (size_t)(hg * 64 + (r & ~3)) * (D * 2), (r & 3) * (D * 2));
      WAITV(46, xv); WAITV(46, b1w);
      layer_norm(xv, xn, lane);
      slice_partials<32>(wv, xn, red + wave * 64, tid);
      arrive(L_HCNT);
#pragma unroll
      for (int r = 0; r < 28; ++r) LD1_NT(wb[r], tc, w2 + (size_t)(hg * 64 + 32 + (r & ~3)) * (D * 2), (r & 3) * (D * 2));
      while (lds_ld(lbase + L_HCNT) < NC) __builtin_amdgcn_s_sleep(1);
      const float hv = rbf(rbf((red[lane] + red[64 + lane]) + (red[128 + lane] + red[192 + lane])) + lo16(b1w));
      const float u = 0.7978845608028654f * (hv + 0.044715f * hv * hv * hv);
      const float e = __builtin_amdgcn_exp2f(u * 2.885390081777927f);
      const float ge = rbf(hv * (1.f - __builtin_amdgcn_rcpf(e + 1.f)));
#pragma unroll
      for (int r = 0; r < 32; ++r) WAITV(28, wa[r]);
#pragma unroll
      for (int r = 28; r < 32; ++r) LD1_NT(wb[r], tc, w2 + (size_t)(hg * 64 + 32 + (r & ~3)) * (D * 2), (r & 3) * (D * 2));
#pragma unroll
      for (int r = 0; r < 32; ++r) y += f32x2{lo16(wa[r]), hi16(wa[r])} * lane_of(ge, r);
      asm volatile("" : "+v"(y));
#pragma unroll
      for (int r = 0; r < 32; ++r) WAITV(0, wb[r]);
#pragma unroll
      for (int r = 0; r < 32; ++r) y += f32x2{lo16(wb[r]), hi16(wb[r])} * lane_of(ge, 32 + r);
    }
    if (own_o) {
#pragma unroll
      for (int r = 0; r < 32; ++r) LD1_NT(wow[r], tc, wo + (size_t)(role * 32 + (r & ~3)) * (D * 2), (r & 3) * (D * 2));
#pragma unroll
      for (int r = 0; r < 32; ++r) WAITV(0, wow[r]);
    } else {
#pragma unroll
      for (int r = 0; r < 32; ++r) wow[r] = 0u;
      asm volatile("global_load_dword %0, %2, %3\n\tglobal_load_dword %1, %2, %4\n\ts_waitcnt vmcnt(0)"
                   : "=&v"(xo), "=&v"(bo) : "v"((lane & 3) * 4u), "s"(x + (own_out ? role >> 1 : 0) * 8), "s"(b2 + (own_out ? role >> 1 : 0) * 8));
    }
    asm volatile("" : "+v"(y));
    make_tag();
    float m = -INFINITY, macc = -INFINITY, l = 0.f;
    f32x2 a0[4], a1[4];
#pragma unroll
    for (int k = 0; k < 4; ++k) { a0[k] = f32x2{0.f, 0.f}; a1[k] = f32x2{0.f, 0.f}; }
    unsigned landed = 0, endq = 0xffffffffu;
    unsigned pc = wave; int slot = wave, gen = 1;
    u32x4 q0, q1;
    LAS u16* scw = (LAS u16*)(smem + L_REC + wave * 2048) + (lane >> 1);
    auto refresh = [&]() {
      u32x2 c; asm volatile("ds_read_b64 %0, %1\n\ts_waitcnt lgkmcnt(0)" : "=v"(c) : "v"(lbase + L_LANDED) : "memory");
      landed = __builtin_amdgcn_readfirstlane(c[0]); endq = __builtin_amdgcn_readfirstlane(c[1]);
    };
    auto advance = [&](int n) { pc += n; slot += n; if (slot >= NSLOT) { slot -= NSLOT; ++gen; } };
    auto k_read = [&](u32x4 (&R)[8]) {
      const LAS u32x4* kt = (const LAS u32x4*)(smem + slot * PIECE) + lane;
#pragma unroll
      for (int i = 0; i < 8; ++i) R[i] = kt[i * 64];
      lds_st(lbase + L_FREE + slot * 4, gen);
    };
    auto k_scores = [&](const u32x4 (&R)[8], int t) {
      float sc[8];
#pragma unroll
      for (int i = 0; i < 8; ++i) {
        float a = 0.f, c = 0.f;
#pragma unroll
        for (int k = 0; k < 4; ++k) { a = dot2(R[i][k], q0[k], a); c = dot2(R[i][k], q1[k], c); }
        sc[i] = swap16_add(a, c);
      }
      const float s = row_transpose_sum8(sc, 0xccccccccccccccccull);
      const unsigned sb = pack_bf16(s, s);
      scw[t * 32] = (u16)sb;
      m = vmax(m, lo16(sb));
    };
    auto v_step = [&](int t) {
      LAS float* pw = (LAS float*)(smem + L_P) + wave * 32;
      const float p = __builtin_amdgcn_exp2f((lo16(scw[t * 32]) - m) * C2);
      pw[lane >> 1] = p;
      l += p;
      f32x4 pp[4];
#pragma unroll
      for (int k = 0; k < 4; ++k) pp[k] = *(const LAS f32x4*)(pw + (lane >> 5) * 16 + 4 * k);
      const LAS u32x4* vt = (const LAS u32x4*)(smem + slot * PIECE) + lane;
#pragma unroll
      for (int hf = 0; hf < 2; ++hf) {
        while (landed < 2 * pc + 1 + hf) refresh();
        u32x4 R[4];
#pragma unroll
        for (int i = 0; i < 4; ++i) R[i] = vt[(hf * 4 + i) * 64];
        if (hf == 1) lds_st(lbase + L_FREE + slot * 4, gen);
#pragma unroll
        for (int i = 0; i < 4; ++i)
#pragma unroll
          for (int k = 0; k < 4; ++k) { const f32x2 v = {lo16(R[i][k]), hi16(R[i][k])}; a0[k] += v * pp[hf][i]; a1[k] += v * pp[2 + hf][i]; }
        asm volatile("" : "+v"(a0[0]), "+v"(a0[1]), "+v"(a0[2]), "+v"(a0[3]), "+v"(a1[0]), "+v"(a1[1]), "+v"(a1[2]), "+v"(a1[3]));
      }
    };
    {
      u32x4 R0[8], R1[8], R2[8], R3[8];
      while (landed < 2 * pc + 2) refresh();
      k_read(R0); advance(4);
      while (landed < 2 * pc + 2) refresh();
      k_read(R1); advance(4);
      while (landed < 2 * pc + 2) refresh();
      k_read(R2); advance(4);
      if (wave != 0) {
        while (landed < 2 * pc + 2) refresh();
        k_read(R3);
      }
      if (wave == 0) {
        u32x4 g0 = {0u, 0u, ~tag[0], 0u}, g1 = g0;
        const unsigned qo = (unsigned)(h * 2048 + lane * 32);
        unsigned spins = 0;
        for (;;) {
          const u64 miss = __ballot(!(g0[2] == tag[0] && g0[3] == tag[1] && g1[2] == tag[0] && g1[3] == tag[1]));
          if (miss == 0) break;
          if (++spins > SPIN_CAP) { bad = true; break; }
          repoll2(g0, g1, qo, qo + 16, ws + WS_Q, miss, miss);
        }
        *(LAS u32x4*)(smem + L_Q + lane * 16) = u32x4{g0[0], g0[1], g1[0], g1[1]};
        lds_st(lbase + L_QFLAG, 1u);
        DBGVAL(15, spins);
        while (landed < 2 * pc + 2) refresh();
        k_read(R3);
      } else {
        while (lds_ld(lbase + L_QFLAG) == 0) __builtin_amdgcn_s_sleep(1);
      }
      q0 = *(const LAS u32x4*)(smem + L_Q + (lane & 31) * 16); q1 = *(const LAS u32x4*)(smem + L_Q + 512 + (lane & 31) * 16);
      STAMP(5);
      advance(4);
      k_scores(R0, 0); k_scores(R1, 1); k_scores(R2, 2); k_scores(R3, 3);
      STAMP(6);
    }
    for (int t = 4; t < SSTAT / 2; ++t) {
      u32x4 R[8];
      while (landed < 2 * pc + 2) refresh();
      k_read(R); advance(4);
      k_scores(R, t);
    }
    m = head_max(m); macc = m;
    f32x2 wof[32];
#pragma unroll
    for (int r = 0; r < 32; ++r) { wof[r] = f32x2{lo16(wow[r]), hi16(wow[r])}; asm volatile("" : "+v"(wof[r])); }
    const int nv = SSTAT / 2 - (wave >> 1);
    for (int t = 0; t < nv; ++t) { v_step(t); advance(4); }
    if (wave >= 2) { advance(2); v_step(SSTAT / 2 - 1); advance(2); } else { pc -= 2; slot -= 2; if (slot < 0) { slot += NSLOT; --gen; } }
    for (;;) {
      while (landed < 2 * pc + 2 && endq >= 2 * pc + 2) refresh();
      if (endq < 2 * pc + 2) break;
      u32x4 R[8];
      k_read(R); advance(6);
      k_scores(R, 0);
      m = head_max(m);
      const float alpha = __builtin_amdgcn_exp2f((macc - m) * C2);
      macc = m; l *= alpha;
      const float al0 = lane_of(alpha, 0), al1 = lane_of(alpha, 16);
#pragma unroll
      for (int k = 0; k < 4; ++k) { a0[k] *= al0; a1[k] *= al1; }
      v_step(0); advance(2);
    }
    STAMP(8);
    LAS float* rec = (LAS float*)(smem + L_REC);
    LAS f32x2* mlr = (LAS f32x2*)(smem + L_ML);
    {
      f32x4 o0, o1;
#pragma unroll
      for (int k = 0; k < 2; ++k) {
        o0[2 * k] = swap32_add(a0[k][0], a1[k][0]); o0[2 * k + 1] = swap32_add(a0[k][1], a1[k][1]);
        o1[2 * k] = swap32_add(a0[2 + k][0], a1[2 + k][0]); o1[2 * k + 1] = swap32_add(a0[2 + k][1], a1[2 + k][1]);
      }
      *(LAS f32x4*)(rec + wave * 512 + 8 * lane) = o0; *(LAS f32x4*)(rec + wave * 512 + 8 * lane + 4) = o1;
      l += dpp<ROR8>(l); l += dpp<HMIR>(l); l += dpp<XOR2>(l); l = swap32_add(l, l);
      if ((lane & 47) == 0) mlr[(lane >> 4) * NC + wave] = f32x2{m, l};
    }
    wg_barrier();
    const unsigned tag16 = tag[0] & 0xffffu;
    {
      const LAS f32x4* mp = (const LAS f32x4*)(mlr + (tid >> 7) * NC);
      const f32x4 m01 = mp[0], m23 = mp[1];
      const LAS f32x2* ap = (const LAS f32x2*)rec + tid;
      const f32x2 v0 = ap[0], v1 = ap[256], v2 = ap[512], v3 = ap[768];
      const float M = vmax(vmax(m01[0], m01[2]), vmax(m23[0], m23[2]));
      const float f0 = __builtin_amdgcn_exp2f((m01[0] - M) * C2), f1 = __builtin_amdgcn_exp2f((m01[2] - M) * C2);
      const float f2 = __builtin_amdgcn_exp2f((m23[0] - M) * C2), f3 = __builtin_amdgcn_exp2f((m23[2] - M) * C2);
      const f32x2 o = (v0 * f0 + v1 * f1) + (v2 * f2 + v3 * f3);
      const unsigned L = __float_as_uint((f0 * m01[1] + f1 * m01[3]) + (f2 * m23[1] + f3 * m23[3]));
      const unsigned pay = (lane & 2) ? L << 16 : ((lane & 1) ? L : __float_as_uint(M)) & 0xffff0000u;
      const u32x4 gd = {__float_as_uint(o[0]), __float_as_uint(o[1]), pay | tag16, tag[1]};
      ST4_SC1(o_pd, gd, ws + WS_PD);
    }
    STAMP(9);
    u32x4 g0, g1;
    if (own_o) {
      g0 = u32x4{0u, 0u, 0u, ~tag[1]}; g1 = g0;
      unsigned spins = 0;
      for (;;) {
        const u64 m0 = __ballot(!((g0[2] & 0xffffu) == tag16 && g0[3] == tag[1])), m1 = __ballot(!((g1[2] & 0xffffu) == tag16 && g1[3] == tag[1]));
        if ((m0 | m1) == 0) break;
        if (++spins > SPIN_CAP) { bad = true; break; }
        repoll2(g0, g1, o_g0, o_g1, gbase, m0, m1);
      }
      STAMP(10);
      const float ma = hi16(dppu<0x00>(g0[2])), mb = hi16(dppu<0x00>(g1[2]));
      const float Mw = rows_max(vmax(ma, mb));
      const float fa = __builtin_amdgcn_exp2f((ma - Mw) * C2), fb = __builtin_amdgcn_exp2f((mb - Mw) * C2);
      const float la = __uint_as_float((dppu<0x55>(g0[2]) & 0xffff0000u) | (dppu<0xAA>(g0[2]) >> 16));
      const float lb = __uint_as_float((dppu<0x55>(g1[2]) & 0xffff0000u) | (dppu<0xAA>(g1[2]) >> 16));
      const float oe = fa * __uint_as_float(g0[0]) + fb * __uint_as_float(g1[0]), oo = fa * __uint_as_float(g0[1]) + fb * __uint_as_float(g1[1]);
      const float Lp = fa * la + fb * lb;
      const float t = swap32_add(swap16_add(oe, oo), swap16_add(Lp, Lp));
      LAS float* redo = (LAS float*)(smem + L_REDO);
      LAS float* ml2 = (LAS float*)(smem + L_ML2);
      if (lane < 32) redo[(2 * (lane & 15) + (lane >> 4)) * NC + wave] = t;
      if (lane == 32) { ml2[wave] = Mw; ml2[NC + wave] = t; }
      wg_barrier();
      const f32x4 ov = ((const LAS f32x4*)redo)[lane & 31], Mv = ((const LAS f32x4*)ml2)[0], Lv = ((const LAS f32x4*)ml2)[1];
      const float M = vmax(vmax(Mv[0], Mv[1]), vmax(Mv[2], Mv[3]));
      f32x4 fv;
#pragma unroll
      for (int w = 0; w < NC; ++w) fv[w] = __builtin_amdgcn_exp2f((Mv[w] - M) * C2);
      const f32x4 on4 = ov * fv, L4 = Lv * fv;
      const float on = rbf(((on4[0] + on4[1]) + (on4[2] + on4[3])) * __builtin_amdgcn_rcpf((L4[0] + L4[1]) + (L4[2] + L4[3])));
      f32x2 y1 = {0.f, 0.f}, y2 = y1, y3 = y1;
#pragma unroll
      for (int r = 0; r < 32; r += 4) {
        y += wof[r] * lane_of(on, r); y1 += wof[r + 1] * lane_of(on, r + 1); y2 += wof[r + 2] * lane_of(on, r + 2); y3 += wof[r + 3] * lane_of(on, r + 3);
      }
      y = (y + y1) + (y2 + y3);
      if (bad) y[0] = __uint_as_float(0x7fc00000u);
      if (tid < 224) {
        const u32x4 gy = {__float_as_uint(y[0]), __float_as_uint(y[1]), tag[0], tag[1]};
        ST4_SC1(o_y, gy, ws + WS_Y);
      }
      STAMP(11);
    } else if (own_out) {
      g0 = u32x4{0u, 0u, ~tag[0], 0u}; g1 = g0;
      unsigned spins = 0;
      for (;;) {
        const u64 m0 = __ballot(!(g0[2] == tag[0] && g0[3] == tag[1])), m1 = __ballot(!(g1[2] == tag[0] && g1[3] == tag[1]));
        if ((m0 | m1) == 0) break;
        if (++spins > SPIN_CAP) { bad = true; break; }
        const u64 gate = (spins == 1 || (m0 & m1) != ~0ull) ? ~0ull : 1ull;
        repoll2(g0, g1, o_g0, o_g1, gbase, m0 & gate, m1 & gate);
      }
      STAMP(12);
      float s0 = __uint_as_float(g0[0]) + __uint_as_float(g1[0]), s1 = __uint_as_float(g0[1]) + __uint_as_float(g1[1]);
      if (bad) s0 = __uint_as_float(0x7fc00000u);
      s0 += dpp<ROR8>(s0); s1 += dpp<ROR8>(s1); s0 += dpp<ROR4>(s0); s1 += dpp<ROR4>(s1);
      float ss = swap16_add(s0, s1); ss = swap32_add(ss, ss);
      LAS float* redy = (LAS float*)(smem + L_REDO);
      if ((lane & 44) == 0) redy[wave * 8 + 2 * (lane & 3) + (lane >> 4)] = ss;
      wg_barrier();
      if (tid < 4) {
        const LAS f32x2* ry = (const LAS f32x2*)redy + tid;
        const f32x2 t = (ry[0] + ry[4]) + (ry[8] + ry[12]);
        out[(role >> 1) * 4 + tid] = pack_bf16(lo16(xo) + t[0] + lo16(bo), hi16(xo) + t[1] + hi16(bo));
        if (role == 1 && tid == 0) *(u32x2*)(ws + WS_E) = tag;
      }
      STAMP(13);
    }
  }
}

extern "C" void kernel_launch(void* const* d_in, const int* in_sizes, int n_in, void* d_out, int out_size, void* d_ws, size_t ws_size, hipStream_t stream) {
  fused_kernel<<<NWG, 320, 0, stream>>>((const char*)d_in[1], (const char*)d_in[2], (char*)d_ws, (const u16*)d_in[0], (const char*)d_in[3], (const char*)d_in[4],
                                        (const char*)d_in[6], (const u16*)d_in[7], (const char*)d_in[8], (const u16*)d_in[9], (unsigned*)d_out);
}
```

```cpp
#include <hip/hip_runtime.h>
#include <hip/hip_bf16.h>
#include <cstdint>
#include <type_traits>

typedef unsigned u32x4 __attribute__((ext_vector_type(4)));
typedef unsigned u32x2 __attribute__((ext_vector_type(2)));
typedef float f32x4 __attribute__((ext_vector_type(4)));
typedef float f32x2 __attribute__((ext_vector_type(2)));
typedef __bf16 bf16x2 __attribute__((ext_vector_type(2)));
typedef unsigned short u16;
typedef unsigned long long u64;
#define LAS __attribute__((address_space(3)))

#define DBG 0
#define INFL 24
#define T_MLP 8
#define T_O 4
#define T_Q 4
#define T_X 2
#define T3NEW 0
#define H2NEW 0
#define WOLATE 1
#define GATE 1
#define P1SLEEP 0
#define CH0 4
#define PRETOUCH 0
#define NC2 8
#define NC1 8

constexpr int D = 448, F = 1792, DH = 256;
constexpr int NWG = 256, NC = 4;
constexpr int PIECE = 8192, NSLOT = 18;
constexpr int RPR = 64;
constexpr int POOL = T_MLP + 3 * T_O + 2 * T_Q + 2 * T_X;
static_assert(POOL >= 2 * NC2 + NC1 && INFL % 4 == 0 && INFL >= 20 && INFL <= 32, "loader structure");
static_assert(T_MLP % 2 == 0 && T_O % 2 == 0 && T_Q % 2 == 0 && T_X % 2 == 0 && T_X >= 2 && T_O >= 2 && T_Q >= 2, "an even number of static rounds, at most 62");
constexpr unsigned PD_STRIDE = 33 * 128, Y_STRIDE = 29 * 128;
constexpr size_t WS_E = 0, WS_CTR = 4096, WS_Q = 16384, WS_PD = 65536, WS_Y = WS_PD + (size_t)NWG * PD_STRIDE, WS_DBG = 4u << 20;
constexpr unsigned SPIN_CAP = 1u << 22;
constexpr unsigned L_CTL = NSLOT * PIECE;
constexpr unsigned L_LANDED = L_CTL, L_QFLAG = L_CTL + 8, L_QCNT = L_CTL + 12, L_HCNT = L_CTL + 16, L_FREE = L_CTL + 32;
constexpr unsigned L_Q = L_CTL + 128, L_RED = L_Q + 1024, L_REDO = L_RED + 1024, L_ML2 = L_REDO + 512, L_P = L_ML2 + 64;
constexpr unsigned L_ML = L_P + NC * 128, L_XN = L_ML + 64, L_REC = L_XN + 2048, L_END = L_REC + NC * 2 * DH * 4;
static_assert(L_END <= 160 * 1024 && D <= 2 * DH, "LDS");
constexpr float C2 = 0.0625f * 1.4426950408889634f;

__device__ __forceinline__ float lo16(unsigned w) { return __uint_as_float(w << 16); }
__device__ __forceinline__ float hi16(unsigned w) { return __uint_as_float(w & 0xffff0000u); }
__device__ __forceinline__ unsigned pack_bf16(float a, float b) {
  bf16x2 r = {(__bf16)a, (__bf16)b};
  return __builtin_bit_cast(unsigned, r);
}
__device__ __forceinline__ float rbf(float x) { return lo16(pack_bf16(x, x)); }
__device__ __forceinline__ float dot2(unsigned k, unsigned q, float acc) {
  return __builtin_amdgcn_fdot2_f32_bf16(__builtin_bit_cast(bf16x2, k), __builtin_bit_cast(bf16x2, q), acc, false);
}
template <int CTRL> __device__ __forceinline__ unsigned dppu(unsigned x) { return (unsigned)__builtin_amdgcn_mov_dpp((int)x, CTRL, 0xf, 0xf, true); }
template <int CTRL> __device__ __forceinline__ float dpp(float x) { return __uint_as_float(dppu<CTRL>(__float_as_uint(x))); }
constexpr int XOR1 = 0xB1, XOR2 = 0x4E, HMIR = 0x141, MIR = 0x140, ROR8 = 0x128, ROR4 = 0x124;
__device__ __forceinline__ float swap16_add(float a, float b) {
  auto r = __builtin_amdgcn_permlane16_swap(__float_as_uint(a), __float_as_uint(b), false, false);
  return __uint_as_float(r[0]) + __uint_as_float(r[1]);
}
__device__ __forceinline__ float swap32_add(float a, float b) {
  auto r = __builtin_amdgcn_permlane32_swap(__float_as_uint(a), __float_as_uint(b), false, false);
  return __uint_as_float(r[0]) + __uint_as_float(r[1]);
}
__device__ __forceinline__ float row_sum16(float x) { x += dpp<XOR1>(x); x += dpp<XOR2>(x); x += dpp<HMIR>(x); x += dpp<MIR>(x); return x; }
__device__ __forceinline__ float wave_sum(float x) { x = row_sum16(x); x = swap16_add(x, x); return swap32_add(x, x); }
__device__ __forceinline__ float lane_of(float x, int l) { return __builtin_bit_cast(float, __builtin_amdgcn_readlane(__builtin_bit_cast(int, x), l)); }
__device__ __forceinline__ float row_transpose_sum8(const float (&v)[8], u64 bit1) {
  float t0, t1, t2, t3, r;
  asm volatile("s_nop 1\n\t"
      "v_add_f32_dpp %0, %5, %5 row_ror:8 row_mask:0xf bank_mask:0xf\n\tv_add_f32_dpp %1, %6, %6 row_ror:8 row_mask:0xf bank_mask:0xf\n\t"
      "v_add_f32_dpp %2, %7, %7 row_ror:8 row_mask:0xf bank_mask:0xf\n\tv_add_f32_dpp %3, %8, %8 row_ror:8 row_mask:0xf bank_mask:0xf\n\t"
      "v_add_f32_dpp %0, %9, %9 row_ror:8 row_mask:0xf bank_mask:0xc\n\tv_add_f32_dpp %1, %10, %10 row_ror:8 row_mask:0xf bank_mask:0xc\n\t"
      "v_add_f32_dpp %2, %11, %11 row_ror:8 row_mask:0xf bank_mask:0xc\n\tv_add_f32_dpp %3, %12, %12 row_ror:8 row_mask:0xf bank_mask:0xc\n\t"
      "v_add_f32_dpp %0, %0, %0 row_half_mirror row_mask:0xf bank_mask:0x5\n\tv_add_f32_dpp %1, %1, %1 row_half_mirror row_mask:0xf bank_mask:0x5\n\t"
      "v_add_f32_dpp %0, %2, %2 row_half_mirror row_mask:0xf bank_mask:0xa\n\tv_add_f32_dpp %1, %3, %3 row_half_mirror row_mask:0xf bank_mask:0xa\n\t"
      "s_nop 1\n\t"
      "v_add_f32_dpp %2, %0, %0 quad_perm:[2,3,0,1] row_mask:0xf bank_mask:0xf\n\tv_add_f32_dpp %3, %1, %1 quad_perm:[2,3,0,1] row_mask:0xf bank_mask:0xf\n\t"
      "v_cndmask_b32 %2, %2, %3, %13\n\ts_nop 1\n\t"
      "v_add_f32_dpp %4, %2, %2 quad_perm:[1,0,3,2] row_mask:0xf bank_mask:0xf"
      : "=&v"(t0), "=&v"(t1), "=&v"(t2), "=&v"(t3), "=&v"(r)
      : "v"(v[0]), "v"(v[1]), "v"(v[2]), "v"(v[3]), "v"(v[4]), "v"(v[5]), "v"(v[6]), "v"(v[7]), "s"(bit1));
  return r;
}
__device__ __forceinline__ float head_max(float x) {
  float y;
  asm volatile("s_nop 1\n\tv_max_f32_dpp %0, %0, %0 row_ror:8 row_mask:0xf bank_mask:0xf\n\ts_nop 1\n\tv_max_f32_dpp %0, %0, %0 row_half_mirror row_mask:0xf bank_mask:0xf\n\t"
               "s_nop 1\n\tv_max_f32_dpp %0, %0, %0 quad_perm:[2,3,0,1] row_mask:0xf bank_mask:0xf\n\tv_mov_b32 %1, %0\n\ts_nop 1\n\tv_permlane32_swap_b32 %0, %1\n\tv_max_f32 %0, %0, %1"
               : "+v"(x), "=&v"(y));
  return x;
}
__device__ __forceinline__ float quads_max(float x) {
  float y;
  asm volatile("s_nop 1\n\tv_max_f32_dpp %0, %0, %0 row_ror:8 row_mask:0xf bank_mask:0xf\n\ts_nop 1\n\tv_max_f32_dpp %0, %0, %0 row_ror:4 row_mask:0xf bank_mask:0xf\n\t"
               "v_mov_b32 %1, %0\n\ts_nop 1\n\tv_permlane16_swap_b32 %0, %1\n\tv_max_f32 %0, %0, %1\n\tv_mov_b32 %1, %0\n\ts_nop 1\n\tv_permlane32_swap_b32 %0, %1\n\tv_max_f32 %0, %0, %1"
               : "+v"(x), "=&v"(y));
  return x;
}
__device__ __forceinline__ float rows_max(float x) {
  float y;
  asm volatile("v_mov_b32 %1, %0\n\ts_nop 1\n\tv_permlane16_swap_b32 %0, %1\n\tv_max_f32 %0, %0, %1\n\tv_mov_b32 %1, %0\n\ts_nop 1\n\tv_permlane32_swap_b32 %0, %1\n\tv_max_f32 %0, %0, %1"
               : "+v"(x), "=&v"(y));
  return x;
}
__device__ __forceinline__ float vmax(float a, float b) { float r; asm("v_max_f32 %0, %1, %2" : "=v"(r) : "v"(a), "v"(b)); return r; }
__device__ __forceinline__ void layer_norm(const u32x4& xv, LAS float* xn, int lane) {
  float xf[8];
#pragma unroll
  for (int k = 0; k < 4; ++k) { xf[2 * k] = lo16(xv[k]); xf[2 * k + 1] = hi16(xv[k]); }
  const float live = lane < 56 ? 1.f : 0.f;
  const float mean = wave_sum((((xf[0] + xf[1]) + (xf[2] + xf[3])) + ((xf[4] + xf[5]) + (xf[6] + xf[7]))) * live) * (1.f / D);
  float ss = 0.f;
#pragma unroll
  for (int k = 0; k < 8; ++k) { const float d = xf[k] - mean; ss += d * d; }
  const float var = wave_sum(ss * live) * (1.f / D);
  const float rden = __builtin_amdgcn_rcpf(rbf(__builtin_amdgcn_sqrtf(rbf(rbf(var) + 1e-5f)))) * live, mb = rbf(mean);
#pragma unroll
  for (int k = 0; k < 8; k += 2) {
    const unsigned c = pack_bf16(xf[k] - mb, xf[k + 1] - mb), n = pack_bf16(lo16(c) * rden, hi16(c) * rden);
    xf[k] = lo16(n); xf[k + 1] = hi16(n);
  }
  *(LAS f32x4*)(xn + 8 * lane) = f32x4{xf[0], xf[1], xf[2], xf[3]}; *(LAS f32x4*)(xn + 8 * lane + 4) = f32x4{xf[4], xf[5], xf[6], xf[7]};
}
template <int NY> __device__ __forceinline__ void slice_partials(u32x4 (&w)[14], const LAS float* xn, LAS float* red, int tid) {
  f32x2 v[4] = {{0.f, 0.f}, {0.f, 0.f}, {0.f, 0.f}, {0.f, 0.f}};
  float xr[14];
#pragma unroll
  for (int i = 0; i < 14; ++i) xr[i] = xn[(tid >> 3) + 32 * i];
#pragma unroll
  for (int i = 0; i < 14; ++i) {
    asm volatile("s_waitcnt vmcnt(%1)" : "+v"(w[i]) : "i"(13 - i + NY));
#pragma unroll
    for (int k = 0; k < 4; ++k) v[k] += f32x2{lo16(w[i][k]), hi16(w[i][k])} * xr[i];
    asm volatile("" : "+v"(v[0]), "+v"(v[1]), "+v"(v[2]), "+v"(v[3]));
  }
  const float a0 = swap32_add(v[0][0], v[2][0]), a1 = swap32_add(v[0][1], v[2][1]), a2 = swap32_add(v[1][0], v[3][0]), a3 = swap32_add(v[1][1], v[3][1]);
  const float b0 = swap16_add(a0, a2), b1 = swap16_add(a1, a3);
  const float t1 = b0 + dpp<ROR8>(b0), t2 = b1 + dpp<ROR8>(b1);
  red[(tid & 7) * 8 + ((tid >> 3) & 7)] = (tid & 8) ? t2 : t1;
}

#define LD_NT(dst, voff, sbase, imm) asm volatile("global_load_dwordx4 %0, %1, %2 offset:%3 nt" : "=v"(dst) : "v"(voff), "s"(sbase), "i"(imm))
#define LD1_NT(dst, voff, sbase, imm) asm volatile("global_load_dword %0, %1, %2 offset:%3 nt" : "=v"(dst) : "v"(voff), "s"(sbase), "i"(imm))
#define LD4_SC1(dst, voff, sbase, imm) asm volatile("global_load_dwordx4 %0, %1, %2 offset:%3 sc1" : "=v"(dst) : "v"(voff), "s"(sbase), "i"(imm) : "memory")
#define ST4_SC1(voff, val, sbase) asm volatile("global_store_dwordx4 %0, %1, %2 sc1\n\ts_nop 1" :: "v"(voff), "v"(val), "s"(sbase) : "memory")
#define WAITV(n, reg) asm volatile("s_waitcnt vmcnt(%1)" : "+v"(reg) : "i"(n))
#define VMCNT(n) asm volatile("s_waitcnt vmcnt(%0)" :: "i"(n) : "memory")
__device__ __forceinline__ void repoll2(u32x4& g0, u32x4& g1, unsigned o0, unsigned o1, const char* base, u64 m0, u64 m1) {
  asm volatile("s_mov_b64 exec, %5\n\tglobal_load_dwordx4 %0, %2, %4 sc1\n\ts_mov_b64 exec, %6\n\tglobal_load_dwordx4 %1, %3, %4 sc1\n\ts_mov_b64 exec, -1\n\ts_waitcnt vmcnt(0)"
               : "+v"(g0), "+v"(g1) : "v"(o0), "v"(o1), "s"(base), "s"(m0), "s"(m1) : "memory");
}
__device__ __forceinline__ void wg_barrier() { asm volatile("s_waitcnt lgkmcnt(0)\n\ts_barrier" ::: "memory"); }
__device__ __forceinline__ void lds_st(unsigned addr, unsigned v) { asm volatile("ds_write_b32 %0, %1" :: "v"(addr), "v"(v) : "memory"); }
__device__ __forceinline__ unsigned lds_ld(unsigned addr) {
  unsigned v; asm volatile("ds_read_b32 %0, %1\n\ts_waitcnt lgkmcnt(0)" : "=v"(v) : "v"(addr) : "memory"); return __builtin_amdgcn_readfirstlane(v); }
template <int N> __device__ __forceinline__ void dma(const char* g, unsigned voff, unsigned lds) {
  if constexpr (N == 4)
    asm volatile("s_mov_b32 m0, %2\n\ts_nop 0\n\tglobal_load_lds_dwordx4 %0, %1 nt\n\tglobal_load_lds_dwordx4 %0, %1 offset:1024 nt\n\t"
                 "global_load_lds_dwordx4 %0, %1 offset:2048 nt\n\tglobal_load_lds_dwordx4 %0, %1 offset:3072 nt" :: "v"(voff), "s"(g), "s"(lds) : "memory");
  else
    asm volatile("s_mov_b32 m0, %2\n\ts_nop 0\n\tglobal_load_lds_dwordx4 %0, %1 nt\n\tglobal_load_lds_dwordx4 %0, %1 offset:1024 nt" :: "v"(voff), "s"(g), "s"(lds) : "memory");
}

#define STAMP(i) do {} while (0)
#define DBGVAL(i, v) do {} while (0)

__global__ __launch_bounds__(320) void fused_kernel(const char* __restrict__ kc, const char* __restrict__ vc, char* __restrict__ ws,
                                                    const u16* __restrict__ x, const char* __restrict__ wq, const char* __restrict__ wo, const char* __restrict__ w1,
                                                    const u16* __restrict__ b1, const char* __restrict__ w2, const u16* __restrict__ b2,
                                                    unsigned* __restrict__ out) {
  __shared__ __attribute__((aligned(1024))) char smem[L_END];
  const unsigned lbase = (unsigned)(uintptr_t)(LAS char*)smem;
  const int b = blockIdx.x, tid = threadIdx.x, lane = tid & 63, wave = __builtin_amdgcn_readfirstlane(tid >> 6);
  const int role = b >> 1;
  const bool own_o = ((b ^ (b >> 3)) & 1) == 0, has_mlp = own_o && (role & 3) == 0 && role < 112;
  const bool make_q = !own_o && (role & 1) == 0, own_out = !own_o && role < 112 && (H2NEW || (role & 1));
  const int S = RPR - (own_o ? ((role & 3) == 0 ? T_MLP : T_O) : (make_q ? T_Q : T_X));
  STAMP(0);
  if (__builtin_expect(wave == NC, 1)) {
    __builtin_amdgcn_s_setprio(3);
    const unsigned voff = lane * 16;
    const int q = b >> 3;
    unsigned* ctr = (unsigned*)(ws + WS_CTR) + q * 64;
    int nq = 0, slot = 0, gen = 0;
    constexpr int LAG = INFL / 4 - 1;
    int frs = 0;
    auto put = [&](const char* g, int u) {
      if ((u & 1) == 0 && frs < gen) { while ((int)lds_ld(lbase + L_FREE + slot * 4) < gen) __builtin_amdgcn_s_sleep(1); }
      dma<4>(g, voff, lbase + slot * PIECE + (u & 1) * 4096);
      ++nq;
      if (u & 1) { if (++slot == NSLOT) { slot = 0; ++gen; } }
    };
    auto settle_w = [&](int u, auto w) {
      constexpr int W = decltype(w)::value;
      if (u & 1) {
        unsigned fr;
        asm volatile("ds_read_b32 %0, %1\n\ts_waitcnt vmcnt(%2) lgkmcnt(0)" : "=&v"(fr) : "v"(lbase + L_FREE + slot * 4), "i"(W - 4) : "memory");
        frs = __builtin_amdgcn_readfirstlane(fr);
      } else VMCNT(W - 4);
      lds_st(lbase + L_LANDED, max(nq - (W / 4 - 1), 0));
    };
    auto settle = [&](int u) { settle_w(u, std::integral_constant<int, INFL>{}); };
    unsigned tv = 0;
    auto ticket = [&]() { asm volatile("s_mov_b64 exec, 1\n\tglobal_atomic_add %0, %1, %2, %3 sc0\n\ts_mov_b64 exec, -1" : "=v"(tv) : "v"(0u), "v"(1u), "s"(ctr) : "memory"); };
    size_t voffs = (size_t)b * (1u << 20);
    {
      const char* g = kc + voffs;
      put(g, 0); put(g + 4096, 1);
      STAMP(4);
      if (lane < 8 + NSLOT) lds_st(lbase + L_CTL + lane * 4, lane == 1 ? 0xffffffffu : 0u);
      wg_barrier();
      for (int p = 1; p < 2 * S; ++p) { g += PIECE; put(g, 0); settle(0); put(g + 4096, 1); settle(1); }
      STAMP(5);
      g = vc + voffs;
      for (int p = 0; p < 2 * S - 6; ++p) { put(g, 0); settle(0); put(g + 4096, 1); settle(1); g += PIECE; }
      while (lds_ld(lbase + L_QFLAG) == 0) __builtin_amdgcn_s_sleep(1);
#pragma unroll
      for (int u = 0; u < 8; ++u) { if (u == 8 - INFL / 4) ticket(); put(g + u * 4096, u); settle(u); }
      STAMP(6);
      voffs += (size_t)(S - 1) * (2 * PIECE);
    }
    for (;;) {
      asm volatile("" : "+v"(tv));
      const int k = __builtin_amdgcn_readfirstlane(tv);
      constexpr int A = POOL - 2 * NC2 - NC1, N4 = (A + CH0 - 1) / CH0;
      int cur, sz;
      if (k < N4) { cur = CH0 * k; sz = min(CH0, A - cur); } else if (k < N4 + NC2) { cur = A + 2 * (k - N4); sz = 2; } else { cur = A + 2 * NC2 + (k - N4 - NC2); sz = 1; }
      if (cur >= POOL) break;
      for (; sz > 0; --sz, ++cur) {
        constexpr int C1 = T_MLP + T_Q, C2_ = C1 + T_O + T_X, C3 = C2_ + T_O + T_Q;
        const int pair = (cur >= C1) + (cur >= C2_) + (cur >= C3), c = cur - (cur >= C3 ? C3 : cur >= C2_ ? C2_ : cur >= C1 ? C1 : 0);
        const int to = pair == 0 ? T_MLP : T_O; const bool first = c < to;
        const int reg = 8 * q + 2 * pair + ((first ? q : q + 1) & 1), pos = first ? RPR - to + c : RPR - ((pair & 1) ? T_X : T_Q) + (c - to);
        const size_t off = (size_t)reg * (1u << 20) + (size_t)pos * (2 * PIECE);
        const char* kp = kc + off; const char* vp = vc + voffs;
#pragma unroll
        for (int u = 0; u < 8; ++u) {
          if (u == 8 - INFL / 4 && sz == 1) ticket();
          put(u < 4 ? kp + u * 4096 : vp + (u - 4) * 4096, u);
          settle(u);
        }
        voffs = off;
      }
    }
    lds_st(lbase + L_LANDED + 4, nq);
    STAMP(1); DBGVAL(15, nq + 4);
    slot += 2; if (slot >= NSLOT) { slot -= NSLOT; ++gen; }
    frs = 0;
#pragma unroll
    for (int u = 0; u < 4; ++u) { put(vc + voffs + u * 4096, u); settle(u); }
#pragma unroll
    for (int i = LAG - 1; i >= 0; --i) { VMCNT(4 * i); lds_st(lbase + L_LANDED, nq - i + (i < 4 ? 4 : 0)); }
    STAMP(2);
  } else {
    const int h = b >> 5;
    LAS float* xn = (LAS float*)(smem + L_XN);
    LAS float* red = (LAS float*)(smem + L_RED);
    u32x2 ev, tag;
    auto arrive = [&](unsigned cnt) -> int {
      unsigned arr;
      asm volatile("s_mov_b64 exec, 1\n\tds_add_rtn_u32 %0, %1, %2\n\ts_mov_b64 exec, -1\n\ts_waitcnt lgkmcnt(0)" : "=&v"(arr) : "v"(lbase + cnt), "v"(1u) : "memory");
      return __builtin_amdgcn_readfirstlane(arr);
    };
    auto make_tag = [&]() {
      WAITV(0, ev);
      const u64 e = (u64)(unsigned)__builtin_amdgcn_readfirstlane(ev[1]) << 32 | (unsigned)__builtin_amdgcn_readfirstlane(ev[0]);
      const u64 z = e * 6364136223846793005ull + 1442695040888963407ull;
      tag = u32x2{(unsigned)z, (unsigned)(z >> 32)};
    };
    asm volatile("global_load_dwordx2 %0, %1, %2 sc1" : "=v"(ev) : "v"(0u), "s"(ws + WS_E) : "memory");
    if (make_q) {
      const int qi = role >> 1;
      if (wave == 0 && (qi & 4) == 0) asm volatile("global_store_dword %0, %1, %2 sc1" :: "v"(0u), "v"(0u), "s"(ws + WS_CTR + ((qi >> 3) * 4 + (qi & 3)) * 256) : "memory");
      u32x4 xv, wv[14];
      asm volatile("global_load_dwordx4 %0, %1, %2" : "=v"(xv) : "v"((lane < 56 ? lane : 55) * 16u), "s"(x));
      const unsigned o = (unsigned)(tid >> 3) * 8192u + (tid & 7) * 16u;
#pragma unroll
      for (int i = 0; i < 14; ++i) LD_NT(wv[i], o, wq + qi * 128 + i * (32 * 8192), 0);
      STAMP(4);
      wg_barrier();
      WAITV(14, xv);
      STAMP(7);
      layer_norm(xv, xn, lane);
      STAMP(1);
      slice_partials<0>(wv, xn, red + wave * 64, tid);
      STAMP(2);
      make_tag();
      if (arrive(L_QCNT) == NC - 1 && lane < 16) {
        const LAS f32x4* rq = (const LAS f32x4*)red + lane;
        const f32x4 s = (rq[0] + rq[16]) + (rq[32] + rq[48]);
        const u32x4 g = {pack_bf16(s[0], s[1]), pack_bf16(s[2], s[3]), tag[0], tag[1]};
        ST4_SC1((unsigned)(qi * 256 + lane * 16), g, ws + WS_Q);
      }
      STAMP(3);
    } else {
      wg_barrier();
    }
    bool bad = false;
    unsigned o_pd = (unsigned)(b * PD_STRIDE + tid * 16), o_y = (unsigned)(role * Y_STRIDE + (tid < 224 ? tid : 0) * 16);
    unsigned o_g0 = own_o ? (T3NEW ? (unsigned)((h * 32 + (lane >> 2)) * PD_STRIDE + (role & 15) * 256 + wave * 64 + (lane & 3) * 16)
                                   : (unsigned)((h * 32 + (tid >> 4)) * PD_STRIDE + (role & 15) * 256 + (tid & 15) * 16))
                          : (H2NEW ? (unsigned)((lane >> 1) * Y_STRIDE + (own_out ? role : 0) * 32 + (lane & 1) * 16)
                                   : (unsigned)((tid >> 2) * Y_STRIDE + (own_out ? role >> 1 : 0) * 64 + (tid & 3) * 16));
    unsigned o_g1 = o_g0 + (own_o ? 16 * PD_STRIDE : (H2NEW ? 32 : 64) * Y_STRIDE);
    asm volatile("" : "+v"(o_pd), "+v"(o_y), "+v"(o_g0), "+v"(o_g1));
    const char* gbase = ws + (own_o ? WS_PD : WS_Y);
    f32x2 y = {0.f, 0.f};
    unsigned wow[32];
    unsigned xo = 0, bo = 0;
    const unsigned tc = (tid < 224 ? tid : 223) * 4u;
    if (has_mlp) {
      const int hg = role >> 2;
      u32x4 xv, wv[14]; unsigned b1w, wa[32], wb[32];
      asm volatile("global_load_dwordx4 %0, %1, %2" : "=v"(xv) : "v"((lane < 56 ? lane : 55) * 16u), "s"(x));
      asm volatile("global_load_ushort %0, %1, %2" : "=v"(b1w) : "v"(lane * 2u), "s"(b1 + hg * 64));
      const unsigned o = (unsigned)(tid >> 3) * (F * 2u) + (tid & 7) * 16u;
#pragma unroll
      for (int i = 0; i < 14; ++i) LD_NT(wv[i], o, w1 + hg * 128 + i * (32 * F * 2), 0);
#pragma unroll
      for (int r = 0; r < 32; ++r) LD1_NT(wa[r], tc, w2 + (size_t)(hg * 64 + (r & ~3)) * (D * 2), (r & 3) * (D * 2));
      WAITV(46, xv); WAITV(46, b1w);
      layer_norm(xv, xn, lane);
      slice_partials<32>(wv, xn, red + wave * 64, tid);
      arrive(L_HCNT);
#pragma unroll
      for (int r = 0; r < 28; ++r) LD1_NT(wb[r], tc, w2 + (size_t)(hg * 64 + 32 + (r & ~3)) * (D * 2), (r & 3) * (D * 2));
      while (lds_ld(lbase + L_HCNT) < NC) __builtin_amdgcn_s_sleep(1);
      const float hv = rbf(rbf((red[lane] + red[64 + lane]) + (red[128 + lane] + red[192 + lane])) + lo16(b1w));
      const float u = 0.7978845608028654f * (hv + 0.044715f * hv * hv * hv);
      const float e = __builtin_amdgcn_exp2f(u * 2.885390081777927f);
      const float ge = rbf(hv * (1.f - __builtin_amdgcn_rcpf(e + 1.f)));
#pragma unroll
      for (int r = 0; r < 32; ++r) WAITV(28, wa[r]);
#pragma unroll
      for (int r = 28; r < 32; ++r) LD1_NT(wb[r], tc, w2 + (size_t)(hg * 64 + 32 + (r & ~3)) * (D * 2), (r & 3) * (D * 2));
#pragma unroll
      for (int r = 0; r < 32; ++r) y += f32x2{lo16(wa[r]), hi16(wa[r])} * lane_of(ge, r);
      asm volatile("" : "+v"(y));
#pragma unroll
      for (int r = 0; r < 32; ++r) WAITV(0, wb[r]);
#pragma unroll
      for (int r = 0; r < 32; ++r) y += f32x2{lo16(wb[r]), hi16(wb[r])} * lane_of(ge, 32 + r);
    }
    if (own_o) {
    } else {
      asm volatile("global_load_dword %0, %2, %3\n\tglobal_load_dword %1, %2, %4\n\ts_waitcnt vmcnt(0)"
                   : "=&v"(xo), "=&v"(bo) : "v"((lane & (H2NEW ? 1 : 3)) * 4u), "s"(x + (own_out ? (H2NEW ? role : role & ~1) : 0) * 4), "s"(b2 + (own_out ? (H2NEW ? role : role & ~1) : 0) * 4));
    }
    asm volatile("" : "+v"(y));
    make_tag();
    float m = -INFINITY, macc = -INFINITY, l = 0.f;
    f32x2 a0[4], a1[4];
#pragma unroll
    for (int k = 0; k < 4; ++k) { a0[k] = f32x2{0.f, 0.f}; a1[k] = f32x2{0.f, 0.f}; }
    unsigned landed = 0, endq = 0xffffffffu;
    unsigned pc = wave; int slot = wave, gen = 1;
    u32x4 q0, q1;
    LAS u16* scw = (LAS u16*)(smem + L_REC + wave * 2048) + (lane >> 1);
    auto refresh = [&]() {
      u32x2 c; asm volatile("ds_read_b64 %0, %1\n\ts_waitcnt lgkmcnt(0)" : "=v"(c) : "v"(lbase + L_LANDED) : "memory");
      landed = __builtin_amdgcn_readfirstlane(c[0]); endq = __builtin_amdgcn_readfirstlane(c[1]);
    };
    auto advance = [&](int n) { pc += n; slot += n; if (slot >= NSLOT) { slot -= NSLOT; ++gen; } };
    auto k_read = [&](u32x4 (&R)[8]) {
      const LAS u32x4* kt = (const LAS u32x4*)(smem + slot * PIECE) + lane;
#pragma unroll
      for (int i = 0; i < 8; ++i) R[i] = kt[i * 64];
      lds_st(lbase + L_FREE + slot * 4, gen);
    };
    auto k_scores = [&](const u32x4 (&R)[8], int t) {
      float sc[8];
#pragma unroll
      for (int i = 0; i < 8; ++i) {
        float a = 0.f, c = 0.f;
#pragma unroll
        for (int k = 0; k < 4; ++k) { a = dot2(R[i][k], q0[k], a); c = dot2(R[i][k], q1[k], c); }
        sc[i] = swap16_add(a, c);
      }
      const float s = row_transpose_sum8(sc, 0xccccccccccccccccull);
      const unsigned sb = pack_bf16(s, s);
      scw[t * 32] = (u16)sb;
      m = vmax(m, lo16(sb));
    };
    auto v_step = [&](int t) {
      LAS float* pw = (LAS float*)(smem + L_P) + wave * 32;
      const float p = __builtin_amdgcn_exp2f((lo16(scw[t * 32]) - m) * C2);
      pw[lane >> 1] = p;
      l += p;
      f32x4 pp[4];
#pragma unroll
      for (int k = 0; k < 4; ++k) pp[k] = *(const LAS f32x4*)(pw + (lane >> 5) * 16 + 4 * k);
      const LAS u32x4* vt = (const LAS u32x4*)(smem + slot * PIECE) + lane;
#pragma unroll
      for (int hf = 0; hf < 2; ++hf) {
        while (landed < 2 * pc + 1 + hf) refresh();
        u32x4 R[4];
#pragma unroll
        for (int i = 0; i < 4; ++i) R[i] = vt[(hf * 4 + i) * 64];
        if (hf == 1) lds_st(lbase + L_FREE + slot * 4, gen);
#pragma unroll
        for (int i = 0; i < 4; ++i)
#pragma unroll
          for (int k = 0; k < 4; ++k) { const f32x2 v = {lo16(R[i][k]), hi16(R[i][k])}; a0[k] += v * pp[hf][i]; a1[k] += v * pp[2 + hf][i]; }
        asm volatile("" : "+v"(a0[0]), "+v"(a0[1]), "+v"(a0[2]), "+v"(a0[3]), "+v"(a1[0]), "+v"(a1[1]), "+v"(a1[2]), "+v"(a1[3]));
      }
    };
    {
      u32x4 R0[8], R1[8], R2[8], R3[8];
      while (landed < 2 * pc + 2) refresh();
      k_read(R0); advance(4);
      while (landed < 2 * pc + 2) refresh();
      k_read(R1); advance(4);
      while (landed < 2 * pc + 2) refresh();
      k_read(R2); advance(4);
      if (wave != 0) {
        while (landed < 2 * pc + 2) refresh();
        k_read(R3);
      }
      if (wave == 0) {
        u32x4 g0 = {0u, 0u, ~tag[0], 0u}, g1 = g0;
        const unsigned qo = (unsigned)(h * 2048 + lane * 32);
        unsigned spins = 0;
        for (;;) {
          const u64 miss = __ballot(!(g0[2] == tag[0] && g0[3] == tag[1] && g1[2] == tag[0] && g1[3] == tag[1]));
          if (miss == 0) break;
          if (++spins > SPIN_CAP) { bad = true; break; }
          repoll2(g0, g1, qo, qo + 16, ws + WS_Q, miss, miss);
        }
        *(LAS u32x4*)(smem + L_Q + lane * 16) = u32x4{g0[0], g0[1], g1[0], g1[1]};
        lds_st(lbase + L_QFLAG, 1u);
        DBGVAL(15, spins);
        while (landed < 2 * pc + 2) refresh();
        k_read(R3);
      } else {
        while (lds_ld(lbase + L_QFLAG) == 0) __builtin_amdgcn_s_sleep(1);
      }
      q0 = *(const LAS u32x4*)(smem + L_Q + (lane & 31) * 16); q1 = *(const LAS u32x4*)(smem + L_Q + 512 + (lane & 31) * 16);
      STAMP(5);
      advance(4);
      k_scores(R0, 0); k_scores(R1, 1); k_scores(R2, 2); k_scores(R3, 3);
      STAMP(6);
    }
    for (int t = 4; t < S / 2; ++t) {
      u32x4 R[8];
      while (landed < 2 * pc + 2) refresh();
      k_read(R); advance(4);
      k_scores(R, t);
    }
    m = head_max(m); macc = m;
    if (PRETOUCH && wave == 1) {
      unsigned d0, d1; const unsigned o = (unsigned)((b & ~7) + (lane & 7)) * (1u << 20) + (1u << 20) - 128u;
      asm volatile("global_load_dword %0, %2, %3 nt\n\tglobal_load_dword %1, %2, %4 nt\n\ts_waitcnt vmcnt(0)" : "=&v"(d0), "=&v"(d1) : "v"(o), "s"(kc), "s"(vc));
    }
    f32x2 wof[32];
    const int nv = S / 2 - (wave >> 1);
    for (int t = 0; t < nv; ++t) { v_step(t); advance(4); }
    if (wave >= 2) { advance(2); v_step(S / 2 - 1); advance(2); } else { pc -= 2; slot -= 2; if (slot < 0) { slot += NSLOT; --gen; } }
    bool pend = false;
    for (;;) {
      while (landed < 2 * pc + 2 && endq >= 2 * pc + 2) refresh();
      if (endq < 2 * pc + 2) break;
      u32x4 R[8];
      k_read(R); advance(6);
      k_scores(R, 0);
      m = head_max(m);
      const float alpha = __builtin_amdgcn_exp2f((macc - m) * C2);
      macc = m; l *= alpha;
      const float al0 = lane_of(alpha, 0), al1 = lane_of(alpha, 16);
#pragma unroll
      for (int k = 0; k < 4; ++k) { a0[k] *= al0; a1[k] *= al1; }
      refresh();
      if (endq < 2 * pc + 6) { pend = true; break; }
      v_step(0); advance(2);
    }
    if (own_o) {
#pragma unroll
      for (int r = 0; r < 32; ++r) LD1_NT(wow[r], tc, wo + (size_t)(role * 32 + (r & ~3)) * (D * 2), (r & 3) * (D * 2));
    }
    if (pend) v_step(0);
    STAMP(8);
    LAS float* rec = (LAS float*)(smem + L_REC);
    LAS f32x2* mlr = (LAS f32x2*)(smem + L_ML);
    {
      f32x4 o0, o1;
#pragma unroll
      for (int k = 0; k < 2; ++k) {
        o0[2 * k] = swap32_add(a0[k][0], a1[k][0]); o0[2 * k + 1] = swap32_add(a0[k][1], a1[k][1]);
        o1[2 * k] = swap32_add(a0[2 + k][0], a1[2 + k][0]); o1[2 * k + 1] = swap32_add(a0[2 + k][1], a1[2 + k][1]);
      }
      *(LAS f32x4*)(rec + wave * 512 + 8 * lane) = o0; *(LAS f32x4*)(rec + wave * 512 + 8 * lane + 4) = o1;
      l += dpp<ROR8>(l); l += dpp<HMIR>(l); l += dpp<XOR2>(l); l = swap32_add(l, l);
      if ((lane & 47) == 0) mlr[(lane >> 4) * NC + wave] = f32x2{m, l};
    }
    wg_barrier();
    const unsigned tag16 = tag[0] & 0xffffu;
    {
      const LAS f32x4* mp = (const LAS f32x4*)(mlr + (tid >> 7) * NC);
      const f32x4 m01 = mp[0], m23 = mp[1];
      const LAS f32x2* ap = (const LAS f32x2*)rec + tid;
      const f32x2 v0 = ap[0], v1 = ap[256], v2 = ap[512], v3 = ap[768];
      const float M = vmax(vmax(m01[0], m01[2]), vmax(m23[0], m23[2]));
      const float f0 = __builtin_amdgcn_exp2f((m01[0] - M) * C2), f1 = __builtin_amdgcn_exp2f((m01[2] - M) * C2);
      const float f2 = __builtin_amdgcn_exp2f((m23[0] - M) * C2), f3 = __builtin_amdgcn_exp2f((m23[2] - M) * C2);
      const f32x2 o = (v0 * f0 + v1 * f1) + (v2 * f2 + v3 * f3);
      const unsigned L = __float_as_uint((f0 * m01[1] + f1 * m01[3]) + (f2 * m23[1] + f3 * m23[3]));
      const unsigned pay = (lane & 2) ? L << 16 : ((lane & 1) ? L : __float_as_uint(M)) & 0xffff0000u;
      const u32x4 gd = {__float_as_uint(o[0]), __float_as_uint(o[1]), pay | tag16, tag[1]};
      ST4_SC1(o_pd, gd, ws + WS_PD);
    }
    STAMP(9);
    u32x4 g0, g1;
    if (own_o) {
      LD4_SC1(g0, o_g0, gbase, 0); LD4_SC1(g1, o_g1, gbase, 0);
#pragma unroll
      for (int r = 0; r < 32; ++r) WAITV(3, wow[r]);
#pragma unroll
      for (int r = 0; r < 32; ++r) { wof[r] = f32x2{lo16(wow[r]), hi16(wow[r])}; asm volatile("" : "+v"(wof[r])); }
      WAITV(0, g0); WAITV(0, g1);
      unsigned spins = 0;
      for (;;) {
        const u64 m0 = __ballot(!((g0[2] & 0xffffu) == tag16 && g0[3] == tag[1])), m1 = __ballot(!((g1[2] & 0xffffu) == tag16 && g1[3] == tag[1]));
        if ((m0 | m1) == 0) break;
        if (++spins > SPIN_CAP) { bad = true; break; }
        if (P1SLEEP && __builtin_popcountll(m0) + __builtin_popcountll(m1) > 32) __builtin_amdgcn_s_sleep(P1SLEEP);
        repoll2(g0, g1, o_g0, o_g1, gbase, m0, m1);
      }
      STAMP(10);
      const float ma = hi16(dppu<0x00>(g0[2])), mb = hi16(dppu<0x00>(g1[2]));
      const float Mw = rows_max(vmax(ma, mb));
      const float fa = __builtin_amdgcn_exp2f((ma - Mw) * C2), fb = __builtin_amdgcn_exp2f((mb - Mw) * C2);
      const float la = __uint_as_float((dppu<0x55>(g0[2]) & 0xffff0000u) | (dppu<0xAA>(g0[2]) >> 16));
      const float lb = __uint_as_float((dppu<0x55>(g1[2]) & 0xffff0000u) | (dppu<0xAA>(g1[2]) >> 16));
      const float oe = fa * __uint_as_float(g0[0]) + fb * __uint_as_float(g1[0]), oo = fa * __uint_as_float(g0[1]) + fb * __uint_as_float(g1[1]);
      const float Lp = fa * la + fb * lb;
      const float t = swap32_add(swap16_add(oe, oo), swap16_add(Lp, Lp));
      LAS float* redo = (LAS float*)(smem + L_REDO);
      LAS float* ml2 = (LAS float*)(smem + L_ML2);
      if (lane < 32) redo[(2 * (lane & 15) + (lane >> 4)) * NC + wave] = t;
      if (lane == 32) { ml2[wave] = Mw; ml2[NC + wave] = t; }
      wg_barrier();
      const f32x4 ov = ((const LAS f32x4*)redo)[lane & 31], Mv = ((const LAS f32x4*)ml2)[0], Lv = ((const LAS f32x4*)ml2)[1];
      const float M = vmax(vmax(Mv[0], Mv[1]), vmax(Mv[2], Mv[3]));
      f32x4 fv;
#pragma unroll
      for (int w = 0; w < NC; ++w) fv[w] = __builtin_amdgcn_exp2f((Mv[w] - M) * C2);
      const f32x4 on4 = ov * fv, L4 = Lv * fv;
      const float on = rbf(((on4[0] + on4[1]) + (on4[2] + on4[3])) * __builtin_amdgcn_rcpf((L4[0] + L4[1]) + (L4[2] + L4[3])));
      f32x2 y1 = {0.f, 0.f}, y2 = y1, y3 = y1;
#pragma unroll
      for (int r = 0; r < 32; r += 4) {
        y += wof[r] * lane_of(on, r); y1 += wof[r + 1] * lane_of(on, r + 1); y2 += wof[r + 2] * lane_of(on, r + 2); y3 += wof[r + 3] * lane_of(on, r + 3);
      }
      y = (y + y1) + (y2 + y3);
      if (bad) y[0] = __uint_as_float(0x7fc00000u);
      if (tid < 224) {
        const u32x4 gy = {__float_as_uint(y[0]), __float_as_uint(y[1]), tag[0], tag[1]};
        ST4_SC1(o_y, gy, ws + WS_Y);
      }
      STAMP(11);
    } else if (own_out) {
      g0 = u32x4{0u, 0u, ~tag[0], 0u}; g1 = g0;
      unsigned spins = 0;
      for (;;) {
        const u64 m0 = __ballot(!(g0[2] == tag[0] && g0[3] == tag[1])), m1 = __ballot(!(g1[2] == tag[0] && g1[3] == tag[1]));
        if ((m0 | m1) == 0) break;
        if (++spins > SPIN_CAP) { bad = true; break; }
        const u64 gate = (!GATE || spins == 1 || (m0 & m1) != ~0ull) ? ~0ull : 1ull;
        repoll2(g0, g1, o_g0, o_g1, gbase, m0 & gate, m1 & gate);
      }
      STAMP(12);
      float s0 = __uint_as_float(g0[0]) + __uint_as_float(g1[0]), s1 = __uint_as_float(g0[1]) + __uint_as_float(g1[1]);
      if (bad) s0 = __uint_as_float(0x7fc00000u);
      s0 += dpp<ROR8>(s0); s1 += dpp<ROR8>(s1); s0 += dpp<ROR4>(s0); s1 += dpp<ROR4>(s1);
      float ss = swap16_add(s0, s1); ss = swap32_add(ss, ss);
      LAS float* redy = (LAS float*)(smem + L_REDO);
      if ((lane & 44) == 0) redy[wave * 8 + 2 * (lane & 3) + (lane >> 4)] = ss;
      wg_barrier();
      if (tid < 4) {
        const LAS f32x2* ry = (const LAS f32x2*)redy + tid;
        const f32x2 t = (ry[0] + ry[4]) + (ry[8] + ry[12]);
        out[(role >> 1) * 4 + tid] = pack_bf16(lo16(xo) + t[0] + lo16(bo), hi16(xo) + t[1] + hi16(bo));
        if (role == 1 && tid == 0) *(u32x2*)(ws + WS_E) = tag;
      }
      STAMP(13);
    }
  }
}

extern "C" void kernel_launch(void* const* d_in, const int* in_sizes, int n_in, void* d_out, int out_size, void* d_ws, size_t ws_size, hipStream_t stream) {
  fused_kernel<<<NWG, 320, 0, stream>>>((const char*)d_in[1], (const char*)d_in[2], (char*)d_ws, (const u16*)d_in[0], (const char*)d_in[3], (const char*)d_in[4],
                                        (const char*)d_in[6], (const u16*)d_in[7], (const char*)d_in[8], (const u16*)d_in[9], (unsigned*)d_out);
}
```

```cpp
#include <hip/hip_runtime.h>
#include <hip/hip_bf16.h>
#include <cstdint>

typedef unsigned u32x4 __attribute__((ext_vector_type(4)));
typedef unsigned u32x2 __attribute__((ext_vector_type(2)));
typedef float f32x4 __attribute__((ext_vector_type(4)));
typedef float f32x2 __attribute__((ext_vector_type(2)));
typedef __bf16 bf16x2 __attribute__((ext_vector_type(2)));
typedef unsigned short u16;
typedef unsigned long long u64;
#define LAS __attribute__((address_space(3)))

constexpr int INFL = 24;
constexpr int T_MLP = 6, T_O = 2, T_Q = 4, T_X = 2;
constexpr int CH0 = 4, NC2 = 4, NC1 = 8;
constexpr int D = 448, F = 1792, DH = 256;
constexpr int NWG = 256, NC = 4;
constexpr int PIECE = 8192, NSLOT = 18;
constexpr int RPR = 64;
constexpr int POOL = T_MLP + 3 * T_O + 2 * T_Q + 2 * T_X;
static_assert(POOL >= 2 * NC2 + NC1 && INFL % 4 == 0 && INFL >= 20 && INFL <= 32, "loader structure");
static_assert(T_MLP % 2 == 0 && T_O % 2 == 0 && T_Q % 2 == 0 && T_X % 2 == 0 && T_X >= 2 && T_O >= 2 && T_Q >= 2, "an even number of static rounds, at most 62");
constexpr unsigned PD_STRIDE = 33 * 128, Y_STRIDE = 29 * 128;
constexpr size_t WS_E = 0, WS_CTR = 4096, WS_Q = 16384, WS_PD = 65536, WS_Y = WS_PD + (size_t)NWG * PD_STRIDE;
constexpr unsigned SPIN_CAP = 1u << 22;
constexpr unsigned L_CTL = NSLOT * PIECE;
constexpr unsigned L_LANDED = L_CTL, L_QFLAG = L_CTL + 8, L_QCNT = L_CTL + 12, L_HCNT = L_CTL + 16, L_FREE = L_CTL + 32;
constexpr unsigned L_Q = L_CTL + 128, L_RED = L_Q + 1024, L_REDO = L_RED + 1024, L_ML2 = L_REDO + 512, L_P = L_ML2 + 64;
constexpr unsigned L_ML = L_P + NC * 128, L_XN = L_ML + 64, L_REC = L_XN + 2048, L_END = L_REC + NC * 2 * DH * 4;
static_assert(L_END <= 160 * 1024 && D <= 2 * DH, "LDS");
constexpr float C2 = 0.0625f * 1.4426950408889634f;

__device__ __forceinline__ float lo16(unsigned w) { return __uint_as_float(w << 16); }
__device__ __forceinline__ float hi16(unsigned w) { return __uint_as_float(w & 0xffff0000u); }
__device__ __forceinline__ unsigned pack_bf16(float a, float b) {
  bf16x2 r = {(__bf16)a, (__bf16)b};
  return __builtin_bit_cast(unsigned, r);
}
__device__ __forceinline__ float rbf(float x) { return lo16(pack_bf16(x, x)); }
__device__ __forceinline__ float dot2(unsigned k, unsigned q, float acc) {
  return __builtin_amdgcn_fdot2_f32_bf16(__builtin_bit_cast(bf16x2, k), __builtin_bit_cast(bf16x2, q), acc, false);
}
template <int CTRL> __device__ __forceinline__ unsigned dppu(unsigned x) { return (unsigned)__builtin_amdgcn_mov_dpp((int)x, CTRL, 0xf, 0xf, true); }
template <int CTRL> __device__ __forceinline__ float dpp(float x) { return __uint_as_float(dppu<CTRL>(__float_as_uint(x))); }
constexpr int XOR1 = 0xB1, XOR2 = 0x4E, HMIR = 0x141, MIR = 0x140, ROR8 = 0x128, ROR4 = 0x124;
__device__ __forceinline__ float swap16_add(float a, float b) {
  auto r = __builtin_amdgcn_permlane16_swap(__float_as_uint(a), __float_as_uint(b), false, false);
  return __uint_as_float(r[0]) + __uint_as_float(r[1]);
}
__device__ __forceinline__ float swap32_add(float a, float b) {
  auto r = __builtin_amdgcn_permlane32_swap(__float_as_uint(a), __float_as_uint(b), false, false);
  return __uint_as_float(r[0]) + __uint_as_float(r[1]);
}
__device__ __forceinline__ float row_sum16(float x) { x += dpp<XOR1>(x); x += dpp<XOR2>(x); x += dpp<HMIR>(x); x += dpp<MIR>(x); return x; }
__device__ __forceinline__ float wave_sum(float x) { x = row_sum16(x); x = swap16_add(x, x); return swap32_add(x, x); }
__device__ __forceinline__ float lane_of(float x, int l) { return __builtin_bit_cast(float, __builtin_amdgcn_readlane(__builtin_bit_cast(int, x), l)); }
__device__ __forceinline__ float row_transpose_sum8(const float (&v)[8], u64 bit1) {
  float t0, t1, t2, t3, r;
  asm volatile("s_nop 1\n\t"
      "v_add_f32_dpp %0, %5, %5 row_ror:8 row_mask:0xf bank_mask:0xf\n\tv_add_f32_dpp %1, %6, %6 row_ror:8 row_mask:0xf bank_mask:0xf\n\t"
      "v_add_f32_dpp %2, %7, %7 row_ror:8 row_mask:0xf bank_mask:0xf\n\tv_add_f32_dpp %3, %8, %8 row_ror:8 row_mask:0xf bank_mask:0xf\n\t"
      "v_add_f32_dpp %0, %9, %9 row_ror:8 row_mask:0xf bank_mask:0xc\n\tv_add_f32_dpp %1, %10, %10 row_ror:8 row_mask:0xf bank_mask:0xc\n\t"
      "v_add_f32_dpp %2, %11, %11 row_ror:8 row_mask:0xf bank_mask:0xc\n\tv_add_f32_dpp %3, %12, %12 row_ror:8 row_mask:0xf bank_mask:0xc\n\t"
      "v_add_f32_dpp %0, %0, %0 row_half_mirror row_mask:0xf bank_mask:0x5\n\tv_add_f32_dpp %1, %1, %1 row_half_mirror row_mask:0xf bank_mask:0x5\n\t"
      "v_add_f32_dpp %0, %2, %2 row_half_mirror row_mask:0xf bank_mask:0xa\n\tv_add_f32_dpp %1, %3, %3 row_half_mirror row_mask:0xf bank_mask:0xa\n\t"
      "s_nop 1\n\t"
      "v_add_f32_dpp %2, %0, %0 quad_perm:[2,3,0,1] row_mask:0xf bank_mask:0xf\n\tv_add_f32_dpp %3, %1, %1 quad_perm:[2,3,0,1] row_mask:0xf bank_mask:0xf\n\t"
      "v_cndmask_b32 %2, %2, %3, %13\n\ts_nop 1\n\t"
      "v_add_f32_dpp %4, %2, %2 quad_perm:[1,0,3,2] row_mask:0xf bank_mask:0xf"
      : "=&v"(t0), "=&v"(t1), "=&v"(t2), "=&v"(t3), "=&v"(r)
      : "v"(v[0]), "v"(v[1]), "v"(v[2]), "v"(v[3]), "v"(v[4]), "v"(v[5]), "v"(v[6]), "v"(v[7]), "s"(bit1));
  return r;
}
__device__ __forceinline__ float head_max(float x) {
  float y;
  asm volatile("s_nop 1\n\tv_max_f32_dpp %0, %0, %0 row_ror:8 row_mask:0xf bank_mask:0xf\n\ts_nop 1\n\tv_max_f32_dpp %0, %0, %0 row_half_mirror row_mask:0xf bank_mask:0xf\n\t"
               "s_nop 1\n\tv_max_f32_dpp %0, %0, %0 quad_perm:[2,3,0,1] row_mask:0xf bank_mask:0xf\n\tv_mov_b32 %1, %0\n\ts_nop 1\n\tv_permlane32_swap_b32 %0, %1\n\tv_max_f32 %0, %0, %1"
               : "+v"(x), "=&v"(y));
  return x;
}
__device__ __forceinline__ float rows_max(float x) {
  float y;
  asm volatile("v_mov_b32 %1, %0\n\ts_nop 1\n\tv_permlane16_swap_b32 %0, %1\n\tv_max_f32 %0, %0, %1\n\tv_mov_b32 %1, %0\n\ts_nop 1\n\tv_permlane32_swap_b32 %0, %1\n\tv_max_f32 %0, %0, %1"
               : "+v"(x), "=&v"(y));
  return x;
}
__device__ __forceinline__ float vmax(float a, float b) { float r; asm("v_max_f32 %0, %1, %2" : "=v"(r) : "v"(a), "v"(b)); return r; }
__device__ __forceinline__ void layer_norm(const u32x4& xv, LAS float* xn, int lane) {
  float xf[8];
#pragma unroll
  for (int k = 0; k < 4; ++k) { xf[2 * k] = lo16(xv[k]); xf[2 * k + 1] = hi16(xv[k]); }
  const float live = lane < 56 ? 1.f : 0.f;
  const float mean = wave_sum((((xf[0] + xf[1]) + (xf[2] + xf[3])) + ((xf[4] + xf[5]) + (xf[6] + xf[7]))) * live) * (1.f / D);
  float ss = 0.f;
#pragma unroll
  for (int k = 0; k < 8; ++k) { const float d = xf[k] - mean; ss += d * d; }
  const float var = wave_sum(ss * live) * (1.f / D);
  const float rden = __builtin_amdgcn_rcpf(rbf(__builtin_amdgcn_sqrtf(rbf(rbf(var) + 1e-5f)))) * live, mb = rbf(mean);
#pragma unroll
  for (int k = 0; k < 8; k += 2) {
    const unsigned c = pack_bf16(xf[k] - mb, xf[k + 1] - mb), n = pack_bf16(lo16(c) * rden, hi16(c) * rden);
    xf[k] = lo16(n); xf[k + 1] = hi16(n);
  }
  *(LAS f32x4*)(xn + 8 * lane) = f32x4{xf[0], xf[1], xf[2], xf[3]}; *(LAS f32x4*)(xn + 8 * lane + 4) = f32x4{xf[4], xf[5], xf[6], xf[7]};
}
template <int NY> __device__ __forceinline__ void slice_partials(u32x4 (&w)[14], const LAS float* xn, LAS float* red, int tid) {
  f32x2 v[4] = {{0.f, 0.f}, {0.f, 0.f}, {0.f, 0.f}, {0.f, 0.f}};
  float xr[14];
#pragma unroll
  for (int i = 0; i < 14; ++i) xr[i] = xn[(tid >> 3) + 32 * i];
#pragma unroll
  for (int i = 0; i < 14; ++i) {
    asm volatile("s_waitcnt vmcnt(%1)" : "+v"(w[i]) : "i"(13 - i + NY));
#pragma unroll
    for (int k = 0; k < 4; ++k) v[k] += f32x2{lo16(w[i][k]), hi16(w[i][k])} * xr[i];
    asm volatile("" : "+v"(v[0]), "+v"(v[1]), "+v"(v[2]), "+v"(v[3]));
  }
  const float a0 = swap32_add(v[0][0], v[2][0]), a1 = swap32_add(v[0][1], v[2][1]), a2 = swap32_add(v[1][0], v[3][0]), a3 = swap32_add(v[1][1], v[3][1]);
  const float b0 = swap16_add(a0, a2), b1 = swap16_add(a1, a3);
  const float t1 = b0 + dpp<ROR8>(b0), t2 = b1 + dpp<ROR8>(b1);
  red[(tid & 7) * 8 + ((tid >> 3) & 7)] = (tid & 8) ? t2 : t1;
}

#define LD_NT(dst, voff, sbase, imm) asm volatile("global_load_dwordx4 %0, %1, %2 offset:%3 nt" : "=v"(dst) : "v"(voff), "s"(sbase), "i"(imm))
#define LD1_NT(dst, voff, sbase, imm) asm volatile("global_load_dword %0, %1, %2 offset:%3 nt" : "=v"(dst) : "v"(voff), "s"(sbase), "i"(imm))
#define LD4_SC1(dst, voff, sbase, imm) asm volatile("global_load_dwordx4 %0, %1, %2 offset:%3 sc1" : "=v"(dst) : "v"(voff), "s"(sbase), "i"(imm) : "memory")
#define ST4_SC1(voff, val, sbase) asm volatile("global_store_dwordx4 %0, %1, %2 sc1\n\ts_nop 1" :: "v"(voff), "v"(val), "s"(sbase) : "memory")
#define WAITV(n, reg) asm volatile("s_waitcnt vmcnt(%1)" : "+v"(reg) : "i"(n))
#define VMCNT(n) asm volatile("s_waitcnt vmcnt(%0)" :: "i"(n) : "memory")
__device__ __forceinline__ void repoll2(u32x4& g0, u32x4& g1, unsigned o0, unsigned o1, const char* base, u64 m0, u64 m1) {
  asm volatile("s_mov_b64 exec, %5\n\tglobal_load_dwordx4 %0, %2, %4 sc1\n\ts_mov_b64 exec, %6\n\tglobal_load_dwordx4 %1, %3, %4 sc1\n\ts_mov_b64 exec, -1\n\ts_waitcnt vmcnt(0)"
               : "+v"(g0), "+v"(g1) : "v"(o0), "v"(o1), "s"(base), "s"(m0), "s"(m1) : "memory");
}
__device__ __forceinline__ void wg_barrier() { asm volatile("s_waitcnt lgkmcnt(0)\n\ts_barrier" ::: "memory"); }
__device__ __forceinline__ void lds_st(unsigned addr, unsigned v) { asm volatile("ds_write_b32 %0, %1" :: "v"(addr), "v"(v) : "memory"); }
__device__ __forceinline__ unsigned lds_ld(unsigned addr) {
  unsigned v; asm volatile("ds_read_b32 %0, %1\n\ts_waitcnt lgkmcnt(0)" : "=v"(v) : "v"(addr) : "memory"); return __builtin_amdgcn_readfirstlane(v); }
__device__ __forceinline__ void dma4(const char* g, unsigned voff, unsigned lds) {
  asm volatile("s_mov_b32 m0, %2\n\ts_nop 0\n\tglobal_load_lds_dwordx4 %0, %1 nt\n\tglobal_load_lds_dwordx4 %0, %1 offset:1024 nt\n\t"
               "global_load_lds_dwordx4 %0, %1 offset:2048 nt\n\tglobal_load_lds_dwordx4 %0, %1 offset:3072 nt" :: "v"(voff), "s"(g), "s"(lds) : "memory");
}

__global__ __launch_bounds__(320, 2) void fused_kernel(const char* __restrict__ kc, const char* __restrict__ vc, char* __restrict__ ws,
                                                    const u16* __restrict__ x, const char* __restrict__ wq, const char* __restrict__ wo, const char* __restrict__ w1,
                                                    const u16* __restrict__ b1, const char* __restrict__ w2, const u16* __restrict__ b2,
                                                    unsigned* __restrict__ out) {
  __shared__ __attribute__((aligned(1024))) char smem[L_END];
  const unsigned lbase = (unsigned)(uintptr_t)(LAS char*)smem;
  const int b = blockIdx.x, tid = threadIdx.x, lane = tid & 63, wave = __builtin_amdgcn_readfirstlane(tid >> 6);
  const int role = b >> 1;
  const bool own_o = ((b ^ (b >> 3)) & 1) == 0, has_mlp = own_o && (role & 3) == 0 && role < 112;
  const bool make_q = !own_o && (role & 1) == 0, own_out = !own_o && (role & 1) == 1 && role < 112;
  const int S = RPR - (own_o ? ((role & 3) == 0 ? T_MLP : T_O) : (make_q ? T_Q : T_X));

  if (__builtin_expect(wave == NC, 1)) {
    __builtin_amdgcn_s_setprio(3);
    const unsigned voff = lane * 16;
    const int q = b >> 3;
    unsigned* ctr = (unsigned*)(ws + WS_CTR) + q * 64;
    int nq = 0, slot = 0, gen = 0;
    constexpr int LAG = INFL / 4 - 1;
    int frs = 0;
    auto put = [&](const char* g, int u) {
      if ((u & 1) == 0 && frs < gen) { while ((int)lds_ld(lbase + L_FREE + slot * 4) < gen) __builtin_amdgcn_s_sleep(1); }
      dma4(g, voff, lbase + slot * PIECE + (u & 1) * 4096);
      ++nq;
      if (u & 1) { if (++slot == NSLOT) { slot = 0; ++gen; } }
    };
    auto settle = [&](int u) {
      if (u & 1) {
        unsigned fr;
        asm volatile("ds_read_b32 %0, %1\n\ts_waitcnt vmcnt(%2) lgkmcnt(0)" : "=&v"(fr) : "v"(lbase + L_FREE + slot * 4), "i"(INFL - 4) : "memory");
        frs = __builtin_amdgcn_readfirstlane(fr);
      } else VMCNT(INFL - 4);
      lds_st(lbase + L_LANDED, nq - LAG);
    };
    auto piece = [&](const char* g) {
      put(g, 0); put(g + 4096, 1);
      unsigned fr;
      asm volatile("ds_read_b32 %0, %1\n\ts_waitcnt vmcnt(%2) lgkmcnt(0)" : "=&v"(fr) : "v"(lbase + L_FREE + slot * 4), "i"(INFL - 8) : "memory");
      frs = __builtin_amdgcn_readfirstlane(fr);
      lds_st(lbase + L_LANDED, max(nq - (LAG - 1), 0));
    };
    unsigned tv = 0;
    auto ticket = [&]() { asm volatile("s_mov_b64 exec, 1\n\tglobal_atomic_add %0, %1, %2, %3 sc0\n\ts_mov_b64 exec, -1" : "=v"(tv) : "v"(0u), "v"(1u), "s"(ctr) : "memory"); };
    size_t voffs = (size_t)b * (1u << 20);
    {
      const char* g = kc + voffs;
      put(g, 0); put(g + 4096, 1);
      if (lane < 8 + NSLOT) lds_st(lbase + L_CTL + lane * 4, lane == 1 ? 0xffffffffu : 0u);
      wg_barrier();
      for (int p = 1; p < 2 * S; ++p) { g += PIECE; piece(g); }

      g = vc + voffs;
      for (int p = 0; p < 2 * S - 6; ++p) { piece(g); g += PIECE; }
      while (lds_ld(lbase + L_QFLAG) == 0) __builtin_amdgcn_s_sleep(1);
#pragma unroll
      for (int u = 0; u < 8; ++u) { if (u == 8 - INFL / 4) ticket(); put(g + u * 4096, u); settle(u); }

      voffs += (size_t)(S - 1) * (2 * PIECE);
    }
    for (;;) {
      asm volatile("" : "+v"(tv));
      const int k = __builtin_amdgcn_readfirstlane(tv);
      constexpr int A = POOL - 2 * NC2 - NC1, N4 = (A + CH0 - 1) / CH0;
      int cur, sz;
      if (k < N4) { cur = CH0 * k; sz = min(CH0, A - cur); } else if (k < N4 + NC2) { cur = A + 2 * (k - N4); sz = 2; } else { cur = A + 2 * NC2 + (k - N4 - NC2); sz = 1; }
      if (cur >= POOL) break;
      for (; sz > 0; --sz, ++cur) {
        constexpr int C1 = T_MLP + T_Q, C2_ = C1 + T_O + T_X, C3 = C2_ + T_O + T_Q;
        const int pair = (cur >= C1) + (cur >= C2_) + (cur >= C3), c = cur - (cur >= C3 ? C3 : cur >= C2_ ? C2_ : cur >= C1 ? C1 : 0);
        const int to = pair == 0 ? T_MLP : T_O; const bool first = c < to;
        const int reg = 8 * q + 2 * pair + ((first ? q : q + 1) & 1), pos = first ? RPR - to + c : RPR - ((pair & 1) ? T_X : T_Q) + (c - to);
        const size_t off = (size_t)reg * (1u << 20) + (size_t)pos * (2 * PIECE);
        const char* kp = kc + off; const char* vp = vc + voffs;
#pragma unroll
        for (int u = 0; u < 8; ++u) {
          if (u == 8 - INFL / 4 && sz == 1) ticket();
          put(u < 4 ? kp + u * 4096 : vp + (u - 4) * 4096, u);
          settle(u);
        }
        voffs = off;
      }
    }
    lds_st(lbase + L_LANDED + 4, nq);

    slot += 2; if (slot >= NSLOT) { slot -= NSLOT; ++gen; }
    frs = 0;
#pragma unroll
    for (int u = 0; u < 4; ++u) { put(vc + voffs + u * 4096, u); settle(u); }
#pragma unroll
    for (int i = LAG - 1; i >= 0; --i) { VMCNT(4 * i); lds_st(lbase + L_LANDED, nq - i + (i < 4 ? 4 : 0)); }

  } else {
    const int h = b >> 5;
    LAS float* xn = (LAS float*)(smem + L_XN);
    LAS float* red = (LAS float*)(smem + L_RED);
    u32x2 ev, tag;
    auto arrive = [&](unsigned cnt) -> int {
      unsigned arr;
      asm volatile("s_mov_b64 exec, 1\n\tds_add_rtn_u32 %0, %1, %2\n\ts_mov_b64 exec, -1\n\ts_waitcnt lgkmcnt(0)" : "=&v"(arr) : "v"(lbase + cnt), "v"(1u) : "memory");
      return __builtin_amdgcn_readfirstlane(arr);
    };
    auto make_tag = [&]() {
      WAITV(0, ev);
      const u64 e = (u64)(unsigned)__builtin_amdgcn_readfirstlane(ev[1]) << 32 | (unsigned)__builtin_amdgcn_readfirstlane(ev[0]);
      const u64 z = e * 6364136223846793005ull + 1442695040888963407ull;
      tag = u32x2{(unsigned)z, (unsigned)(z >> 32)};
    };
    asm volatile("global_load_dwordx2 %0, %1, %2 sc1" : "=v"(ev) : "v"(0u), "s"(ws + WS_E) : "memory");
    if (make_q) {
      const int qi = role >> 1;
      if (wave == 0 && (qi & 4) == 0) asm volatile("global_store_dword %0, %1, %2 sc1" :: "v"(0u), "v"(0u), "s"(ws + WS_CTR + ((qi >> 3) * 4 + (qi & 3)) * 256) : "memory");
      u32x4 xv, wv[14];
      asm volatile("global_load_dwordx4 %0, %1, %2" : "=v"(xv) : "v"((lane < 56 ? lane : 55) * 16u), "s"(x));
      const unsigned o = (unsigned)(tid >> 3) * 8192u + (tid & 7) * 16u;
#pragma unroll
      for (int i = 0; i < 14; ++i) LD_NT(wv[i], o, wq + qi * 128 + i * (32 * 8192), 0);
      wg_barrier();
      WAITV(14, xv);
      layer_norm(xv, xn, lane);
      slice_partials<0>(wv, xn, red + wave * 64, tid);
      make_tag();
      if (arrive(L_QCNT) == NC - 1 && lane < 16) {
        const LAS f32x4* rq = (const LAS f32x4*)red + lane;
        const f32x4 s = (rq[0] + rq[16]) + (rq[32] + rq[48]);
        const u32x4 g = {pack_bf16(s[0], s[1]), pack_bf16(s[2], s[3]), tag[0], tag[1]};
        ST4_SC1((unsigned)(qi * 256 + lane * 16), g, ws + WS_Q);
      }
    } else {
      wg_barrier();
    }
    bool bad = false;
    unsigned o_pd = (unsigned)(b * PD_STRIDE + tid * 16), o_y = (unsigned)(role * Y_STRIDE + (tid < 224 ? tid : 0) * 16);
    unsigned o_g0 = own_o ? (unsigned)((h * 32 + (tid >> 4)) * PD_STRIDE + (role & 15) * 256 + (tid & 15) * 16)
                          : (unsigned)((tid >> 2) * Y_STRIDE + (own_out ? role >> 1 : 0) * 64 + (tid & 3) * 16);
    unsigned o_g1 = o_g0 + (own_o ? 16 * PD_STRIDE : 64 * Y_STRIDE);
    asm volatile("" : "+v"(o_pd), "+v"(o_y), "+v"(o_g0), "+v"(o_g1));
    const char* gbase = ws + (own_o ? WS_PD : WS_Y);
    f32x2 y = {0.f, 0.f};
    unsigned xo = 0, bo = 0;
    const unsigned tc = (tid < 224 ? tid : 223) * 4u;
    if (has_mlp) {
      const int hg = role >> 2;
      u32x4 xv, wv[14]; unsigned b1w, wa[32], wb[32];
      asm volatile("global_load_dwordx4 %0, %1, %2" : "=v"(xv) : "v"((lane < 56 ? lane : 55) * 16u), "s"(x));
      asm volatile("global_load_ushort %0, %1, %2" : "=v"(b1w) : "v"(lane * 2u), "s"(b1 + hg * 64));
      const unsigned o = (unsigned)(tid >> 3) * (F * 2u) + (tid & 7) * 16u;
#pragma unroll
      for (int i = 0; i < 14; ++i) LD_NT(wv[i], o, w1 + hg * 128 + i * (32 * F * 2), 0);
#pragma unroll
      for (int r = 0; r < 32; ++r) LD1_NT(wa[r], tc, w2 + (size_t)(hg * 64 + (r & ~3)) * (D * 2), (r & 3) * (D * 2));
      WAITV(46, xv); WAITV(46, b1w);
      layer_norm(xv, xn, lane);
      slice_partials<32>(wv, xn, red + wave * 64, tid);
      arrive(L_HCNT);
#pragma unroll
      for (int r = 0; r < 28; ++r) LD1_NT(wb[r], tc, w2 + (size_t)(hg * 64 + 32 + (r & ~3)) * (D * 2), (r & 3) * (D * 2));
      while (lds_ld(lbase + L_HCNT) < NC) __builtin_amdgcn_s_sleep(1);
      const float hv = rbf(rbf((red[lane] + red[64 + lane]) + (red[128 + lane] + red[192 + lane])) + lo16(b1w));
      const float u = 0.7978845608028654f * (hv + 0.044715f * hv * hv * hv);
      const float e = __builtin_amdgcn_exp2f(u * -2.885390081777927f);
      const float ge = rbf(hv * __builtin_amdgcn_rcpf(1.f + e));
#pragma unroll
      for (int r = 0; r < 32; ++r) WAITV(28, wa[r]);
#pragma unroll
      for (int r = 28; r < 32; ++r) LD1_NT(wb[r], tc, w2 + (size_t)(hg * 64 + 32 + (r & ~3)) * (D * 2), (r & 3) * (D * 2));
#pragma unroll
      for (int r = 0; r < 32; ++r) y += f32x2{lo16(wa[r]), hi16(wa[r])} * lane_of(ge, r);
      asm volatile("" : "+v"(y));
#pragma unroll
      for (int r = 0; r < 32; ++r) WAITV(0, wb[r]);
#pragma unroll
      for (int r = 0; r < 32; ++r) y += f32x2{lo16(wb[r]), hi16(wb[r])} * lane_of(ge, 32 + r);
    }
    if (!own_o) {
      asm volatile("global_load_dword %0, %2, %3\n\tglobal_load_dword %1, %2, %4\n\ts_waitcnt vmcnt(0)"
                   : "=&v"(xo), "=&v"(bo) : "v"((lane & 3) * 4u), "s"(x + (own_out ? role >> 1 : 0) * 8), "s"(b2 + (own_out ? role >> 1 : 0) * 8));
    }
    asm volatile("" : "+v"(y));
    make_tag();
    float m = -INFINITY, macc = -INFINITY, l = 0.f;
    f32x2 a0[4], a1[4];
#pragma unroll
    for (int k = 0; k < 4; ++k) { a0[k] = f32x2{0.f, 0.f}; a1[k] = f32x2{0.f, 0.f}; }
    unsigned landed = 0, endq = 0xffffffffu;
    unsigned pc = wave; int slot = wave, gen = 1;
    u32x4 q0, q1;
    LAS u16* scw = (LAS u16*)(smem + L_REC + wave * 2048) + (lane >> 1);
    auto refresh = [&]() {
      u32x2 c; asm volatile("ds_read_b64 %0, %1\n\ts_waitcnt lgkmcnt(0)" : "=v"(c) : "v"(lbase + L_LANDED) : "memory");
      landed = __builtin_amdgcn_readfirstlane(c[0]); endq = __builtin_amdgcn_readfirstlane(c[1]);
    };
    auto advance = [&](int n) { pc += n; slot += n; if (slot >= NSLOT) { slot -= NSLOT; ++gen; } };
    auto k_read = [&](u32x4 (&R)[8]) {
      const LAS u32x4* kt = (const LAS u32x4*)(smem + slot * PIECE) + lane;
#pragma unroll
      for (int i = 0; i < 8; ++i) R[i] = kt[i * 64];
      lds_st(lbase + L_FREE + slot * 4, gen);
    };
    auto k_scores = [&](const u32x4 (&R)[8], int t) {
      float sc[8];
#pragma unroll
      for (int i = 0; i < 8; ++i) {
        float a = 0.f, c = 0.f;
#pragma unroll
        for (int k = 0; k < 4; ++k) { a = dot2(R[i][k], q0[k], a); c = dot2(R[i][k], q1[k], c); }
        sc[i] = swap16_add(a, c);
      }
      const float s = row_transpose_sum8(sc, 0xccccccccccccccccull);
      const unsigned sb = pack_bf16(s, s);
      scw[t * 32] = (u16)sb;
      m = vmax(m, lo16(sb));
    };
    auto v_step = [&](int t) {
      LAS float* pw = (LAS float*)(smem + L_P) + wave * 32;
      const float p = __builtin_amdgcn_exp2f((lo16(scw[t * 32]) - m) * C2);
      pw[lane >> 1] = p;
      l += p;
      f32x4 pp[4];
#pragma unroll
      for (int k = 0; k < 4; ++k) pp[k] = *(const LAS f32x4*)(pw + (lane >> 5) * 16 + 4 * k);
      const LAS u32x4* vt = (const LAS u32x4*)(smem + slot * PIECE) + lane;
#pragma unroll
      for (int hf = 0; hf < 2; ++hf) {
        while (landed < 2 * pc + 1 + hf) refresh();
        u32x4 R[4];
#pragma unroll
        for (int i = 0; i < 4; ++i) R[i] = vt[(hf * 4 + i) * 64];
        if (hf == 1) lds_st(lbase + L_FREE + slot * 4, gen);
#pragma unroll
        for (int i = 0; i < 4; ++i)
#pragma unroll
          for (int k = 0; k < 4; ++k) { const f32x2 v = {lo16(R[i][k]), hi16(R[i][k])}; a0[k] += v * pp[hf][i]; a1[k] += v * pp[2 + hf][i]; }
        asm volatile("" : "+v"(a0[0]), "+v"(a0[1]), "+v"(a0[2]), "+v"(a0[3]), "+v"(a1[0]), "+v"(a1[1]), "+v"(a1[2]), "+v"(a1[3]));
      }
    };
    {
      u32x4 R0[8], R1[8], R2[8], R3[8];
      while (landed < 2 * pc + 2) refresh();
      k_read(R0); advance(4);
      while (landed < 2 * pc + 2) refresh();
      k_read(R1); advance(4);
      while (landed < 2 * pc + 2) refresh();
      k_read(R2); advance(4);
      if (wave != 0) {
        while (landed < 2 * pc + 2) refresh();
        k_read(R3);
      }
      if (wave == 0) {
        u32x4 g0 = {0u, 0u, ~tag[0], 0u}, g1 = g0;
        const unsigned qo = (unsigned)(h * 2048 + lane * 32);
        unsigned spins = 0;
        for (;;) {
          const u64 miss = __ballot(!(g0[2] == tag[0] && g0[3] == tag[1] && g1[2] == tag[0] && g1[3] == tag[1]));
          if (miss == 0) break;
          if (++spins > SPIN_CAP) { bad = true; break; }
          repoll2(g0, g1, qo, qo + 16, ws + WS_Q, miss, miss);
        }
        *(LAS u32x4*)(smem + L_Q + lane * 16) = u32x4{g0[0], g0[1], g1[0], g1[1]};
        lds_st(lbase + L_QFLAG, 1u);
        while (landed < 2 * pc + 2) refresh();
        k_read(R3);
      } else {
        while (lds_ld(lbase + L_QFLAG) == 0) __builtin_amdgcn_s_sleep(1);
      }
      q0 = *(const LAS u32x4*)(smem + L_Q + (lane & 31) * 16); q1 = *(const LAS u32x4*)(smem + L_Q + 512 + (lane & 31) * 16);
      advance(4);
      k_scores(R0, 0); k_scores(R1, 1); k_scores(R2, 2); k_scores(R3, 3);
    }
    for (int t = 4; t < S / 2; ++t) {
      u32x4 R[8];
      while (landed < 2 * pc + 2) refresh();
      k_read(R); advance(4);
      k_scores(R, t);
    }
    m = head_max(m); macc = m;
    const int nv = S / 2 - (wave >> 1);
    for (int t = 0; t < nv; ++t) { v_step(t); advance(4); }
    if (wave >= 2) { advance(2); v_step(S / 2 - 1); advance(2); } else { pc -= 2; slot -= 2; if (slot < 0) { slot += NSLOT; --gen; } }
    bool pend = false;
    for (;;) {
      while (landed < 2 * pc + 2 && endq >= 2 * pc + 2) refresh();
      if (endq < 2 * pc + 2) break;
      u32x4 R[8];
      k_read(R); advance(6);
      k_scores(R, 0);
      m = head_max(m);
      const float alpha = __builtin_amdgcn_exp2f((macc - m) * C2);
      macc = m; l *= alpha;
      const float al0 = lane_of(alpha, 0), al1 = lane_of(alpha, 16);
#pragma unroll
      for (int k = 0; k < 4; ++k) { a0[k] *= al0; a1[k] *= al1; }
      refresh();
      if (endq < 2 * pc + 6) { pend = true; break; }
      v_step(0); advance(2);
    }
    unsigned wow[32];
    if (own_o) {
#pragma unroll
      for (int r = 0; r < 32; ++r) LD1_NT(wow[r], tc, wo + (size_t)(role * 32 + (r & ~3)) * (D * 2), (r & 3) * (D * 2));
    }
    if (pend) v_step(0);

    LAS float* rec = (LAS float*)(smem + L_REC);
    LAS f32x2* mlr = (LAS f32x2*)(smem + L_ML);
    {
      f32x4 o0, o1;
#pragma unroll
      for (int k = 0; k < 2; ++k) {
        o0[2 * k] = swap32_add(a0[k][0], a1[k][0]); o0[2 * k + 1] = swap32_add(a0[k][1], a1[k][1]);
        o1[2 * k] = swap32_add(a0[2 + k][0], a1[2 + k][0]); o1[2 * k + 1] = swap32_add(a0[2 + k][1], a1[2 + k][1]);
      }
      *(LAS f32x4*)(rec + wave * 512 + 8 * lane) = o0; *(LAS f32x4*)(rec + wave * 512 + 8 * lane + 4) = o1;
      l += dpp<ROR8>(l); l += dpp<HMIR>(l); l += dpp<XOR2>(l); l = swap32_add(l, l);
      if ((lane & 47) == 0) mlr[(lane >> 4) * NC + wave] = f32x2{m, l};
    }
    wg_barrier();
    const unsigned tag16 = tag[0] & 0xffffu;
    {
      const LAS f32x4* mp = (const LAS f32x4*)(mlr + (tid >> 7) * NC);
      const f32x4 m01 = mp[0], m23 = mp[1];
      const LAS f32x2* ap = (const LAS f32x2*)rec + tid;
      const f32x2 v0 = ap[0], v1 = ap[256], v2 = ap[512], v3 = ap[768];
      const float M = vmax(vmax(m01[0], m01[2]), vmax(m23[0], m23[2]));
      const float f0 = __builtin_amdgcn_exp2f((m01[0] - M) * C2), f1 = __builtin_amdgcn_exp2f((m01[2] - M) * C2);
      const float f2 = __builtin_amdgcn_exp2f((m23[0] - M) * C2), f3 = __builtin_amdgcn_exp2f((m23[2] - M) * C2);
      const f32x2 o = (v0 * f0 + v1 * f1) + (v2 * f2 + v3 * f3);
      const unsigned L = __float_as_uint((f0 * m01[1] + f1 * m01[3]) + (f2 * m23[1] + f3 * m23[3]));
      const unsigned pay = (lane & 2) ? L << 16 : ((lane & 1) ? L : __float_as_uint(M)) & 0xffff0000u;
      const u32x4 gd = {__float_as_uint(o[0]), __float_as_uint(o[1]), pay | tag16, tag[1]};
      ST4_SC1(o_pd, gd, ws + WS_PD);
    }

    u32x4 g0, g1;
    if (own_o) {
      LD4_SC1(g0, o_g0, gbase, 0); LD4_SC1(g1, o_g1, gbase, 0);
      f32x2 wof[32];
#pragma unroll
      for (int r = 0; r < 32; ++r) WAITV(3, wow[r]);
#pragma unroll
      for (int r = 0; r < 32; ++r) { wof[r] = f32x2{lo16(wow[r]), hi16(wow[r])}; asm volatile("" : "+v"(wof[r])); }
      WAITV(0, g0); WAITV(0, g1);
      unsigned spins = 0;
      for (;;) {
        const u64 m0 = __ballot(!((g0[2] & 0xffffu) == tag16 && g0[3] == tag[1])), m1 = __ballot(!((g1[2] & 0xffffu) == tag16 && g1[3] == tag[1]));
        if ((m0 | m1) == 0) break;
        if (++spins > SPIN_CAP) { bad = true; break; }
        repoll2(g0, g1, o_g0, o_g1, gbase, m0, m1);
      }

      const float ma = hi16(dppu<0x00>(g0[2])), mb = hi16(dppu<0x00>(g1[2]));
      const float Mw = rows_max(vmax(ma, mb));
      const float fa = __builtin_amdgcn_exp2f((ma - Mw) * C2), fb = __builtin_amdgcn_exp2f((mb - Mw) * C2);
      const float la = __uint_as_float((dppu<0x55>(g0[2]) & 0xffff0000u) | (dppu<0xAA>(g0[2]) >> 16));
      const float lb = __uint_as_float((dppu<0x55>(g1[2]) & 0xffff0000u) | (dppu<0xAA>(g1[2]) >> 16));
      const float oe = fa * __uint_as_float(g0[0]) + fb * __uint_as_float(g1[0]), oo = fa * __uint_as_float(g0[1]) + fb * __uint_as_float(g1[1]);
      const float Lp = fa * la + fb * lb;
      const float t = swap32_add(swap16_add(oe, oo), swap16_add(Lp, Lp));
      LAS float* redo = (LAS float*)(smem + L_REDO);
      LAS float* ml2 = (LAS float*)(smem + L_ML2);
      if (lane < 32) redo[(2 * (lane & 15) + (lane >> 4)) * NC + wave] = t;
      if (lane == 32) { ml2[wave] = Mw; ml2[NC + wave] = t; }
      wg_barrier();
      const f32x4 ov = ((const LAS f32x4*)redo)[lane & 31], Mv = ((const LAS f32x4*)ml2)[0], Lv = ((const LAS f32x4*)ml2)[1];
      const float M = vmax(vmax(Mv[0], Mv[1]), vmax(Mv[2], Mv[3]));
      f32x4 fv;
#pragma unroll
      for (int w = 0; w < NC; ++w) fv[w] = __builtin_amdgcn_exp2f((Mv[w] - M) * C2);
      const f32x4 on4 = ov * fv, L4 = Lv * fv;
      const float on = rbf(((on4[0] + on4[1]) + (on4[2] + on4[3])) * __builtin_amdgcn_rcpf((L4[0] + L4[1]) + (L4[2] + L4[3])));
      f32x2 y1 = {0.f, 0.f}, y2 = y1, y3 = y1;
#pragma unroll
      for (int r = 0; r < 32; r += 4) {
        y += wof[r] * lane_of(on, r); y1 += wof[r + 1] * lane_of(on, r + 1); y2 += wof[r + 2] * lane_of(on, r + 2); y3 += wof[r + 3] * lane_of(on, r + 3);
      }
      y = (y + y1) + (y2 + y3);
      if (bad) y[0] = __uint_as_float(0x7fc00000u);
      if (tid < 224) {
        const u32x4 gy = {__float_as_uint(y[0]), __float_as_uint(y[1]), tag[0], tag[1]};
        ST4_SC1(o_y, gy, ws + WS_Y);
      }

    } else if (own_out) {
      g0 = u32x4{0u, 0u, ~tag[0], 0u}; g1 = g0;
      unsigned spins = 0;
      for (;;) {
        const u64 m0 = __ballot(!(g0[2] == tag[0] && g0[3] == tag[1])), m1 = __ballot(!(g1[2] == tag[0] && g1[3] == tag[1]));
        if ((m0 | m1) == 0) break;
        if (++spins > SPIN_CAP) { bad = true; break; }
        const u64 gate = (spins == 1 || (m0 & m1) != ~0ull) ? ~0ull : 1ull;
        repoll2(g0, g1, o_g0, o_g1, gbase, m0 & gate, m1 & gate);
      }

      float s0 = __uint_as_float(g0[0]) + __uint_as_float(g1[0]), s1 = __uint_as_float(g0[1]) + __uint_as_float(g1[1]);
      if (bad) s0 = __uint_as_float(0x7fc00000u);
      s0 += dpp<ROR8>(s0); s1 += dpp<ROR8>(s1); s0 += dpp<ROR4>(s0); s1 += dpp<ROR4>(s1);
      float ss = swap16_add(s0, s1); ss = swap32_add(ss, ss);
      LAS float* redy = (LAS float*)(smem + L_REDO);
      if ((lane & 44) == 0) redy[wave * 8 + 2 * (lane & 3) + (lane >> 4)] = ss;
      wg_barrier();
      if (tid < 4) {
        const LAS f32x2* ry = (const LAS f32x2*)redy + tid;
        const f32x2 t = (ry[0] + ry[4]) + (ry[8] + ry[12]);
        out[(role >> 1) * 4 + tid] = pack_bf16(lo16(xo) + t[0] + lo16(bo), hi16(xo) + t[1] + hi16(bo));
        if (role == 1 && tid == 0) *(u32x2*)(ws + WS_E) = tag;
      }

    }
  }
}

extern "C" void kernel_launch(void* const* d_in, const int* in_sizes, int n_in, void* d_out, int out_size, void* d_ws, size_t ws_size, hipStream_t stream) {
  fused_kernel<<<NWG, 320, 0, stream>>>((const char*)d_in[1], (const char*)d_in[2], (char*)d_ws, (const u16*)d_in[0], (const char*)d_in[3], (const char*)d_in[4],
                                        (const char*)d_in[6], (const u16*)d_in[7], (const char*)d_in[8], (const u16*)d_in[9], (unsigned*)d_out);
}
```

```cpp
#define PT 0
#include <hip/hip_runtime.h>
#include <hip/hip_bf16.h>
#include <cstdint>

typedef unsigned u32x4 __attribute__((ext_vector_type(4)));
typedef unsigned u32x2 __attribute__((ext_vector_type(2)));
typedef float f32x4 __attribute__((ext_vector_type(4)));
typedef float f32x2 __attribute__((ext_vector_type(2)));
typedef __bf16 bf16x2 __attribute__((ext_vector_type(2)));
typedef unsigned short u16;
typedef unsigned long long u64;
#define LAS __attribute__((address_space(3)))

#define PTT 20
#define WV 17
constexpr int INFL = 24;
constexpr int T_MLP = 6, T_O = 2, T_Q = 4, T_X = 2;
constexpr int CH0 = 4, NC2 = 4, NC1 = 8;
constexpr int D = 448, F = 1792, DH = 256;
constexpr int NWG = 256, NC = 4;
constexpr int PIECE = 8192, NSLOT = 18;
constexpr int RPR = 64;
constexpr int POOL = T_MLP + 3 * T_O + 2 * T_Q + 2 * T_X;
static_assert(POOL >= 2 * NC2 + NC1 && INFL % 4 == 0 && INFL >= 20 && INFL <= 32, "loader structure");
static_assert(T_MLP % 2 == 0 && T_O % 2 == 0 && T_Q % 2 == 0 && T_X % 2 == 0 && T_X >= 2 && T_O >= 2 && T_Q >= 2, "an even number of static rounds, at most 62");
constexpr unsigned PD_STRIDE = 33 * 128, Y_STRIDE = 29 * 128;
constexpr size_t WS_E = 0, WS_CTR = 4096, WS_Q = 16384, WS_PD = 65536, WS_Y = WS_PD + (size_t)NWG * PD_STRIDE;
constexpr unsigned SPIN_CAP = 1u << 22;
constexpr unsigned L_CTL = NSLOT * PIECE;
constexpr unsigned L_LANDED = L_CTL, L_QFLAG = L_CTL + 8, L_QCNT = L_CTL + 12, L_HCNT = L_CTL + 16, L_FREE = L_CTL + 32;
constexpr unsigned L_Q = L_CTL + 128, L_RED = L_Q + 1024, L_REDO = L_RED + 1024, L_ML2 = L_REDO + 512, L_P = L_ML2 + 64;
constexpr unsigned L_ML = L_P + NC * 128, L_XN = L_ML + 64, L_REC = L_XN + 2048, L_END = L_REC + NC * 2 * DH * 4;
static_assert(L_END <= 160 * 1024 && D <= 2 * DH, "LDS");
constexpr float C2 = 0.0625f * 1.4426950408889634f;

__device__ __forceinline__ float lo16(unsigned w) { return __uint_as_float(w << 16); }
__device__ __forceinline__ float hi16(unsigned w) { return __uint_as_float(w & 0xffff0000u); }
__device__ __forceinline__ unsigned pack_bf16(float a, float b) {
  bf16x2 r = {(__bf16)a, (__bf16)b};
  return __builtin_bit_cast(unsigned, r);
}
__device__ __forceinline__ float rbf(float x) { return lo16(pack_bf16(x, x)); }
__device__ __forceinline__ float dot2(unsigned k, unsigned q, float acc) {
  return __builtin_amdgcn_fdot2_f32_bf16(__builtin_bit_cast(bf16x2, k), __builtin_bit_cast(bf16x2, q), acc, false);
}
template <int CTRL> __device__ __forceinline__ unsigned dppu(unsigned x) { return (unsigned)__builtin_amdgcn_mov_dpp((int)x, CTRL, 0xf, 0xf, true); }
template <int CTRL> __device__ __forceinline__ float dpp(float x) { return __uint_as_float(dppu<CTRL>(__float_as_uint(x))); }
constexpr int XOR1 = 0xB1, XOR2 = 0x4E, HMIR = 0x141, MIR = 0x140, ROR8 = 0x128, ROR4 = 0x124;
__device__ __forceinline__ float swap16_add(float a, float b) {
  auto r = __builtin_amdgcn_permlane16_swap(__float_as_uint(a), __float_as_uint(b), false, false);
  return __uint_as_float(r[0]) + __uint_as_float(r[1]);
}
__device__ __forceinline__ float swap32_add(float a, float b) {
  auto r = __builtin_amdgcn_permlane32_swap(__float_as_uint(a), __float_as_uint(b), false, false);
  return __uint_as_float(r[0]) + __uint_as_float(r[1]);
}
__device__ __forceinline__ float row_sum16(float x) { x += dpp<XOR1>(x); x += dpp<XOR2>(x); x += dpp<HMIR>(x); x += dpp<MIR>(x); return x; }
__device__ __forceinline__ float wave_sum(float x) { x = row_sum16(x); x = swap16_add(x, x); return swap32_add(x, x); }
__device__ __forceinline__ float lane_of(float x, int l) { return __builtin_bit_cast(float, __builtin_amdgcn_readlane(__builtin_bit_cast(int, x), l)); }
__device__ __forceinline__ float row_transpose_sum8(const float (&v)[8], u64 bit1) {
  float t0, t1, t2, t3, r;
  asm volatile("s_nop 1\n\t"
      "v_add_f32_dpp %0, %5, %5 row_ror:8 row_mask:0xf bank_mask:0xf\n\tv_add_f32_dpp %1, %6, %6 row_ror:8 row_mask:0xf bank_mask:0xf\n\t"
      "v_add_f32_dpp %2, %7, %7 row_ror:8 row_mask:0xf bank_mask:0xf\n\tv_add_f32_dpp %3, %8, %8 row_ror:8 row_mask:0xf bank_mask:0xf\n\t"
      "v_add_f32_dpp %0, %9, %9 row_ror:8 row_mask:0xf bank_mask:0xc\n\tv_add_f32_dpp %1, %10, %10 row_ror:8 row_mask:0xf bank_mask:0xc\n\t"
      "v_add_f32_dpp %2, %11, %11 row_ror:8 row_mask:0xf bank_mask:0xc\n\tv_add_f32_dpp %3, %12, %12 row_ror:8 row_mask:0xf bank_mask:0xc\n\t"
      "v_add_f32_dpp %0, %0, %0 row_half_mirror row_mask:0xf bank_mask:0x5\n\tv_add_f32_dpp %1, %1, %1 row_half_mirror row_mask:0xf bank_mask:0x5\n\t"
      "v_add_f32_dpp %0, %2, %2 row_half_mirror row_mask:0xf bank_mask:0xa\n\tv_add_f32_dpp %1, %3, %3 row_half_mirror row_mask:0xf bank_mask:0xa\n\t"
      "s_nop 1\n\t"
      "v_add_f32_dpp %2, %0, %0 quad_perm:[2,3,0,1] row_mask:0xf bank_mask:0xf\n\tv_add_f32_dpp %3, %1, %1 quad_perm:[2,3,0,1] row_mask:0xf bank_mask:0xf\n\t"
      "v_cndmask_b32 %2, %2, %3, %13\n\ts_nop 1\n\t"
      "v_add_f32_dpp %4, %2, %2 quad_perm:[1,0,3,2] row_mask:0xf bank_mask:0xf"
      : "=&v"(t0), "=&v"(t1), "=&v"(t2), "=&v"(t3), "=&v"(r)
      : "v"(v[0]), "v"(v[1]), "v"(v[2]), "v"(v[3]), "v"(v[4]), "v"(v[5]), "v"(v[6]), "v"(v[7]), "s"(bit1));
  return r;
}
__device__ __forceinline__ float head_max(float x) {
  float y;
  asm volatile("s_nop 1\n\tv_max_f32_dpp %0, %0, %0 row_ror:8 row_mask:0xf bank_mask:0xf\n\ts_nop 1\n\tv_max_f32_dpp %0, %0, %0 row_half_mirror row_mask:0xf bank_mask:0xf\n\t"
               "s_nop 1\n\tv_max_f32_dpp %0, %0, %0 quad_perm:[2,3,0,1] row_mask:0xf bank_mask:0xf\n\tv_mov_b32 %1, %0\n\ts_nop 1\n\tv_permlane32_swap_b32 %0, %1\n\tv_max_f32 %0, %0, %1"
               : "+v"(x), "=&v"(y));
  return x;
}
__device__ __forceinline__ float rows_max(float x) {
  float y;
  asm volatile("v_mov_b32 %1, %0\n\ts_nop 1\n\tv_permlane16_swap_b32 %0, %1\n\tv_max_f32 %0, %0, %1\n\tv_mov_b32 %1, %0\n\ts_nop 1\n\tv_permlane32_swap_b32 %0, %1\n\tv_max_f32 %0, %0, %1"
               : "+v"(x), "=&v"(y));
  return x;
}
__device__ __forceinline__ float vmax(float a, float b) { float r; asm("v_max_f32 %0, %1, %2" : "=v"(r) : "v"(a), "v"(b)); return r; }
__device__ __forceinline__ void layer_norm(const u32x4& xv, LAS float* xn, int lane) {
  float xf[8];
#pragma unroll
  for (int k = 0; k < 4; ++k) { xf[2 * k] = lo16(xv[k]); xf[2 * k + 1] = hi16(xv[k]); }
  const float live = lane < 56 ? 1.f : 0.f;
  const float mean = wave_sum((((xf[0] + xf[1]) + (xf[2] + xf[3])) + ((xf[4] + xf[5]) + (xf[6] + xf[7]))) * live) * (1.f / D);
  float ss = 0.f;
#pragma unroll
  for (int k = 0; k < 8; ++k) { const float d = xf[k] - mean; ss += d * d; }
  const float var = wave_sum(ss * live) * (1.f / D);
  const float rden = __builtin_amdgcn_rcpf(rbf(__builtin_amdgcn_sqrtf(rbf(rbf(var) + 1e-5f)))) * live, mb = rbf(mean);
#pragma unroll
  for (int k = 0; k < 8; k += 2) {
    const unsigned c = pack_bf16(xf[k] - mb, xf[k + 1] - mb), n = pack_bf16(lo16(c) * rden, hi16(c) * rden);
    xf[k] = lo16(n); xf[k + 1] = hi16(n);
  }
  *(LAS f32x4*)(xn + 8 * lane) = f32x4{xf[0], xf[1], xf[2], xf[3]}; *(LAS f32x4*)(xn + 8 * lane + 4) = f32x4{xf[4], xf[5], xf[6], xf[7]};
}
template <int NY> __device__ __forceinline__ void slice_partials(u32x4 (&w)[14], const LAS float* xn, LAS float* red, int tid) {
  f32x2 v[4] = {{0.f, 0.f}, {0.f, 0.f}, {0.f, 0.f}, {0.f, 0.f}};
  float xr[14];
#pragma unroll
  for (int i = 0; i < 14; ++i) xr[i] = xn[(tid >> 3) + 32 * i];
#pragma unroll
  for (int i = 0; i < 14; ++i) {
    asm volatile("s_waitcnt vmcnt(%1)" : "+v"(w[i]) : "i"(13 - i + NY));
#pragma unroll
    for (int k = 0; k < 4; ++k) v[k] += f32x2{lo16(w[i][k]), hi16(w[i][k])} * xr[i];
    asm volatile("" : "+v"(v[0]), "+v"(v[1]), "+v"(v[2]), "+v"(v[3]));
  }
  const float a0 = swap32_add(v[0][0], v[2][0]), a1 = swap32_add(v[0][1], v[2][1]), a2 = swap32_add(v[1][0], v[3][0]), a3 = swap32_add(v[1][1], v[3][1]);
  const float b0 = swap16_add(a0, a2), b1 = swap16_add(a1, a3);
  const float t1 = b0 + dpp<ROR8>(b0), t2 = b1 + dpp<ROR8>(b1);
  red[(tid & 7) * 8 + ((tid >> 3) & 7)] = (tid & 8) ? t2 : t1;
}

#define LD_NT(dst, voff, sbase, imm) asm volatile("global_load_dwordx4 %0, %1, %2 offset:%3 nt" : "=v"(dst) : "v"(voff), "s"(sbase), "i"(imm))
#define LD1_NT(dst, voff, sbase, imm) asm volatile("global_load_dword %0, %1, %2 offset:%3 nt" : "=v"(dst) : "v"(voff), "s"(sbase), "i"(imm))
#define LD4_SC1(dst, voff, sbase, imm) asm volatile("global_load_dwordx4 %0, %1, %2 offset:%3 sc1" : "=v"(dst) : "v"(voff), "s"(sbase), "i"(imm) : "memory")
#define ST4_SC1(voff, val, sbase) asm volatile("global_store_dwordx4 %0, %1, %2 sc1\n\ts_nop 1" :: "v"(voff), "v"(val), "s"(sbase) : "memory")
#define WAITV(n, reg) asm volatile("s_waitcnt vmcnt(%1)" : "+v"(reg) : "i"(n))
#define VMCNT(n) asm volatile("s_waitcnt vmcnt(%0)" :: "i"(n) : "memory")
__device__ __forceinline__ void repoll2(u32x4& g0, u32x4& g1, unsigned o0, unsigned o1, const char* base, u64 m0, u64 m1) {
  asm volatile("s_mov_b64 exec, %5\n\tglobal_load_dwordx4 %0, %2, %4 sc1\n\ts_mov_b64 exec, %6\n\tglobal_load_dwordx4 %1, %3, %4 sc1\n\ts_mov_b64 exec, -1\n\ts_waitcnt vmcnt(0)"
               : "+v"(g0), "+v"(g1) : "v"(o0), "v"(o1), "s"(base), "s"(m0), "s"(m1) : "memory");
}
__device__ __forceinline__ void wg_barrier() { asm volatile("s_waitcnt lgkmcnt(0)\n\ts_barrier" ::: "memory"); }
__device__ __forceinline__ void lds_st(unsigned addr, unsigned v) { asm volatile("ds_write_b32 %0, %1" :: "v"(addr), "v"(v) : "memory"); }
__device__ __forceinline__ unsigned lds_ld(unsigned addr) {
  unsigned v; asm volatile("ds_read_b32 %0, %1\n\ts_waitcnt lgkmcnt(0)" : "=v"(v) : "v"(addr) : "memory"); return __builtin_amdgcn_readfirstlane(v); }
__device__ __forceinline__ void dma4(const char* g, unsigned voff, unsigned lds) {
  asm volatile("s_mov_b32 m0, %2\n\ts_nop 0\n\tglobal_load_lds_dwordx4 %0, %1 nt\n\tglobal_load_lds_dwordx4 %0, %1 offset:1024 nt\n\t"
               "global_load_lds_dwordx4 %0, %1 offset:2048 nt\n\tglobal_load_lds_dwordx4 %0, %1 offset:3072 nt" :: "v"(voff), "s"(g), "s"(lds) : "memory");
}

__global__ __launch_bounds__(320, 2) void fused_kernel(const char* __restrict__ kc, const char* __restrict__ vc, char* __restrict__ ws,
                                                    const u16* __restrict__ x, const char* __restrict__ wq, const char* __restrict__ wo, const char* __restrict__ w1,
                                                    const u16* __restrict__ b1, const char* __restrict__ w2, const u16* __restrict__ b2,
                                                    unsigned* __restrict__ out) {
  __shared__ __attribute__((aligned(1024))) char smem[L_END];
  const unsigned lbase = (unsigned)(uintptr_t)(LAS char*)smem;
  const int b = blockIdx.x, tid = threadIdx.x, lane = tid & 63, wave = __builtin_amdgcn_readfirstlane(tid >> 6);
  const int role = b >> 1;
  const bool own_o = ((b ^ (b >> 3)) & 1) == 0, has_mlp = own_o && (role & 3) == 0 && role < 112;
  const bool make_q = !own_o && (role & 1) == 0, own_out = !own_o && (role & 1) == 1 && role < 112;
  const int S = RPR - (own_o ? ((role & 3) == 0 ? T_MLP : T_O) : (make_q ? T_Q : T_X));

  if (__builtin_expect(wave == NC, 1)) {
    __builtin_amdgcn_s_setprio(3);
    const unsigned voff = lane * 16;
    const int q = b >> 3;
    unsigned* ctr = (unsigned*)(ws + WS_CTR) + q * 64;
    int nq = 0, slot = 0, gen = 0;
    constexpr int LAG = INFL / 4 - 1;
    int frs = 0;
    auto put = [&](const char* g, int u) {
      if ((u & 1) == 0 && frs < gen) { while ((int)lds_ld(lbase + L_FREE + slot * 4) < gen) __builtin_amdgcn_s_sleep(1); }
      dma4(g, voff, lbase + slot * PIECE + (u & 1) * 4096);
      ++nq;
      if (u & 1) { if (++slot == NSLOT) { slot = 0; ++gen; } }
    };
    auto settle = [&](int u) {
      if (u & 1) {
        unsigned fr;
        asm volatile("ds_read_b32 %0, %1\n\ts_waitcnt vmcnt(%2) lgkmcnt(0)" : "=&v"(fr) : "v"(lbase + L_FREE + slot * 4), "i"(INFL - 4) : "memory");
        frs = __builtin_amdgcn_readfirstlane(fr);
      } else VMCNT(INFL - 4);
      lds_st(lbase + L_LANDED, nq - LAG);
    };
    auto piece = [&](const char* g) {
      put(g, 0); put(g + 4096, 1);
      unsigned fr;
      asm volatile("ds_read_b32 %0, %1\n\ts_waitcnt vmcnt(%2) lgkmcnt(0)" : "=&v"(fr) : "v"(lbase + L_FREE + slot * 4), "i"(WV) : "memory");
      frs = __builtin_amdgcn_readfirstlane(fr);
      lds_st(lbase + L_LANDED, max(nq - (WV + 3) / 4, 0));
    };
    unsigned tv = 0;
    auto ticket = [&]() { asm volatile("s_mov_b64 exec, 1\n\tglobal_atomic_add %0, %1, %2, %3 sc0\n\ts_mov_b64 exec, -1" : "=v"(tv) : "v"(0u), "v"(1u), "s"(ctr) : "memory"); };
    size_t voffs = (size_t)b * (1u << 20);
    {
      const char* g = kc + voffs;
      put(g, 0); put(g + 4096, 1);
      if (lane < 8 + NSLOT) lds_st(lbase + L_CTL + lane * 4, lane == 1 ? 0xffffffffu : 0u);
      wg_barrier();
      for (int p = 1; p < 2 * S; ++p) { g += PIECE; piece(g); }

      g = vc + voffs;
      for (int p = 0; p < 2 * S - 6; ++p) { piece(g); g += PIECE; }
      while (lds_ld(lbase + L_QFLAG) == 0) __builtin_amdgcn_s_sleep(1);
#pragma unroll
      for (int u = 0; u < 8; ++u) { if (u == 8 - INFL / 4) ticket(); put(g + u * 4096, u); settle(u); }

      voffs += (size_t)(S - 1) * (2 * PIECE);
    }
    for (;;) {
      asm volatile("" : "+v"(tv));
      const int k = __builtin_amdgcn_readfirstlane(tv);
      constexpr int A = POOL - 2 * NC2 - NC1, N4 = (A + CH0 - 1) / CH0;
      int cur, sz;
      if (k < N4) { cur = CH0 * k; sz = min(CH0, A - cur); } else if (k < N4 + NC2) { cur = A + 2 * (k - N4); sz = 2; } else { cur = A + 2 * NC2 + (k - N4 - NC2); sz = 1; }
      if (cur >= POOL) break;
      for (; sz > 0; --sz, ++cur) {
        constexpr int C1 = T_MLP + T_Q, C2_ = C1 + T_O + T_X, C3 = C2_ + T_O + T_Q;
        const int pair = (cur >= C1) + (cur >= C2_) + (cur >= C3), c = cur - (cur >= C3 ? C3 : cur >= C2_ ? C2_ : cur >= C1 ? C1 : 0);
        const int to = pair == 0 ? T_MLP : T_O; const bool first = c < to;
        const int reg = 8 * q + 2 * pair + ((first ? q : q + 1) & 1), pos = first ? RPR - to + c : RPR - ((pair & 1) ? T_X : T_Q) + (c - to);
        const size_t off = (size_t)reg * (1u << 20) + (size_t)pos * (2 * PIECE);
        const char* kp = kc + off; const char* vp = vc + voffs;
#pragma unroll
        for (int u = 0; u < 8; ++u) {
          if (u == 8 - INFL / 4 && sz == 1) ticket();
          put(u < 4 ? kp + u * 4096 : vp + (u - 4) * 4096, u);
          settle(u);
        }
        voffs = off;
      }
    }
    lds_st(lbase + L_LANDED + 4, nq);

    slot += 2; if (slot >= NSLOT) { slot -= NSLOT; ++gen; }
    frs = 0;
#pragma unroll
    for (int u = 0; u < 4; ++u) { put(vc + voffs + u * 4096, u); settle(u); }
#pragma unroll
    for (int i = LAG - 1; i >= 0; --i) { VMCNT(4 * i); lds_st(lbase + L_LANDED, nq - i + (i < 4 ? 4 : 0)); }

  } else {
    const int h = b >> 5;
    LAS float* xn = (LAS float*)(smem + L_XN);
    LAS float* red = (LAS float*)(smem + L_RED);
    u32x2 ev, tag;
    auto arrive = [&](unsigned cnt) -> int {
      unsigned arr;
      asm volatile("s_mov_b64 exec, 1\n\tds_add_rtn_u32 %0, %1, %2\n\ts_mov_b64 exec, -1\n\ts_waitcnt lgkmcnt(0)" : "=&v"(arr) : "v"(lbase + cnt), "v"(1u) : "memory");
      return __builtin_amdgcn_readfirstlane(arr);
    };
    auto make_tag = [&]() {
      WAITV(0, ev);
      const u64 e = (u64)(unsigned)__builtin_amdgcn_readfirstlane(ev[1]) << 32 | (unsigned)__builtin_amdgcn_readfirstlane(ev[0]);
      const u64 z = e * 6364136223846793005ull + 1442695040888963407ull;
      tag = u32x2{(unsigned)z, (unsigned)(z >> 32)};
    };
    asm volatile("global_load_dwordx2 %0, %1, %2 sc1" : "=v"(ev) : "v"(0u), "s"(ws + WS_E) : "memory");
    if (make_q) {
      const int qi = role >> 1;
      if (wave == 0 && (qi & 4) == 0) asm volatile("global_store_dword %0, %1, %2 sc1" :: "v"(0u), "v"(0u), "s"(ws + WS_CTR + ((qi >> 3) * 4 + (qi & 3)) * 256) : "memory");
      u32x4 xv, wv[14];
      asm volatile("global_load_dwordx4 %0, %1, %2" : "=v"(xv) : "v"((lane < 56 ? lane : 55) * 16u), "s"(x));
      const unsigned o = (unsigned)(tid >> 3) * 8192u + (tid & 7) * 16u;
#pragma unroll
      for (int i = 0; i < 14; ++i) LD_NT(wv[i], o, wq + qi * 128 + i * (32 * 8192), 0);
      wg_barrier();
      WAITV(14, xv);
      layer_norm(xv, xn, lane);
      slice_partials<0>(wv, xn, red + wave * 64, tid);
      make_tag();
      if (arrive(L_QCNT) == NC - 1 && lane < 16) {
        const LAS f32x4* rq = (const LAS f32x4*)red + lane;
        const f32x4 s = (rq[0] + rq[16]) + (rq[32] + rq[48]);
        const u32x4 g = {pack_bf16(s[0], s[1]), pack_bf16(s[2], s[3]), tag[0], tag[1]};
        ST4_SC1((unsigned)(qi * 256 + lane * 16), g, ws + WS_Q);
      }
    } else {
      wg_barrier();
    }
    bool bad = false;
    unsigned o_pd = (unsigned)(b * PD_STRIDE + tid * 16), o_y = (unsigned)(role * Y_STRIDE + (tid < 224 ? tid : 0) * 16);
    unsigned o_g0 = own_o ? (unsigned)((h * 32 + (tid >> 4)) * PD_STRIDE + (role & 15) * 256 + (tid & 15) * 16)
                          : (unsigned)((tid >> 2) * Y_STRIDE + (own_out ? role >> 1 : 0) * 64 + (tid & 3) * 16);
    unsigned o_g1 = o_g0 + (own_o ? 16 * PD_STRIDE : 64 * Y_STRIDE);
    asm volatile("" : "+v"(o_pd), "+v"(o_y), "+v"(o_g0), "+v"(o_g1));
    const char* gbase = ws + (own_o ? WS_PD : WS_Y);
    f32x2 y = {0.f, 0.f};
    unsigned xo = 0, bo = 0;
    const unsigned tc = (tid < 224 ? tid : 223) * 4u;
    if (has_mlp) {
      const int hg = role >> 2;
      u32x4 xv, wv[14]; unsigned b1w, wa[32], wb[32];
      asm volatile("global_load_dwordx4 %0, %1, %2" : "=v"(xv) : "v"((lane < 56 ? lane : 55) * 16u), "s"(x));
      asm volatile("global_load_ushort %0, %1, %2" : "=v"(b1w) : "v"(lane * 2u), "s"(b1 + hg * 64));
      const unsigned o = (unsigned)(tid >> 3) * (F * 2u) + (tid & 7) * 16u;
#pragma unroll
      for (int i = 0; i < 14; ++i) LD_NT(wv[i], o, w1 + hg * 128 + i * (32 * F * 2), 0);
#pragma unroll
      for (int r = 0; r < 32; ++r) LD1_NT(wa[r], tc, w2 + (size_t)(hg * 64 + (r & ~3)) * (D * 2), (r & 3) * (D * 2));
      WAITV(46, xv); WAITV(46, b1w);
      layer_norm(xv, xn, lane);
      slice_partials<32>(wv, xn, red + wave * 64, tid);
      arrive(L_HCNT);
#pragma unroll
      for (int r = 0; r < 28; ++r) LD1_NT(wb[r], tc, w2 + (size_t)(hg * 64 + 32 + (r & ~3)) * (D * 2), (r & 3) * (D * 2));
      while (lds_ld(lbase + L_HCNT) < NC) __builtin_amdgcn_s_sleep(1);
      const float hv = rbf(rbf((red[lane] + red[64 + lane]) + (red[128 + lane] + red[192 + lane])) + lo16(b1w));
      const float u = 0.7978845608028654f * (hv + 0.044715f * hv * hv * hv);
      const float e = __builtin_amdgcn_exp2f(u * -2.885390081777927f);
      const float ge = rbf(hv * __builtin_amdgcn_rcpf(1.f + e));
#pragma unroll
      for (int r = 0; r < 32; ++r) WAITV(28, wa[r]);
#pragma unroll
      for (int r = 28; r < 32; ++r) LD1_NT(wb[r], tc, w2 + (size_t)(hg * 64 + 32 + (r & ~3)) * (D * 2), (r & 3) * (D * 2));
#pragma unroll
      for (int r = 0; r < 32; ++r) y += f32x2{lo16(wa[r]), hi16(wa[r])} * lane_of(ge, r);
      asm volatile("" : "+v"(y));
#pragma unroll
      for (int r = 0; r < 32; ++r) WAITV(0, wb[r]);
#pragma unroll
      for (int r = 0; r < 32; ++r) y += f32x2{lo16(wb[r]), hi16(wb[r])} * lane_of(ge, 32 + r);
    }
    if (!own_o) {
      asm volatile("global_load_dword %0, %2, %3\n\tglobal_load_dword %1, %2, %4\n\ts_waitcnt vmcnt(0)"
                   : "=&v"(xo), "=&v"(bo) : "v"((lane & 3) * 4u), "s"(x + (own_out ? role >> 1 : 0) * 8), "s"(b2 + (own_out ? role >> 1 : 0) * 8));
    }
    asm volatile("" : "+v"(y));
    make_tag();
    float m = -INFINITY, macc = -INFINITY, l = 0.f;
    f32x2 a0[4], a1[4];
#pragma unroll
    for (int k = 0; k < 4; ++k) { a0[k] = f32x2{0.f, 0.f}; a1[k] = f32x2{0.f, 0.f}; }
    unsigned landed = 0, endq = 0xffffffffu;
    unsigned pc = wave; int slot = wave, gen = 1;
    u32x4 q0, q1;
    LAS u16* scw = (LAS u16*)(smem + L_REC + wave * 2048) + (lane >> 1);
    auto refresh = [&]() {
      u32x2 c; asm volatile("ds_read_b64 %0, %1\n\ts_waitcnt lgkmcnt(0)" : "=v"(c) : "v"(lbase + L_LANDED) : "memory");
      landed = __builtin_amdgcn_readfirstlane(c[0]); endq = __builtin_amdgcn_readfirstlane(c[1]);
    };
    auto advance = [&](int n) { pc += n; slot += n; if (slot >= NSLOT) { slot -= NSLOT; ++gen; } };
    auto k_read = [&](u32x4 (&R)[8]) {
      const LAS u32x4* kt = (const LAS u32x4*)(smem + slot * PIECE) + lane;
#pragma unroll
      for (int i = 0; i < 8; ++i) R[i] = kt[i * 64];
      lds_st(lbase + L_FREE + slot * 4, gen);
    };
    auto k_scores = [&](const u32x4 (&R)[8], int t) {
      float sc[8];
#pragma unroll
      for (int i = 0; i < 8; ++i) {
        float a = 0.f, c = 0.f;
#pragma unroll
        for (int k = 0; k < 4; ++k) { a = dot2(R[i][k], q0[k], a); c = dot2(R[i][k], q1[k], c); }
        sc[i] = swap16_add(a, c);
      }
      const float s = row_transpose_sum8(sc, 0xccccccccccccccccull);
      const unsigned sb = pack_bf16(s, s);
      scw[t * 32] = (u16)sb;
      m = vmax(m, lo16(sb));
    };
    auto v_step = [&](int t) {
      LAS float* pw = (LAS float*)(smem + L_P) + wave * 32;
      const float p = __builtin_amdgcn_exp2f((lo16(scw[t * 32]) - m) * C2);
      pw[lane >> 1] = p;
      l += p;
      f32x4 pp[4];
#pragma unroll
      for (int k = 0; k < 4; ++k) pp[k] = *(const LAS f32x4*)(pw + (lane >> 5) * 16 + 4 * k);
      const LAS u32x4* vt = (const LAS u32x4*)(smem + slot * PIECE) + lane;
#pragma unroll
      for (int hf = 0; hf < 2; ++hf) {
        while (landed < 2 * pc + 1 + hf) refresh();
        u32x4 R[4];
#pragma unroll
        for (int i = 0; i < 4; ++i) R[i] = vt[(hf * 4 + i) * 64];
        if (hf == 1) lds_st(lbase + L_FREE + slot * 4, gen);
#pragma unroll
        for (int i = 0; i < 4; ++i)
#pragma unroll
          for (int k = 0; k < 4; ++k) { const f32x2 v = {lo16(R[i][k]), hi16(R[i][k])}; a0[k] += v * pp[hf][i]; a1[k] += v * pp[2 + hf][i]; }
        asm volatile("" : "+v"(a0[0]), "+v"(a0[1]), "+v"(a0[2]), "+v"(a0[3]), "+v"(a1[0]), "+v"(a1[1]), "+v"(a1[2]), "+v"(a1[3]));
      }
    };
    {
      u32x4 R0[8], R1[8], R2[8], R3[8];
      while (landed < 2 * pc + 2) refresh();
      k_read(R0); advance(4);
      while (landed < 2 * pc + 2) refresh();
      k_read(R1); advance(4);
      while (landed < 2 * pc + 2) refresh();
      k_read(R2); advance(4);
      if (wave != 0) {
        while (landed < 2 * pc + 2) refresh();
        k_read(R3);
      }
      if (wave == 0) {
        u32x4 g0 = {0u, 0u, ~tag[0], 0u}, g1 = g0;
        const unsigned qo = (unsigned)(h * 2048 + lane * 32);
        unsigned spins = 0;
        for (;;) {
          const u64 miss = __ballot(!(g0[2] == tag[0] && g0[3] == tag[1] && g1[2] == tag[0] && g1[3] == tag[1]));
          if (miss == 0) break;
          if (++spins > SPIN_CAP) { bad = true; break; }
          repoll2(g0, g1, qo, qo + 16, ws + WS_Q, miss, miss);
        }
        *(LAS u32x4*)(smem + L_Q + lane * 16) = u32x4{g0[0], g0[1], g1[0], g1[1]};
        lds_st(lbase + L_QFLAG, 1u);
        while (landed < 2 * pc + 2) refresh();
        k_read(R3);
      } else {
        while (lds_ld(lbase + L_QFLAG) == 0) __builtin_amdgcn_s_sleep(1);
      }
      q0 = *(const LAS u32x4*)(smem + L_Q + (lane & 31) * 16); q1 = *(const LAS u32x4*)(smem + L_Q + 512 + (lane & 31) * 16);
      advance(4);
      k_scores(R0, 0); k_scores(R1, 1); k_scores(R2, 2); k_scores(R3, 3);
    }
    for (int t = 4; t < S / 2; ++t) {
      u32x4 R[8];
      while (landed < 2 * pc + 2) refresh();
      k_read(R); advance(4);
      k_scores(R, t);
    }
    m = head_max(m); macc = m;
    const int nv = S / 2 - (wave >> 1);
    for (int t = 0; t < nv; ++t) {
      v_step(t); advance(4);
      if (PT && t == PTT) {
        unsigned d0, d1;
        if (PT == 1) asm volatile("global_load_dword %0, %2, %4 sc1\n\tglobal_load_dword %1, %3, %5 sc1\n\ts_waitcnt vmcnt(0)" : "=&v"(d0), "=&v"(d1) : "v"(o_pd), "v"(o_y), "s"(ws + WS_PD), "s"(ws + WS_Y) : "memory");
        if (PT == 2) asm volatile("global_load_dword %0, %2, %4\n\tglobal_load_dword %1, %3, %5\n\ts_waitcnt vmcnt(0)" : "=&v"(d0), "=&v"(d1) : "v"(o_pd), "v"(o_y), "s"(ws + WS_PD), "s"(ws + WS_Y) : "memory");
        if (PT == 3) asm volatile("global_load_dword %0, %2, %4 nt\n\tglobal_load_dword %1, %3, %5 nt\n\ts_waitcnt vmcnt(0)" : "=&v"(d0), "=&v"(d1) : "v"(o_pd), "v"(o_y), "s"(ws + WS_PD), "s"(ws + WS_Y) : "memory");
      }
    }
    if (wave >= 2) { advance(2); v_step(S / 2 - 1); advance(2); } else { pc -= 2; slot -= 2; if (slot < 0) { slot += NSLOT; --gen; } }
    bool pend = false;
    for (;;) {
      while (landed < 2 * pc + 2 && endq >= 2 * pc + 2) refresh();
      if (endq < 2 * pc + 2) break;
      u32x4 R[8];
      k_read(R); advance(6);
      k_scores(R, 0);
      m = head_max(m);
      const float alpha = __builtin_amdgcn_exp2f((macc - m) * C2);
      macc = m; l *= alpha;
      const float al0 = lane_of(alpha, 0), al1 = lane_of(alpha, 16);
#pragma unroll
      for (int k = 0; k < 4; ++k) { a0[k] *= al0; a1[k] *= al1; }
      refresh();
      if (endq < 2 * pc + 6) { pend = true; break; }
      v_step(0); advance(2);
    }
    unsigned wow[32];
    if (own_o) {
#pragma unroll
      for (int r = 0; r < 32; ++r) LD1_NT(wow[r], tc, wo + (size_t)(role * 32 + (r & ~3)) * (D * 2), (r & 3) * (D * 2));
    }
    if (pend) v_step(0);

    LAS float* rec = (LAS float*)(smem + L_REC);
    LAS f32x2* mlr = (LAS f32x2*)(smem + L_ML);
    {
      f32x4 o0, o1;
#pragma unroll
      for (int k = 0; k < 2; ++k) {
        o0[2 * k] = swap32_add(a0[k][0], a1[k][0]); o0[2 * k + 1] = swap32_add(a0[k][1], a1[k][1]);
        o1[2 * k] = swap32_add(a0[2 + k][0], a1[2 + k][0]); o1[2 * k + 1] = swap32_add(a0[2 + k][1], a1[2 + k][1]);
      }
      *(LAS f32x4*)(rec + wave * 512 + 8 * lane) = o0; *(LAS f32x4*)(rec + wave * 512 + 8 * lane + 4) = o1;
      l += dpp<ROR8>(l); l += dpp<HMIR>(l); l += dpp<XOR2>(l); l = swap32_add(l, l);
      if ((lane & 47) == 0) mlr[(lane >> 4) * NC + wave] = f32x2{m, l};
    }
    wg_barrier();
    const unsigned tag16 = tag[0] & 0xffffu;
    {
      const LAS f32x4* mp = (const LAS f32x4*)(mlr + (tid >> 7) * NC);
      const f32x4 m01 = mp[0], m23 = mp[1];
      const LAS f32x2* ap = (const LAS f32x2*)rec + tid;
      const f32x2 v0 = ap[0], v1 = ap[256], v2 = ap[512], v3 = ap[768];
      const float M = vmax(vmax(m01[0], m01[2]), vmax(m23[0], m23[2]));
      const float f0 = __builtin_amdgcn_exp2f((m01[0] - M) * C2), f1 = __builtin_amdgcn_exp2f((m01[2] - M) * C2);
      const float f2 = __builtin_amdgcn_exp2f((m23[0] - M) * C2), f3 = __builtin_amdgcn_exp2f((m23[2] - M) * C2);
      const f32x2 o = (v0 * f0 + v1 * f1) + (v2 * f2 + v3 * f3);
      const unsigned L = __float_as_uint((f0 * m01[1] + f1 * m01[3]) + (f2 * m23[1] + f3 * m23[3]));
      const unsigned pay = (lane & 2) ? L << 16 : ((lane & 1) ? L : __float_as_uint(M)) & 0xffff0000u;
      const u32x4 gd = {__float_as_uint(o[0]), __float_as_uint(o[1]), pay | tag16, tag[1]};
      ST4_SC1(o_pd, gd, ws + WS_PD);
    }

    u32x4 g0, g1;
    if (own_o) {
      LD4_SC1(g0, o_g0, gbase, 0); LD4_SC1(g1, o_g1, gbase, 0);
      f32x2 wof[32];
#pragma unroll
      for (int r = 0; r < 32; ++r) WAITV(3, wow[r]);
#pragma unroll
      for (int r = 0; r < 32; ++r) { wof[r] = f32x2{lo16(wow[r]), hi16(wow[r])}; asm volatile("" : "+v"(wof[r])); }
      WAITV(0, g0); WAITV(0, g1);
      unsigned spins = 0;
      for (;;) {
        const u64 m0 = __ballot(!((g0[2] & 0xffffu) == tag16 && g0[3] == tag[1])), m1 = __ballot(!((g1[2] & 0xffffu) == tag16 && g1[3] == tag[1]));
        if ((m0 | m1) == 0) break;
        if (++spins > SPIN_CAP) { bad = true; break; }
        repoll2(g0, g1, o_g0, o_g1, gbase, m0, m1);
      }

      const float ma = hi16(dppu<0x00>(g0[2])), mb = hi16(dppu<0x00>(g1[2]));
      const float Mw = rows_max(vmax(ma, mb));
      const float fa = __builtin_amdgcn_exp2f((ma - Mw) * C2), fb = __builtin_amdgcn_exp2f((mb - Mw) * C2);
      const float la = __uint_as_float((dppu<0x55>(g0[2]) & 0xffff0000u) | (dppu<0xAA>(g0[2]) >> 16));
      const float lb = __uint_as_float((dppu<0x55>(g1[2]) & 0xffff0000u) | (dppu<0xAA>(g1[2]) >> 16));
      const float oe = fa * __uint_as_float(g0[0]) + fb * __uint_as_float(g1[0]), oo = fa * __uint_as_float(g0[1]) + fb * __uint_as_float(g1[1]);
      const float Lp = fa * la + fb * lb;
      const float t = swap32_add(swap16_add(oe, oo), swap16_add(Lp, Lp));
      LAS float* redo = (LAS float*)(smem + L_REDO);
      LAS float* ml2 = (LAS float*)(smem + L_ML2);
      if (lane < 32) redo[(2 * (lane & 15) + (lane >> 4)) * NC + wave] = t;
      if (lane == 32) { ml2[wave] = Mw; ml2[NC + wave] = t; }
      wg_barrier();
      const f32x4 ov = ((const LAS f32x4*)redo)[lane & 31], Mv = ((const LAS f32x4*)ml2)[0], Lv = ((const LAS f32x4*)ml2)[1];
      const float M = vmax(vmax(Mv[0], Mv[1]), vmax(Mv[2], Mv[3]));
      f32x4 fv;
#pragma unroll
      for (int w = 0; w < NC; ++w) fv[w] = __builtin_amdgcn_exp2f((Mv[w] - M) * C2);
      const f32x4 on4 = ov * fv, L4 = Lv * fv;
      const float on = rbf(((on4[0] + on4[1]) + (on4[2] + on4[3])) * __builtin_amdgcn_rcpf((L4[0] + L4[1]) + (L4[2] + L4[3])));
      f32x2 y1 = {0.f, 0.f}, y2 = y1, y3 = y1;
#pragma unroll
      for (int r = 0; r < 32; r += 4) {
        y += wof[r] * lane_of(on, r); y1 += wof[r + 1] * lane_of(on, r + 1); y2 += wof[r + 2] * lane_of(on, r + 2); y3 += wof[r + 3] * lane_of(on, r + 3);
      }
      y = (y + y1) + (y2 + y3);
      if (bad) y[0] = __uint_as_float(0x7fc00000u);
      if (tid < 224) {
        const u32x4 gy = {__float_as_uint(y[0]), __float_as_uint(y[1]), tag[0], tag[1]};
        ST4_SC1(o_y, gy, ws + WS_Y);
      }

    } else if (own_out) {
      g0 = u32x4{0u, 0u, ~tag[0], 0u}; g1 = g0;
      unsigned spins = 0;
      for (;;) {
        const u64 m0 = __ballot(!(g0[2] == tag[0] && g0[3] == tag[1])), m1 = __ballot(!(g1[2] == tag[0] && g1[3] == tag[1]));
        if ((m0 | m1) == 0) break;
        if (++spins > SPIN_CAP) { bad = true; break; }
        const u64 gate = (spins == 1 || (m0 & m1) != ~0ull) ? ~0ull : 1ull;
        repoll2(g0, g1, o_g0, o_g1, gbase, m0 & gate, m1 & gate);
      }

      float s0 = __uint_as_float(g0[0]) + __uint_as_float(g1[0]), s1 = __uint_as_float(g0[1]) + __uint_as_float(g1[1]);
      if (bad) s0 = __uint_as_float(0x7fc00000u);
      s0 += dpp<ROR8>(s0); s1 += dpp<ROR8>(s1); s0 += dpp<ROR4>(s0); s1 += dpp<ROR4>(s1);
      float ss = swap16_add(s0, s1); ss = swap32_add(ss, ss);
      LAS float* redy = (LAS float*)(smem + L_REDO);
      if ((lane & 44) == 0) redy[wave * 8 + 2 * (lane & 3) + (lane >> 4)] = ss;
      wg_barrier();
      if (tid < 4) {
        const LAS f32x2* ry = (const LAS f32x2*)redy + tid;
        const f32x2 t = (ry[0] + ry[4]) + (ry[8] + ry[12]);
        out[(role >> 1) * 4 + tid] = pack_bf16(lo16(xo) + t[0] + lo16(bo), hi16(xo) + t[1] + hi16(bo));
        if (role == 1 && tid == 0) *(u32x2*)(ws + WS_E) = tag;
      }

    }
  }
}

extern "C" void kernel_launch(void* const* d_in, const int* in_sizes, int n_in, void* d_out, int out_size, void* d_ws, size_t ws_size, hipStream_t stream) {
  fused_kernel<<<NWG, 320, 0, stream>>>((const char*)d_in[1], (const char*)d_in[2], (char*)d_ws, (const u16*)d_in[0], (const char*)d_in[3], (const char*)d_in[4],
                                        (const char*)d_in[6], (const u16*)d_in[7], (const char*)d_in[8], (const u16*)d_in[9], (unsigned*)d_out);
}
```

```cpp
#define OUTPOL 2
#include <hip/hip_runtime.h>
#include <hip/hip_bf16.h>
#include <cstdint>

typedef unsigned u32x4 __attribute__((ext_vector_type(4)));
typedef unsigned u32x2 __attribute__((ext_vector_type(2)));
typedef float f32x4 __attribute__((ext_vector_type(4)));
typedef float f32x2 __attribute__((ext_vector_type(2)));
typedef __bf16 bf16x2 __attribute__((ext_vector_type(2)));
typedef unsigned short u16;
typedef unsigned long long u64;
#define LAS __attribute__((address_space(3)))

#define WV 17
constexpr int INFL = 24;
constexpr int T_MLP = 6, T_O = 2, T_Q = 4, T_X = 2;
constexpr int CH0 = 4, NC2 = 4, NC1 = 8;
constexpr int D = 448, F = 1792, DH = 256;
constexpr int NWG = 256, NC = 4;
constexpr int PIECE = 8192, NSLOT = 18;
constexpr int RPR = 64;
constexpr int POOL = T_MLP + 3 * T_O + 2 * T_Q + 2 * T_X;
static_assert(POOL >= 2 * NC2 + NC1 && INFL % 4 == 0 && INFL >= 20 && INFL <= 32, "loader structure");
static_assert(T_MLP % 2 == 0 && T_O % 2 == 0 && T_Q % 2 == 0 && T_X % 2 == 0 && T_X >= 2 && T_O >= 2 && T_Q >= 2, "an even number of static rounds, at most 62");
constexpr unsigned PD_STRIDE = 33 * 128, Y_STRIDE = 29 * 128;
constexpr size_t WS_E = 0, WS_CTR = 4096, WS_Q = 16384, WS_PD = 65536, WS_Y = WS_PD + (size_t)NWG * PD_STRIDE;
constexpr unsigned SPIN_CAP = 1u << 22;
constexpr unsigned L_CTL = NSLOT * PIECE;
constexpr unsigned L_LANDED = L_CTL, L_QFLAG = L_CTL + 8, L_QCNT = L_CTL + 12, L_HCNT = L_CTL + 16, L_FREE = L_CTL + 32;
constexpr unsigned L_Q = L_CTL + 128, L_RED = L_Q + 1024, L_REDO = L_RED + 1024, L_ML2 = L_REDO + 512, L_P = L_ML2 + 64;
constexpr unsigned L_ML = L_P + NC * 128, L_XN = L_ML + 64, L_REC = L_XN + 2048, L_END = L_REC + NC * 2 * DH * 4;
static_assert(L_END <= 160 * 1024 && D <= 2 * DH, "LDS");
constexpr float C2 = 0.0625f * 1.4426950408889634f;

__device__ __forceinline__ float lo16(unsigned w) { return __uint_as_float(w << 16); }
__device__ __forceinline__ float hi16(unsigned w) { return __uint_as_float(w & 0xffff0000u); }
__device__ __forceinline__ unsigned pack_bf16(float a, float b) {
  bf16x2 r = {(__bf16)a, (__bf16)b};
  return __builtin_bit_cast(unsigned, r);
}
__device__ __forceinline__ float rbf(float x) { return lo16(pack_bf16(x, x)); }
__device__ __forceinline__ float dot2(unsigned k, unsigned q, float acc) {
  return __builtin_amdgcn_fdot2_f32_bf16(__builtin_bit_cast(bf16x2, k), __builtin_bit_cast(bf16x2, q), acc, false);
}
template <int CTRL> __device__ __forceinline__ unsigned dppu(unsigned x) { return (unsigned)__builtin_amdgcn_mov_dpp((int)x, CTRL, 0xf, 0xf, true); }
template <int CTRL> __device__ __forceinline__ float dpp(float x) { return __uint_as_float(dppu<CTRL>(__float_as_uint(x))); }
constexpr int XOR1 = 0xB1, XOR2 = 0x4E, HMIR = 0x141, MIR = 0x140, ROR8 = 0x128, ROR4 = 0x124;
__device__ __forceinline__ float swap16_add(float a, float b) {
  auto r = __builtin_amdgcn_permlane16_swap(__float_as_uint(a), __float_as_uint(b), false, false);
  return __uint_as_float(r[0]) + __uint_as_float(r[1]);
}
__device__ __forceinline__ float swap32_add(float a, float b) {
  auto r = __builtin_amdgcn_permlane32_swap(__float_as_uint(a), __float_as_uint(b), false, false);
  return __uint_as_float(r[0]) + __uint_as_float(r[1]);
}
__device__ __forceinline__ float row_sum16(float x) { x += dpp<XOR1>(x); x += dpp<XOR2>(x); x += dpp<HMIR>(x); x += dpp<MIR>(x); return x; }
__device__ __forceinline__ float wave_sum(float x) { x = row_sum16(x); x = swap16_add(x, x); return swap32_add(x, x); }
__device__ __forceinline__ float lane_of(float x, int l) { return __builtin_bit_cast(float, __builtin_amdgcn_readlane(__builtin_bit_cast(int, x), l)); }
__device__ __forceinline__ float row_transpose_sum8(const float (&v)[8], u64 bit1) {
  float t0, t1, t2, t3, r;
  asm volatile("s_nop 1\n\t"
      "v_add_f32_dpp %0, %5, %5 row_ror:8 row_mask:0xf bank_mask:0xf\n\tv_add_f32_dpp %1, %6, %6 row_ror:8 row_mask:0xf bank_mask:0xf\n\t"
      "v_add_f32_dpp %2, %7, %7 row_ror:8 row_mask:0xf bank_mask:0xf\n\tv_add_f32_dpp %3, %8, %8 row_ror:8 row_mask:0xf bank_mask:0xf\n\t"
      "v_add_f32_dpp %0, %9, %9 row_ror:8 row_mask:0xf bank_mask:0xc\n\tv_add_f32_dpp %1, %10, %10 row_ror:8 row_mask:0xf bank_mask:0xc\n\t"
      "v_add_f32_dpp %2, %11, %11 row_ror:8 row_mask:0xf bank_mask:0xc\n\tv_add_f32_dpp %3, %12, %12 row_ror:8 row_mask:0xf bank_mask:0xc\n\t"
      "v_add_f32_dpp %0, %0, %0 row_half_mirror row_mask:0xf bank_mask:0x5\n\tv_add_f32_dpp %1, %1, %1 row_half_mirror row_mask:0xf bank_mask:0x5\n\t"
      "v_add_f32_dpp %0, %2, %2 row_half_mirror row_mask:0xf bank_mask:0xa\n\tv_add_f32_dpp %1, %3, %3 row_half_mirror row_mask:0xf bank_mask:0xa\n\t"
      "s_nop 1\n\t"
      "v_add_f32_dpp %2, %0, %0 quad_perm:[2,3,0,1] row_mask:0xf bank_mask:0xf\n\tv_add_f32_dpp %3, %1, %1 quad_perm:[2,3,0,1] row_mask:0xf bank_mask:0xf\n\t"
      "v_cndmask_b32 %2, %2, %3, %13\n\ts_nop 1\n\t"
      "v_add_f32_dpp %4, %2, %2 quad_perm:[1,0,3,2] row_mask:0xf bank_mask:0xf"
      : "=&v"(t0), "=&v"(t1), "=&v"(t2), "=&v"(t3), "=&v"(r)
      : "v"(v[0]), "v"(v[1]), "v"(v[2]), "v"(v[3]), "v"(v[4]), "v"(v[5]), "v"(v[6]), "v"(v[7]), "s"(bit1));
  return r;
}
__device__ __forceinline__ float head_max(float x) {
  float y;
  asm volatile("s_nop 1\n\tv_max_f32_dpp %0, %0, %0 row_ror:8 row_mask:0xf bank_mask:0xf\n\ts_nop 1\n\tv_max_f32_dpp %0, %0, %0 row_half_mirror row_mask:0xf bank_mask:0xf\n\t"
               "s_nop 1\n\tv_max_f32_dpp %0, %0, %0 quad_perm:[2,3,0,1] row_mask:0xf bank_mask:0xf\n\tv_mov_b32 %1, %0\n\ts_nop 1\n\tv_permlane32_swap_b32 %0, %1\n\tv_max_f32 %0, %0, %1"
               : "+v"(x), "=&v"(y));
  return x;
}
__device__ __forceinline__ float rows_max(float x) {
  float y;
  asm volatile("v_mov_b32 %1, %0\n\ts_nop 1\n\tv_permlane16_swap_b32 %0, %1\n\tv_max_f32 %0, %0, %1\n\tv_mov_b32 %1, %0\n\ts_nop 1\n\tv_permlane32_swap_b32 %0, %1\n\tv_max_f32 %0, %0, %1"
               : "+v"(x), "=&v"(y));
  return x;
}
__device__ __forceinline__ float vmax(float a, float b) { float r; asm("v_max_f32 %0, %1, %2" : "=v"(r) : "v"(a), "v"(b)); return r; }
__device__ __forceinline__ void layer_norm(const u32x4& xv, LAS float* xn, int lane) {
  float xf[8];
#pragma unroll
  for (int k = 0; k < 4; ++k) { xf[2 * k] = lo16(xv[k]); xf[2 * k + 1] = hi16(xv[k]); }
  const float live = lane < 56 ? 1.f : 0.f;
  const float mean = wave_sum((((xf[0] + xf[1]) + (xf[2] + xf[3])) + ((xf[4] + xf[5]) + (xf[6] + xf[7]))) * live) * (1.f / D);
  float ss = 0.f;
#pragma unroll
  for (int k = 0; k < 8; ++k) { const float d = xf[k] - mean; ss += d * d; }
  const float var = wave_sum(ss * live) * (1.f / D);
  const float rden = __builtin_amdgcn_rcpf(rbf(__builtin_amdgcn_sqrtf(rbf(rbf(var) + 1e-5f)))) * live, mb = rbf(mean);
#pragma unroll
  for (int k = 0; k < 8; k += 2) {
    const unsigned c = pack_bf16(xf[k] - mb, xf[k + 1] - mb), n = pack_bf16(lo16(c) * rden, hi16(c) * rden);
    xf[k] = lo16(n); xf[k + 1] = hi16(n);
  }
  *(LAS f32x4*)(xn + 8 * lane) = f32x4{xf[0], xf[1], xf[2], xf[3]}; *(LAS f32x4*)(xn + 8 * lane + 4) = f32x4{xf[4], xf[5], xf[6], xf[7]};
}
template <int NY> __device__ __forceinline__ void slice_partials(u32x4 (&w)[14], const LAS float* xn, LAS float* red, int tid) {
  f32x2 v[4] = {{0.f, 0.f}, {0.f, 0.f}, {0.f, 0.f}, {0.f, 0.f}};
  float xr[14];
#pragma unroll
  for (int i = 0; i < 14; ++i) xr[i] = xn[(tid >> 3) + 32 * i];
#pragma unroll
  for (int i = 0; i < 14; ++i) {
    asm volatile("s_waitcnt vmcnt(%1)" : "+v"(w[i]) : "i"(13 - i + NY));
#pragma unroll
    for (int k = 0; k < 4; ++k) v[k] += f32x2{lo16(w[i][k]), hi16(w[i][k])} * xr[i];
    asm volatile("" : "+v"(v[0]), "+v"(v[1]), "+v"(v[2]), "+v"(v[3]));
  }
  const float a0 = swap32_add(v[0][0], v[2][0]), a1 = swap32_add(v[0][1], v[2][1]), a2 = swap32_add(v[1][0], v[3][0]), a3 = swap32_add(v[1][1], v[3][1]);
  const float b0 = swap16_add(a0, a2), b1 = swap16_add(a1, a3);
  const float t1 = b0 + dpp<ROR8>(b0), t2 = b1 + dpp<ROR8>(b1);
  red[(tid & 7) * 8 + ((tid >> 3) & 7)] = (tid & 8) ? t2 : t1;
}

#define LD_NT(dst, voff, sbase, imm) asm volatile("global_load_dwordx4 %0, %1, %2 offset:%3 nt" : "=v"(dst) : "v"(voff), "s"(sbase), "i"(imm))
#define LD1_NT(dst, voff, sbase, imm) asm volatile("global_load_dword %0, %1, %2 offset:%3 nt" : "=v"(dst) : "v"(voff), "s"(sbase), "i"(imm))
#define LD4_SC1(dst, voff, sbase, imm) asm volatile("global_load_dwordx4 %0, %1, %2 offset:%3 sc1" : "=v"(dst) : "v"(voff), "s"(sbase), "i"(imm) : "memory")
#define ST4_SC1(voff, val, sbase) asm volatile("global_store_dwordx4 %0, %1, %2 sc1\n\ts_nop 1" :: "v"(voff), "v"(val), "s"(sbase) : "memory")
#define WAITV(n, reg) asm volatile("s_waitcnt vmcnt(%1)" : "+v"(reg) : "i"(n))
#define VMCNT(n) asm volatile("s_waitcnt vmcnt(%0)" :: "i"(n) : "memory")
__device__ __forceinline__ void repoll2(u32x4& g0, u32x4& g1, unsigned o0, unsigned o1, const char* base, u64 m0, u64 m1) {
  asm volatile("s_mov_b64 exec, %5\n\tglobal_load_dwordx4 %0, %2, %4 sc1\n\ts_mov_b64 exec, %6\n\tglobal_load_dwordx4 %1, %3, %4 sc1\n\ts_mov_b64 exec, -1\n\ts_waitcnt vmcnt(0)"
               : "+v"(g0), "+v"(g1) : "v"(o0), "v"(o1), "s"(base), "s"(m0), "s"(m1) : "memory");
}
__device__ __forceinline__ void wg_barrier() { asm volatile("s_waitcnt lgkmcnt(0)\n\ts_barrier" ::: "memory"); }
__device__ __forceinline__ void lds_st(unsigned addr, unsigned v) { asm volatile("ds_write_b32 %0, %1" :: "v"(addr), "v"(v) : "memory"); }
__device__ __forceinline__ unsigned lds_ld(unsigned addr) {
  unsigned v; asm volatile("ds_read_b32 %0, %1\n\ts_waitcnt lgkmcnt(0)" : "=v"(v) : "v"(addr) : "memory"); return __builtin_amdgcn_readfirstlane(v); }
__device__ __forceinline__ void dma4(const char* g, unsigned voff, unsigned lds) {
  asm volatile("s_mov_b32 m0, %2\n\ts_nop 0\n\tglobal_load_lds_dwordx4 %0, %1 nt\n\tglobal_load_lds_dwordx4 %0, %1 offset:1024 nt\n\t"
               "global_load_lds_dwordx4 %0, %1 offset:2048 nt\n\tglobal_load_lds_dwordx4 %0, %1 offset:3072 nt" :: "v"(voff), "s"(g), "s"(lds) : "memory");
}

__global__ __launch_bounds__(320, 2) void fused_kernel(const char* __restrict__ kc, const char* __restrict__ vc, char* __restrict__ ws,
                                                    const u16* __restrict__ x, const char* __restrict__ wq, const char* __restrict__ wo, const char* __restrict__ w1,
                                                    const u16* __restrict__ b1, const char* __restrict__ w2, const u16* __restrict__ b2,
                                                    unsigned* __restrict__ out) {
  __shared__ __attribute__((aligned(1024))) char smem[L_END];
  const unsigned lbase = (unsigned)(uintptr_t)(LAS char*)smem;
  const int b = blockIdx.x, tid = threadIdx.x, lane = tid & 63, wave = __builtin_amdgcn_readfirstlane(tid >> 6);
  const int role = b >> 1;
  const bool own_o = ((b ^ (b >> 3)) & 1) == 0, has_mlp = own_o && (role & 3) == 0 && role < 112;
  const bool make_q = !own_o && (role & 1) == 0, own_out = !own_o && (role & 1) == 1 && role < 112;
  const int S = RPR - (own_o ? ((role & 3) == 0 ? T_MLP : T_O) : (make_q ? T_Q : T_X));

  if (__builtin_expect(wave == NC, 1)) {
    __builtin_amdgcn_s_setprio(3);
    const unsigned voff = lane * 16;
    const int q = b >> 3;
    unsigned* ctr = (unsigned*)(ws + WS_CTR) + q * 64;
    int nq = 0, slot = 0, gen = 0;
    constexpr int LAG = INFL / 4 - 1;
    int frs = 0;
    auto put = [&](const char* g, int u) {
      if ((u & 1) == 0 && frs < gen) { while ((int)lds_ld(lbase + L_FREE + slot * 4) < gen) __builtin_amdgcn_s_sleep(1); }
      dma4(g, voff, lbase + slot * PIECE + (u & 1) * 4096);
      ++nq;
      if (u & 1) { if (++slot == NSLOT) { slot = 0; ++gen; } }
    };
    auto settle = [&](int u) {
      if (u & 1) {
        unsigned fr;
        asm volatile("ds_read_b32 %0, %1\n\ts_waitcnt vmcnt(%2) lgkmcnt(0)" : "=&v"(fr) : "v"(lbase + L_FREE + slot * 4), "i"(INFL - 4) : "memory");
        frs = __builtin_amdgcn_readfirstlane(fr);
      } else VMCNT(INFL - 4);
      lds_st(lbase + L_LANDED, nq - LAG);
    };
    auto piece = [&](const char* g) {
      put(g, 0); put(g + 4096, 1);
      unsigned fr;
      asm volatile("ds_read_b32 %0, %1\n\ts_waitcnt vmcnt(%2) lgkmcnt(0)" : "=&v"(fr) : "v"(lbase + L_FREE + slot * 4), "i"(WV) : "memory");
      frs = __builtin_amdgcn_readfirstlane(fr);
      lds_st(lbase + L_LANDED, max(nq - (WV + 3) / 4, 0));
    };
    unsigned tv = 0;
    auto ticket = [&]() { asm volatile("s_mov_b64 exec, 1\n\tglobal_atomic_add %0, %1, %2, %3 sc0\n\ts_mov_b64 exec, -1" : "=v"(tv) : "v"(0u), "v"(1u), "s"(ctr) : "memory"); };
    size_t voffs = (size_t)b * (1u << 20);
    {
      const char* g = kc + voffs;
      put(g, 0); put(g + 4096, 1);
      if (lane < 8 + NSLOT) lds_st(lbase + L_CTL + lane * 4, lane == 1 ? 0xffffffffu : 0u);
      wg_barrier();
      for (int p = 1; p < 2 * S; ++p) { g += PIECE; piece(g); }

      g = vc + voffs;
      for (int p = 0; p < 2 * S - 6; ++p) { piece(g); g += PIECE; }
      while (lds_ld(lbase + L_QFLAG) == 0) __builtin_amdgcn_s_sleep(1);
#pragma unroll
      for (int u = 0; u < 8; ++u) { if (u == 8 - INFL / 4) ticket(); put(g + u * 4096, u); settle(u); }

      voffs += (size_t)(S - 1) * (2 * PIECE);
    }
    for (;;) {
      asm volatile("" : "+v"(tv));
      const int k = __builtin_amdgcn_readfirstlane(tv);
      constexpr int A = POOL - 2 * NC2 - NC1, N4 = (A + CH0 - 1) / CH0;
      int cur, sz;
      if (k < N4) { cur = CH0 * k; sz = min(CH0, A - cur); } else if (k < N4 + NC2) { cur = A + 2 * (k - N4); sz = 2; } else { cur = A + 2 * NC2 + (k - N4 - NC2); sz = 1; }
      if (cur >= POOL) break;
      for (; sz > 0; --sz, ++cur) {
        constexpr int C1 = T_MLP + T_Q, C2_ = C1 + T_O + T_X, C3 = C2_ + T_O + T_Q;
        const int pair = (cur >= C1) + (cur >= C2_) + (cur >= C3), c = cur - (cur >= C3 ? C3 : cur >= C2_ ? C2_ : cur >= C1 ? C1 : 0);
        const int to = pair == 0 ? T_MLP : T_O; const bool first = c < to;
        const int reg = 8 * q + 2 * pair + ((first ? q : q + 1) & 1), pos = first ? RPR - to + c : RPR - ((pair & 1) ? T_X : T_Q) + (c - to);
        const size_t off = (size_t)reg * (1u << 20) + (size_t)pos * (2 * PIECE);
        const char* kp = kc + off; const char* vp = vc + voffs;
#pragma unroll
        for (int u = 0; u < 8; ++u) {
          if (u == 8 - INFL / 4 && sz == 1) ticket();
          put(u < 4 ? kp + u * 4096 : vp + (u - 4) * 4096, u);
          settle(u);
        }
        voffs = off;
      }
    }
    lds_st(lbase + L_LANDED + 4, nq);

    slot += 2; if (slot >= NSLOT) { slot -= NSLOT; ++gen; }
    frs = 0;
#pragma unroll
    for (int u = 0; u < 4; ++u) { put(vc + voffs + u * 4096, u); settle(u); }
#pragma unroll
    for (int i = LAG - 1; i >= 0; --i) { VMCNT(4 * i); lds_st(lbase + L_LANDED, nq - i + (i < 4 ? 4 : 0)); }

  } else {
    const int h = b >> 5;
    LAS float* xn = (LAS float*)(smem + L_XN);
    LAS float* red = (LAS float*)(smem + L_RED);
    u32x2 ev, tag;
    auto arrive = [&](unsigned cnt) -> int {
      unsigned arr;
      asm volatile("s_mov_b64 exec, 1\n\tds_add_rtn_u32 %0, %1, %2\n\ts_mov_b64 exec, -1\n\ts_waitcnt lgkmcnt(0)" : "=&v"(arr) : "v"(lbase + cnt), "v"(1u) : "memory");
      return __builtin_amdgcn_readfirstlane(arr);
    };
    auto make_tag = [&]() {
      WAITV(0, ev);
      const u64 e = (u64)(unsigned)__builtin_amdgcn_readfirstlane(ev[1]) << 32 | (unsigned)__builtin_amdgcn_readfirstlane(ev[0]);
      const u64 z = e * 6364136223846793005ull + 1442695040888963407ull;
      tag = u32x2{(unsigned)z, (unsigned)(z >> 32)};
    };
    asm volatile("global_load_dwordx2 %0, %1, %2 sc1" : "=v"(ev) : "v"(0u), "s"(ws + WS_E) : "memory");
    if (make_q) {
      const int qi = role >> 1;
      if (wave == 0 && (qi & 4) == 0) asm volatile("global_store_dword %0, %1, %2 sc1" :: "v"(0u), "v"(0u), "s"(ws + WS_CTR + ((qi >> 3) * 4 + (qi & 3)) * 256) : "memory");
      u32x4 xv, wv[14];
      asm volatile("global_load_dwordx4 %0, %1, %2" : "=v"(xv) : "v"((lane < 56 ? lane : 55) * 16u), "s"(x));
      const unsigned o = (unsigned)(tid >> 3) * 8192u + (tid & 7) * 16u;
#pragma unroll
      for (int i = 0; i < 14; ++i) LD_NT(wv[i], o, wq + qi * 128 + i * (32 * 8192), 0);
      wg_barrier();
      WAITV(14, xv);
      layer_norm(xv, xn, lane);
      slice_partials<0>(wv, xn, red + wave * 64, tid);
      make_tag();
      if (arrive(L_QCNT) == NC - 1 && lane < 16) {
        const LAS f32x4* rq = (const LAS f32x4*)red + lane;
        const f32x4 s = (rq[0] + rq[16]) + (rq[32] + rq[48]);
        const u32x4 g = {pack_bf16(s[0], s[1]), pack_bf16(s[2], s[3]), tag[0], tag[1]};
        ST4_SC1((unsigned)(qi * 256 + lane * 16), g, ws + WS_Q);
      }
    } else {
      wg_barrier();
    }
    bool bad = false;
    unsigned o_pd = (unsigned)(b * PD_STRIDE + tid * 16), o_y = (unsigned)(role * Y_STRIDE + (tid < 224 ? tid : 0) * 16);
    unsigned o_g0 = own_o ? (unsigned)((h * 32 + (tid >> 4)) * PD_STRIDE + (role & 15) * 256 + (tid & 15) * 16)
                          : (unsigned)((tid >> 2) * Y_STRIDE + (own_out ? role >> 1 : 0) * 64 + (tid & 3) * 16);
    unsigned o_g1 = o_g0 + (own_o ? 16 * PD_STRIDE : 64 * Y_STRIDE);
    asm volatile("" : "+v"(o_pd), "+v"(o_y), "+v"(o_g0), "+v"(o_g1));
    const char* gbase = ws + (own_o ? WS_PD : WS_Y);
    f32x2 y = {0.f, 0.f};
    unsigned xo = 0, bo = 0;
    const unsigned tc = (tid < 224 ? tid : 223) * 4u;
    if (has_mlp) {
      const int hg = role >> 2;
      u32x4 xv, wv[14]; unsigned b1w, wa[32], wb[32];
      asm volatile("global_load_dwordx4 %0, %1, %2" : "=v"(xv) : "v"((lane < 56 ? lane : 55) * 16u), "s"(x));
      asm volatile("global_load_ushort %0, %1, %2" : "=v"(b1w) : "v"(lane * 2u), "s"(b1 + hg * 64));
      const unsigned o = (unsigned)(tid >> 3) * (F * 2u) + (tid & 7) * 16u;
#pragma unroll
      for (int i = 0; i < 14; ++i) LD_NT(wv[i], o, w1 + hg * 128 + i * (32 * F * 2), 0);
#pragma unroll
      for (int r = 0; r < 32; ++r) LD1_NT(wa[r], tc, w2 + (size_t)(hg * 64 + (r & ~3)) * (D * 2), (r & 3) * (D * 2));
      WAITV(46, xv); WAITV(46, b1w);
      layer_norm(xv, xn, lane);
      slice_partials<32>(wv, xn, red + wave * 64, tid);
      arrive(L_HCNT);
#pragma unroll
      for (int r = 0; r < 28; ++r) LD1_NT(wb[r], tc, w2 + (size_t)(hg * 64 + 32 + (r & ~3)) * (D * 2), (r & 3) * (D * 2));
      while (lds_ld(lbase + L_HCNT) < NC) __builtin_amdgcn_s_sleep(1);
      const float hv = rbf(rbf((red[lane] + red[64 + lane]) + (red[128 + lane] + red[192 + lane])) + lo16(b1w));
      const float u = 0.7978845608028654f * (hv + 0.044715f * hv * hv * hv);
      const float e = __builtin_amdgcn_exp2f(u * -2.885390081777927f);
      const float ge = rbf(hv * __builtin_amdgcn_rcpf(1.f + e));
#pragma unroll
      for (int r = 0; r < 32; ++r) WAITV(28, wa[r]);
#pragma unroll
      for (int r = 28; r < 32; ++r) LD1_NT(wb[r], tc, w2 + (size_t)(hg * 64 + 32 + (r & ~3)) * (D * 2), (r & 3) * (D * 2));
#pragma unroll
      for (int r = 0; r < 32; ++r) y += f32x2{lo16(wa[r]), hi16(wa[r])} * lane_of(ge, r);
      asm volatile("" : "+v"(y));
#pragma unroll
      for (int r = 0; r < 32; ++r) WAITV(0, wb[r]);
#pragma unroll
      for (int r = 0; r < 32; ++r) y += f32x2{lo16(wb[r]), hi16(wb[r])} * lane_of(ge, 32 + r);
    }
    if (!own_o) {
      asm volatile("global_load_dword %0, %2, %3\n\tglobal_load_dword %1, %2, %4\n\ts_waitcnt vmcnt(0)"
                   : "=&v"(xo), "=&v"(bo) : "v"((lane & 3) * 4u), "s"(x + (own_out ? role >> 1 : 0) * 8), "s"(b2 + (own_out ? role >> 1 : 0) * 8));
    }
    asm volatile("" : "+v"(y));
    make_tag();
    float m = -INFINITY, macc = -INFINITY, l = 0.f;
    f32x2 a0[4], a1[4];
#pragma unroll
    for (int k = 0; k < 4; ++k) { a0[k] = f32x2{0.f, 0.f}; a1[k] = f32x2{0.f, 0.f}; }
    unsigned landed = 0, endq = 0xffffffffu;
    unsigned pc = wave; int slot = wave, gen = 1;
    u32x4 q0, q1;
    LAS u16* scw = (LAS u16*)(smem + L_REC + wave * 2048) + (lane >> 1);
    auto refresh = [&]() {
      u32x2 c; asm volatile("ds_read_b64 %0, %1\n\ts_waitcnt lgkmcnt(0)" : "=v"(c) : "v"(lbase + L_LANDED) : "memory");
      landed = __builtin_amdgcn_readfirstlane(c[0]); endq = __builtin_amdgcn_readfirstlane(c[1]);
    };
    auto advance = [&](int n) { pc += n; slot += n; if (slot >= NSLOT) { slot -= NSLOT; ++gen; } };
    auto k_read = [&](u32x4 (&R)[8]) {
      const LAS u32x4* kt = (const LAS u32x4*)(smem + slot * PIECE) + lane;
#pragma unroll
      for (int i = 0; i < 8; ++i) R[i] = kt[i * 64];
      lds_st(lbase + L_FREE + slot * 4, gen);
    };
    auto k_scores = [&](const u32x4 (&R)[8], int t) {
      float sc[8];
#pragma unroll
      for (int i = 0; i < 8; ++i) {
        float a = 0.f, c = 0.f;
#pragma unroll
        for (int k = 0; k < 4; ++k) { a = dot2(R[i][k], q0[k], a); c = dot2(R[i][k], q1[k], c); }
        sc[i] = swap16_add(a, c);
      }
      const float s = row_transpose_sum8(sc, 0xccccccccccccccccull);
      const unsigned sb = pack_bf16(s, s);
      scw[t * 32] = (u16)sb;
      m = vmax(m, lo16(sb));
    };
    auto v_step = [&](int t) {
      LAS float* pw = (LAS float*)(smem + L_P) + wave * 32;
      const float p = __builtin_amdgcn_exp2f((lo16(scw[t * 32]) - m) * C2);
      pw[lane >> 1] = p;
      l += p;
      f32x4 pp[4];
#pragma unroll
      for (int k = 0; k < 4; ++k) pp[k] = *(const LAS f32x4*)(pw + (lane >> 5) * 16 + 4 * k);
      const LAS u32x4* vt = (const LAS u32x4*)(smem + slot * PIECE) + lane;
#pragma unroll
      for (int hf = 0; hf < 2; ++hf) {
        while (landed < 2 * pc + 1 + hf) refresh();
        u32x4 R[4];
#pragma unroll
        for (int i = 0; i < 4; ++i) R[i] = vt[(hf * 4 + i) * 64];
        if (hf == 1) lds_st(lbase + L_FREE + slot * 4, gen);
#pragma unroll
        for (int i = 0; i < 4; ++i)
#pragma unroll
          for (int k = 0; k < 4; ++k) { const f32x2 v = {lo16(R[i][k]), hi16(R[i][k])}; a0[k] += v * pp[hf][i]; a1[k] += v * pp[2 + hf][i]; }
        asm volatile("" : "+v"(a0[0]), "+v"(a0[1]), "+v"(a0[2]), "+v"(a0[3]), "+v"(a1[0]), "+v"(a1[1]), "+v"(a1[2]), "+v"(a1[3]));
      }
    };
    {
      u32x4 R0[8], R1[8], R2[8], R3[8];
      while (landed < 2 * pc + 2) refresh();
      k_read(R0); advance(4);
      while (landed < 2 * pc + 2) refresh();
      k_read(R1); advance(4);
      while (landed < 2 * pc + 2) refresh();
      k_read(R2); advance(4);
      if (wave != 0) {
        while (landed < 2 * pc + 2) refresh();
        k_read(R3);
      }
      if (wave == 0) {
        u32x4 g0 = {0u, 0u, ~tag[0], 0u}, g1 = g0;
        const unsigned qo = (unsigned)(h * 2048 + lane * 32);
        unsigned spins = 0;
        for (;;) {
          const u64 miss = __ballot(!(g0[2] == tag[0] && g0[3] == tag[1] && g1[2] == tag[0] && g1[3] == tag[1]));
          if (miss == 0) break;
          if (++spins > SPIN_CAP) { bad = true; break; }
          repoll2(g0, g1, qo, qo + 16, ws + WS_Q, miss, miss);
        }
        *(LAS u32x4*)(smem + L_Q + lane * 16) = u32x4{g0[0], g0[1], g1[0], g1[1]};
        lds_st(lbase + L_QFLAG, 1u);
        while (landed < 2 * pc + 2) refresh();
        k_read(R3);
      } else {
        while (lds_ld(lbase + L_QFLAG) == 0) __builtin_amdgcn_s_sleep(1);
      }
      q0 = *(const LAS u32x4*)(smem + L_Q + (lane & 31) * 16); q1 = *(const LAS u32x4*)(smem + L_Q + 512 + (lane & 31) * 16);
      advance(4);
      k_scores(R0, 0); k_scores(R1, 1); k_scores(R2, 2); k_scores(R3, 3);
    }
    for (int t = 4; t < S / 2; ++t) {
      u32x4 R[8];
      while (landed < 2 * pc + 2) refresh();
      k_read(R); advance(4);
      k_scores(R, t);
    }
    m = head_max(m); macc = m;
    const int nv = S / 2 - (wave >> 1);
    for (int t = 0; t < nv; ++t) { v_step(t); advance(4); }
    if (wave >= 2) { advance(2); v_step(S / 2 - 1); advance(2); } else { pc -= 2; slot -= 2; if (slot < 0) { slot += NSLOT; --gen; } }
    bool pend = false;
    for (;;) {
      while (landed < 2 * pc + 2 && endq >= 2 * pc + 2) refresh();
      if (endq < 2 * pc + 2) break;
      u32x4 R[8];
      k_read(R); advance(6);
      k_scores(R, 0);
      m = head_max(m);
      const float alpha = __builtin_amdgcn_exp2f((macc - m) * C2);
      macc = m; l *= alpha;
      const float al0 = lane_of(alpha, 0), al1 = lane_of(alpha, 16);
#pragma unroll
      for (int k = 0; k < 4; ++k) { a0[k] *= al0; a1[k] *= al1; }
      refresh();
      if (endq < 2 * pc + 6) { pend = true; break; }
      v_step(0); advance(2);
    }
    unsigned wow[32];
    if (own_o) {
#pragma unroll
      for (int r = 0; r < 32; ++r) LD1_NT(wow[r], tc, wo + (size_t)(role * 32 + (r & ~3)) * (D * 2), (r & 3) * (D * 2));
    }
    if (pend) v_step(0);

    LAS float* rec = (LAS float*)(smem + L_REC);
    LAS f32x2* mlr = (LAS f32x2*)(smem + L_ML);
    {
      f32x4 o0, o1;
#pragma unroll
      for (int k = 0; k < 2; ++k) {
        o0[2 * k] = swap32_add(a0[k][0], a1[k][0]); o0[2 * k + 1] = swap32_add(a0[k][1], a1[k][1]);
        o1[2 * k] = swap32_add(a0[2 + k][0], a1[2 + k][0]); o1[2 * k + 1] = swap32_add(a0[2 + k][1], a1[2 + k][1]);
      }
      *(LAS f32x4*)(rec + wave * 512 + 8 * lane) = o0; *(LAS f32x4*)(rec + wave * 512 + 8 * lane + 4) = o1;
      l += dpp<ROR8>(l); l += dpp<HMIR>(l); l += dpp<XOR2>(l); l = swap32_add(l, l);
      if ((lane & 47) == 0) mlr[(lane >> 4) * NC + wave] = f32x2{m, l};
    }
    wg_barrier();
    const unsigned tag16 = tag[0] & 0xffffu;
    {
      const LAS f32x4* mp = (const LAS f32x4*)(mlr + (tid >> 7) * NC);
      const f32x4 m01 = mp[0], m23 = mp[1];
      const LAS f32x2* ap = (const LAS f32x2*)rec + tid;
      const f32x2 v0 = ap[0], v1 = ap[256], v2 = ap[512], v3 = ap[768];
      const float M = vmax(vmax(m01[0], m01[2]), vmax(m23[0], m23[2]));
      const float f0 = __builtin_amdgcn_exp2f((m01[0] - M) * C2), f1 = __builtin_amdgcn_exp2f((m01[2] - M) * C2);
      const float f2 = __builtin_amdgcn_exp2f((m23[0] - M) * C2), f3 = __builtin_amdgcn_exp2f((m23[2] - M) * C2);
      const f32x2 o = (v0 * f0 + v1 * f1) + (v2 * f2 + v3 * f3);
      const unsigned L = __float_as_uint((f0 * m01[1] + f1 * m01[3]) + (f2 * m23[1] + f3 * m23[3]));
      const unsigned pay = (lane & 2) ? L << 16 : ((lane & 1) ? L : __float_as_uint(M)) & 0xffff0000u;
      const u32x4 gd = {__float_as_uint(o[0]), __float_as_uint(o[1]), pay | tag16, tag[1]};
      ST4_SC1(o_pd, gd, ws + WS_PD);
    }

    u32x4 g0, g1;
    if (own_o) {
      LD4_SC1(g0, o_g0, gbase, 0); LD4_SC1(g1, o_g1, gbase, 0);
      f32x2 wof[32];
#pragma unroll
      for (int r = 0; r < 32; ++r) WAITV(3, wow[r]);
#pragma unroll
      for (int r = 0; r < 32; ++r) { wof[r] = f32x2{lo16(wow[r]), hi16(wow[r])}; asm volatile("" : "+v"(wof[r])); }
      WAITV(0, g0); WAITV(0, g1);
      unsigned spins = 0;
      for (;;) {
        const u64 m0 = __ballot(!((g0[2] & 0xffffu) == tag16 && g0[3] == tag[1])), m1 = __ballot(!((g1[2] & 0xffffu) == tag16 && g1[3] == tag[1]));
        if ((m0 | m1) == 0) break;
        if (++spins > SPIN_CAP) { bad = true; break; }
        repoll2(g0, g1, o_g0, o_g1, gbase, m0, m1);
      }

      const float ma = hi16(dppu<0x00>(g0[2])), mb = hi16(dppu<0x00>(g1[2]));
      const float Mw = rows_max(vmax(ma, mb));
      const float fa = __builtin_amdgcn_exp2f((ma - Mw) * C2), fb = __builtin_amdgcn_exp2f((mb - Mw) * C2);
      const float la = __uint_as_float((dppu<0x55>(g0[2]) & 0xffff0000u) | (dppu<0xAA>(g0[2]) >> 16));
      const float lb = __uint_as_float((dppu<0x55>(g1[2]) & 0xffff0000u) | (dppu<0xAA>(g1[2]) >> 16));
      const float oe = fa * __uint_as_float(g0[0]) + fb * __uint_as_float(g1[0]), oo = fa * __uint_as_float(g0[1]) + fb * __uint_as_float(g1[1]);
      const float Lp = fa * la + fb * lb;
      const float t = swap32_add(swap16_add(oe, oo), swap16_add(Lp, Lp));
      LAS float* redo = (LAS float*)(smem + L_REDO);
      LAS float* ml2 = (LAS float*)(smem + L_ML2);
      if (lane < 32) redo[(2 * (lane & 15) + (lane >> 4)) * NC + wave] = t;
      if (lane == 32) { ml2[wave] = Mw; ml2[NC + wave] = t; }
      wg_barrier();
      const f32x4 ov = ((const LAS f32x4*)redo)[lane & 31], Mv = ((const LAS f32x4*)ml2)[0], Lv = ((const LAS f32x4*)ml2)[1];
      const float M = vmax(vmax(Mv[0], Mv[1]), vmax(Mv[2], Mv[3]));
      f32x4 fv;
#pragma unroll
      for (int w = 0; w < NC; ++w) fv[w] = __builtin_amdgcn_exp2f((Mv[w] - M) * C2);
      const f32x4 on4 = ov * fv, L4 = Lv * fv;
      const float on = rbf(((on4[0] + on4[1]) + (on4[2] + on4[3])) * __builtin_amdgcn_rcpf((L4[0] + L4[1]) + (L4[2] + L4[3])));
      f32x2 y1 = {0.f, 0.f}, y2 = y1, y3 = y1;
#pragma unroll
      for (int r = 0; r < 32; r += 4) {
        y += wof[r] * lane_of(on, r); y1 += wof[r + 1] * lane_of(on, r + 1); y2 += wof[r + 2] * lane_of(on, r + 2); y3 += wof[r + 3] * lane_of(on, r + 3);
      }
      y = (y + y1) + (y2 + y3);
      if (bad) y[0] = __uint_as_float(0x7fc00000u);
      if (tid < 224) {
        const u32x4 gy = {__float_as_uint(y[0]), __float_as_uint(y[1]), tag[0], tag[1]};
        ST4_SC1(o_y, gy, ws + WS_Y);
      }

    } else if (own_out) {
      g0 = u32x4{0u, 0u, ~tag[0], 0u}; g1 = g0;
      unsigned spins = 0;
      for (;;) {
        const u64 m0 = __ballot(!(g0[2] == tag[0] && g0[3] == tag[1])), m1 = __ballot(!(g1[2] == tag[0] && g1[3] == tag[1]));
        if ((m0 | m1) == 0) break;
        if (++spins > SPIN_CAP) { bad = true; break; }
        const u64 gate = (spins == 1 || (m0 & m1) != ~0ull) ? ~0ull : 1ull;
        repoll2(g0, g1, o_g0, o_g1, gbase, m0 & gate, m1 & gate);
      }

      float s0 = __uint_as_float(g0[0]) + __uint_as_float(g1[0]), s1 = __uint_as_float(g0[1]) + __uint_as_float(g1[1]);
      if (bad) s0 = __uint_as_float(0x7fc00000u);
      s0 += dpp<ROR8>(s0); s1 += dpp<ROR8>(s1); s0 += dpp<ROR4>(s0); s1 += dpp<ROR4>(s1);
      float ss = swap16_add(s0, s1); ss = swap32_add(ss, ss);
      LAS float* redy = (LAS float*)(smem + L_REDO);
      if ((lane & 44) == 0) redy[wave * 8 + 2 * (lane & 3) + (lane >> 4)] = ss;
      wg_barrier();
      if (tid < 4) {
        const LAS f32x2* ry = (const LAS f32x2*)redy + tid;
        const f32x2 t = (ry[0] + ry[4]) + (ry[8] + ry[12]);
        const unsigned r = pack_bf16(lo16(xo) + t[0] + lo16(bo), hi16(xo) + t[1] + hi16(bo));
        if (OUTPOL == 0) out[(role >> 1) * 4 + tid] = r;
        if (OUTPOL == 1) asm volatile("global_store_dword %0, %1, %2 sc1" :: "v"(tid * 4u), "v"(r), "s"(out + (role >> 1) * 4) : "memory");
        if (OUTPOL == 2) asm volatile("global_store_dword %0, %1, %2 sc0 sc1" :: "v"(tid * 4u), "v"(r), "s"(out + (role >> 1) * 4) : "memory");
        if (OUTPOL == 3) asm volatile("global_store_dword %0, %1, %2 nt" :: "v"(tid * 4u), "v"(r), "s"(out + (role >> 1) * 4) : "memory");
        if (role == 1 && tid == 0) {
          if (OUTPOL == 0) *(u32x2*)(ws + WS_E) = tag;
          else asm volatile("global_store_dwordx2 %0, %1, %2 sc1" :: "v"(0u), "v"(tag), "s"(ws + WS_E) : "memory");
        }
      }

    }
  }
}

extern "C" void kernel_launch(void* const* d_in, const int* in_sizes, int n_in, void* d_out, int out_size, void* d_ws, size_t ws_size, hipStream_t stream) {
  fused_kernel<<<NWG, 320, 0, stream>>>((const char*)d_in[1], (const char*)d_in[2], (char*)d_ws, (const u16*)d_in[0], (const char*)d_in[3], (const char*)d_in[4],
                                        (const char*)d_in[6], (const u16*)d_in[7], (const char*)d_in[8], (const u16*)d_in[9], (unsigned*)d_out);
}
```

```cpp
#include <hip/hip_runtime.h>
#include <hip/hip_bf16.h>
#include <cstdint>

typedef unsigned u32x4 __attribute__((ext_vector_type(4)));
typedef unsigned u32x2 __attribute__((ext_vector_type(2)));
typedef float f32x4 __attribute__((ext_vector_type(4)));
typedef float f32x2 __attribute__((ext_vector_type(2)));
typedef __bf16 bf16x2 __attribute__((ext_vector_type(2)));
typedef unsigned short u16;
typedef unsigned long long u64;
#define LAS __attribute__((address_space(3)))

constexpr int WV = 17;
constexpr int INFL = 24;
constexpr int T_MLP = 6, T_O = 2, T_Q = 4, T_X = 2;
constexpr int CH0 = 4, NC2 = 4, NC1 = 8;
constexpr int D = 448, F = 1792, DH = 256;
constexpr int NWG = 256, NC = 4;
constexpr int PIECE = 8192, NSLOT = 18;
constexpr int RPR = 64;
constexpr int POOL = T_MLP + 3 * T_O + 2 * T_Q + 2 * T_X;
static_assert(POOL >= 2 * NC2 + NC1 && INFL % 4 == 0 && INFL >= 20 && INFL <= 32, "loader structure");
static_assert(T_MLP % 2 == 0 && T_O % 2 == 0 && T_Q % 2 == 0 && T_X % 2 == 0 && T_X >= 2 && T_O >= 2 && T_Q >= 2, "an even number of static rounds, at most 62");
constexpr unsigned PD_STRIDE = 33 * 128, Y_STRIDE = 29 * 128;
constexpr size_t WS_E = 0, WS_CTR = 4096, WS_Q = 16384, WS_PD = 65536, WS_Y = WS_PD + (size_t)NWG * PD_STRIDE;
constexpr unsigned SPIN_CAP = 1u << 22;
constexpr unsigned L_CTL = NSLOT * PIECE;
constexpr unsigned L_LANDED = L_CTL, L_QFLAG = L_CTL + 8, L_QCNT = L_CTL + 12, L_HCNT = L_CTL + 16, L_FREE = L_CTL + 32;
constexpr unsigned L_Q = L_CTL + 128, L_RED = L_Q + 1024, L_REDO = L_RED + 1024, L_ML2 = L_REDO + 512, L_P = L_ML2 + 64;
constexpr unsigned L_ML = L_P + NC * 128, L_XN = L_ML + 64, L_REC = L_XN + 2048, L_END = L_REC + NC * 2 * DH * 4;
static_assert(L_END <= 160 * 1024 && D <= 2 * DH, "LDS");
constexpr float C2 = 0.0625f * 1.4426950408889634f;

__device__ __forceinline__ int region_of(int b) { return (b & ~15) | ((b & 7) << 1) | ((b >> 3) & 1); }
__device__ __forceinline__ float lo16(unsigned w) { return __uint_as_float(w << 16); }
__device__ __forceinline__ float hi16(unsigned w) { return __uint_as_float(w & 0xffff0000u); }
__device__ __forceinline__ unsigned pack_bf16(float a, float b) {
  bf16x2 r = {(__bf16)a, (__bf16)b};
  return __builtin_bit_cast(unsigned, r);
}
__device__ __forceinline__ float rbf(float x) { return lo16(pack_bf16(x, x)); }
__device__ __forceinline__ float dot2(unsigned k, unsigned q, float acc) {
  return __builtin_amdgcn_fdot2_f32_bf16(__builtin_bit_cast(bf16x2, k), __builtin_bit_cast(bf16x2, q), acc, false);
}
template <int CTRL> __device__ __forceinline__ unsigned dppu(unsigned x) { return (unsigned)__builtin_amdgcn_mov_dpp((int)x, CTRL, 0xf, 0xf, true); }
template <int CTRL> __device__ __forceinline__ float dpp(float x) { return __uint_as_float(dppu<CTRL>(__float_as_uint(x))); }
constexpr int XOR1 = 0xB1, XOR2 = 0x4E, HMIR = 0x141, MIR = 0x140, ROR8 = 0x128, ROR4 = 0x124;
__device__ __forceinline__ float swap16_add(float a, float b) {
  auto r = __builtin_amdgcn_permlane16_swap(__float_as_uint(a), __float_as_uint(b), false, false);
  return __uint_as_float(r[0]) + __uint_as_float(r[1]);
}
__device__ __forceinline__ float swap32_add(float a, float b) {
  auto r = __builtin_amdgcn_permlane32_swap(__float_as_uint(a), __float_as_uint(b), false, false);
  return __uint_as_float(r[0]) + __uint_as_float(r[1]);
}
__device__ __forceinline__ float row_sum16(float x) { x += dpp<XOR1>(x); x += dpp<XOR2>(x); x += dpp<HMIR>(x); x += dpp<MIR>(x); return x; }
__device__ __forceinline__ float wave_sum(float x) { x = row_sum16(x); x = swap16_add(x, x); return swap32_add(x, x); }
__device__ __forceinline__ float lane_of(float x, int l) { return __builtin_bit_cast(float, __builtin_amdgcn_readlane(__builtin_bit_cast(int, x), l)); }
__device__ __forceinline__ float row_transpose_sum8(const float (&v)[8], u64 bit1) {
  float t0, t1, t2, t3, r;
  asm volatile("s_nop 1\n\t"
      "v_add_f32_dpp %0, %5, %5 row_ror:8 row_mask:0xf bank_mask:0xf\n\tv_add_f32_dpp %1, %6, %6 row_ror:8 row_mask:0xf bank_mask:0xf\n\t"
      "v_add_f32_dpp %2, %7, %7 row_ror:8 row_mask:0xf bank_mask:0xf\n\tv_add_f32_dpp %3, %8, %8 row_ror:8 row_mask:0xf bank_mask:0xf\n\t"
      "v_add_f32_dpp %0, %9, %9 row_ror:8 row_mask:0xf bank_mask:0xc\n\tv_add_f32_dpp %1, %10, %10 row_ror:8 row_mask:0xf bank_mask:0xc\n\t"
      "v_add_f32_dpp %2, %11, %11 row_ror:8 row_mask:0xf bank_mask:0xc\n\tv_add_f32_dpp %3, %12, %12 row_ror:8 row_mask:0xf bank_mask:0xc\n\t"
      "v_add_f32_dpp %0, %0, %0 row_half_mirror row_mask:0xf bank_mask:0x5\n\tv_add_f32_dpp %1, %1, %1 row_half_mirror row_mask:0xf bank_mask:0x5\n\t"
      "v_add_f32_dpp %0, %2, %2 row_half_mirror row_mask:0xf bank_mask:0xa\n\tv_add_f32_dpp %1, %3, %3 row_half_mirror row_mask:0xf bank_mask:0xa\n\t"
      "s_nop 1\n\t"
      "v_add_f32_dpp %2, %0, %0 quad_perm:[2,3,0,1] row_mask:0xf bank_mask:0xf\n\tv_add_f32_dpp %3, %1, %1 quad_perm:[2,3,0,1] row_mask:0xf bank_mask:0xf\n\t"
      "v_cndmask_b32 %2, %2, %3, %13\n\ts_nop 1\n\t"
      "v_add_f32_dpp %4, %2, %2 quad_perm:[1,0,3,2] row_mask:0xf bank_mask:0xf"
      : "=&v"(t0), "=&v"(t1), "=&v"(t2), "=&v"(t3), "=&v"(r)
      : "v"(v[0]), "v"(v[1]), "v"(v[2]), "v"(v[3]), "v"(v[4]), "v"(v[5]), "v"(v[6]), "v"(v[7]), "s"(bit1));
  return r;
}
__device__ __forceinline__ float head_max(float x) {
  float y;
  asm volatile("s_nop 1\n\tv_max_f32_dpp %0, %0, %0 row_ror:8 row_mask:0xf bank_mask:0xf\n\ts_nop 1\n\tv_max_f32_dpp %0, %0, %0 row_half_mirror row_mask:0xf bank_mask:0xf\n\t"
               "s_nop 1\n\tv_max_f32_dpp %0, %0, %0 quad_perm:[2,3,0,1] row_mask:0xf bank_mask:0xf\n\tv_mov_b32 %1, %0\n\ts_nop 1\n\tv_permlane32_swap_b32 %0, %1\n\tv_max_f32 %0, %0, %1"
               : "+v"(x), "=&v"(y));
  return x;
}
__device__ __forceinline__ float rows_max(float x) {
  float y;
  asm volatile("v_mov_b32 %1, %0\n\ts_nop 1\n\tv_permlane16_swap_b32 %0, %1\n\tv_max_f32 %0, %0, %1\n\tv_mov_b32 %1, %0\n\ts_nop 1\n\tv_permlane32_swap_b32 %0, %1\n\tv_max_f32 %0, %0, %1"
               : "+v"(x), "=&v"(y));
  return x;
}
__device__ __forceinline__ float vmax(float a, float b) { float r; asm("v_max_f32 %0, %1, %2" : "=v"(r) : "v"(a), "v"(b)); return r; }
__device__ __forceinline__ void layer_norm(const u32x4& xv, LAS float* xn, int lane) {
  float xf[8];
#pragma unroll
  for (int k = 0; k < 4; ++k) { xf[2 * k] = lo16(xv[k]); xf[2 * k + 1] = hi16(xv[k]); }
  const float live = lane < 56 ? 1.f : 0.f;
  const float mean = wave_sum((((xf[0] + xf[1]) + (xf[2] + xf[3])) + ((xf[4] + xf[5]) + (xf[6] + xf[7]))) * live) * (1.f / D);
  float ss = 0.f;
#pragma unroll
  for (int k = 0; k < 8; ++k) { const float d = xf[k] - mean; ss += d * d; }
  const float var = wave_sum(ss * live) * (1.f / D);
  const float rden = __builtin_amdgcn_rcpf(rbf(__builtin_amdgcn_sqrtf(rbf(rbf(var) + 1e-5f)))) * live, mb = rbf(mean);
#pragma unroll
  for (int k = 0; k < 8; k += 2) {
    const unsigned c = pack_bf16(xf[k] - mb, xf[k + 1] - mb), n = pack_bf16(lo16(c) * rden, hi16(c) * rden);
    xf[k] = lo16(n); xf[k + 1] = hi16(n);
  }
  *(LAS f32x4*)(xn + 8 * lane) = f32x4{xf[0], xf[1], xf[2], xf[3]}; *(LAS f32x4*)(xn + 8 * lane + 4) = f32x4{xf[4], xf[5], xf[6], xf[7]};
}
template <int NY> __device__ __forceinline__ void slice_partials(u32x4 (&w)[14], const LAS float* xn, LAS float* red, int tid) {
  f32x2 v[4] = {{0.f, 0.f}, {0.f, 0.f}, {0.f, 0.f}, {0.f, 0.f}};
  float xr[14];
#pragma unroll
  for (int i = 0; i < 14; ++i) xr[i] = xn[(tid >> 3) + 32 * i];
#pragma unroll
  for (int i = 0; i < 14; ++i) {
    asm volatile("s_waitcnt vmcnt(%1)" : "+v"(w[i]) : "i"(13 - i + NY));
#pragma unroll
    for (int k = 0; k < 4; ++k) v[k] += f32x2{lo16(w[i][k]), hi16(w[i][k])} * xr[i];
    asm volatile("" : "+v"(v[0]), "+v"(v[1]), "+v"(v[2]), "+v"(v[3]));
  }
  const float a0 = swap32_add(v[0][0], v[2][0]), a1 = swap32_add(v[0][1], v[2][1]), a2 = swap32_add(v[1][0], v[3][0]), a3 = swap32_add(v[1][1], v[3][1]);
  const float b0 = swap16_add(a0, a2), b1 = swap16_add(a1, a3);
  const float t1 = b0 + dpp<ROR8>(b0), t2 = b1 + dpp<ROR8>(b1);
  red[(tid & 7) * 8 + ((tid >> 3) & 7)] = (tid & 8) ? t2 : t1;
}

#define LD_NT(dst, voff, sbase, imm) asm volatile("global_load_dwordx4 %0, %1, %2 offset:%3 nt" : "=v"(dst) : "v"(voff), "s"(sbase), "i"(imm))
#define LD1_NT(dst, voff, sbase, imm) asm volatile("global_load_dword %0, %1, %2 offset:%3 nt" : "=v"(dst) : "v"(voff), "s"(sbase), "i"(imm))
#define LD4_SC1(dst, voff, sbase, imm) asm volatile("global_load_dwordx4 %0, %1, %2 offset:%3 sc1" : "=v"(dst) : "v"(voff), "s"(sbase), "i"(imm) : "memory")
#define ST4_SC1(voff, val, sbase) asm volatile("global_store_dwordx4 %0, %1, %2 sc1\n\ts_nop 1" :: "v"(voff), "v"(val), "s"(sbase) : "memory")
#define WAITV(n, reg) asm volatile("s_waitcnt vmcnt(%1)" : "+v"(reg) : "i"(n))
#define VMCNT(n) asm volatile("s_waitcnt vmcnt(%0)" :: "i"(n) : "memory")
__device__ __forceinline__ void repoll2(u32x4& g0, u32x4& g1, unsigned o0, unsigned o1, const char* base, u64 m0, u64 m1) {
  asm volatile("s_mov_b64 exec, %5\n\tglobal_load_dwordx4 %0, %2, %4 sc1\n\ts_mov_b64 exec, %6\n\tglobal_load_dwordx4 %1, %3, %4 sc1\n\ts_mov_b64 exec, -1\n\ts_waitcnt vmcnt(0)"
               : "+v"(g0), "+v"(g1) : "v"(o0), "v"(o1), "s"(base), "s"(m0), "s"(m1) : "memory");
}
__device__ __forceinline__ void wg_barrier() { asm volatile("s_waitcnt lgkmcnt(0)\n\ts_barrier" ::: "memory"); }
__device__ __forceinline__ void lds_st(unsigned addr, unsigned v) { asm volatile("ds_write_b32 %0, %1" :: "v"(addr), "v"(v) : "memory"); }
__device__ __forceinline__ unsigned lds_ld(unsigned addr) {
  unsigned v; asm volatile("ds_read_b32 %0, %1\n\ts_waitcnt lgkmcnt(0)" : "=v"(v) : "v"(addr) : "memory"); return __builtin_amdgcn_readfirstlane(v); }
__device__ __forceinline__ void dma4(const char* g, unsigned voff, unsigned lds) {
  asm volatile("s_mov_b32 m0, %2\n\ts_nop 0\n\tglobal_load_lds_dwordx4 %0, %1 nt\n\tglobal_load_lds_dwordx4 %0, %1 offset:1024 nt\n\t"
               "global_load_lds_dwordx4 %0, %1 offset:2048 nt\n\tglobal_load_lds_dwordx4 %0, %1 offset:3072 nt" :: "v"(voff), "s"(g), "s"(lds) : "memory");
}

__global__ __launch_bounds__(320, 2) void fused_kernel(const char* __restrict__ kc, const char* __restrict__ vc, char* __restrict__ ws,
                                                    const u16* __restrict__ x, const char* __restrict__ wq, const char* __restrict__ wo, const char* __restrict__ w1,
                                                    const u16* __restrict__ b1, const char* __restrict__ w2, const u16* __restrict__ b2,
                                                    unsigned* __restrict__ out) {
  __shared__ __attribute__((aligned(1024))) char smem[L_END];
  const unsigned lbase = (unsigned)(uintptr_t)(LAS char*)smem;
  const int b = blockIdx.x, tid = threadIdx.x, lane = tid & 63, wave = __builtin_amdgcn_readfirstlane(tid >> 6);
  const int role = b >> 1;
  const bool own_o = ((b ^ (b >> 3)) & 1) == 0, has_mlp = own_o && (role & 3) == 0 && role < 112;
  const bool make_q = !own_o && (role & 1) == 0, own_out = !own_o && (role & 1) == 1 && role < 112;
  const int S = RPR - (own_o ? ((role & 3) == 0 ? T_MLP : T_O) : (make_q ? T_Q : T_X));

  if (__builtin_expect(wave == NC, 1)) {
    __builtin_amdgcn_s_setprio(3);
    const unsigned voff = lane * 16;
    const int q = b >> 3;
    unsigned* ctr = (unsigned*)(ws + WS_CTR) + q * 64;
    int nq = 0, slot = 0, gen = 0;
    constexpr int LAG = INFL / 4 - 1;
    int frs = 0;
    auto put = [&](const char* g, int u) {
      if ((u & 1) == 0 && frs < gen) { while ((int)lds_ld(lbase + L_FREE + slot * 4) < gen) __builtin_amdgcn_s_sleep(1); }
      dma4(g, voff, lbase + slot * PIECE + (u & 1) * 4096);
      ++nq;
      if (u & 1) { if (++slot == NSLOT) { slot = 0; ++gen; } }
    };
    auto settle = [&](int u) {
      if (u & 1) {
        unsigned fr;
        asm volatile("ds_read_b32 %0, %1\n\ts_waitcnt vmcnt(%2) lgkmcnt(0)" : "=&v"(fr) : "v"(lbase + L_FREE + slot * 4), "i"(INFL - 4) : "memory");
        frs = __builtin_amdgcn_readfirstlane(fr);
      } else VMCNT(INFL - 4);
      lds_st(lbase + L_LANDED, nq - LAG);
    };
    auto piece = [&](const char* g) {
      put(g, 0); put(g + 4096, 1);
      unsigned fr;
      asm volatile("ds_read_b32 %0, %1\n\ts_waitcnt vmcnt(%2) lgkmcnt(0)" : "=&v"(fr) : "v"(lbase + L_FREE + slot * 4), "i"(WV) : "memory");
      frs = __builtin_amdgcn_readfirstlane(fr);
      lds_st(lbase + L_LANDED, max(nq - (WV + 3) / 4, 0));
    };
    unsigned tv = 0;
    auto ticket = [&]() { asm volatile("s_mov_b64 exec, 1\n\tglobal_atomic_add %0, %1, %2, %3 sc0\n\ts_mov_b64 exec, -1" : "=v"(tv) : "v"(0u), "v"(1u), "s"(ctr) : "memory"); };
    size_t voffs = (size_t)region_of(b) * (1u << 20);
    {
      const char* g = kc + voffs;
      put(g, 0); put(g + 4096, 1);
      if (lane < 8 + NSLOT) lds_st(lbase + L_CTL + lane * 4, lane == 1 ? 0xffffffffu : 0u);
      wg_barrier();
      for (int p = 1; p < 2 * S; ++p) { g += PIECE; piece(g); }
      g = vc + voffs;
      for (int p = 0; p < 2 * S - 6; ++p) { piece(g); g += PIECE; }
      while (lds_ld(lbase + L_QFLAG) == 0) __builtin_amdgcn_s_sleep(1);
#pragma unroll
      for (int u = 0; u < 8; ++u) { if (u == 8 - INFL / 4) ticket(); put(g + u * 4096, u); settle(u); }
      voffs += (size_t)(S - 1) * (2 * PIECE);
    }
    for (;;) {
      asm volatile("" : "+v"(tv));
      const int k = __builtin_amdgcn_readfirstlane(tv);
      constexpr int A = POOL - 2 * NC2 - NC1, N4 = (A + CH0 - 1) / CH0;
      if ((unsigned)k >= (unsigned)(N4 + NC2 + NC1)) break;
      int cur, sz;
      if (k < N4) { cur = CH0 * k; sz = min(CH0, A - cur); } else if (k < N4 + NC2) { cur = A + 2 * (k - N4); sz = 2; } else { cur = A + 2 * NC2 + (k - N4 - NC2); sz = 1; }
      for (; sz > 0; --sz, ++cur) {
        constexpr int C1 = T_MLP + T_Q, C2_ = C1 + T_O + T_X, C3 = C2_ + T_O + T_Q;
        const int pair = (cur >= C1) + (cur >= C2_) + (cur >= C3), c = cur - (cur >= C3 ? C3 : cur >= C2_ ? C2_ : cur >= C1 ? C1 : 0);
        const int to = pair == 0 ? T_MLP : T_O; const bool first = c < to;
        const int wg = 8 * q + 2 * pair + ((first ? q : q + 1) & 1), pos = first ? RPR - to + c : RPR - ((pair & 1) ? T_X : T_Q) + (c - to);
        const size_t off = (size_t)region_of(wg) * (1u << 20) + (size_t)pos * (2 * PIECE);
        const char* kp = kc + off; const char* vp = vc + voffs;
#pragma unroll
        for (int u = 0; u < 8; ++u) {
          if (u == 8 - INFL / 4 && sz == 1) ticket();
          put(u < 4 ? kp + u * 4096 : vp + (u - 4) * 4096, u);
          settle(u);
        }
        voffs = off;
      }
    }
    lds_st(lbase + L_LANDED + 4, nq);
    slot += 2; if (slot >= NSLOT) { slot -= NSLOT; ++gen; }
    frs = 0;
#pragma unroll
    for (int u = 0; u < 4; ++u) { put(vc + voffs + u * 4096, u); settle(u); }
#pragma unroll
    for (int i = LAG - 1; i >= 0; --i) { VMCNT(4 * i); lds_st(lbase + L_LANDED, nq - i + (i < 4 ? 4 : 0)); }

  } else {
    const int h = b >> 5;
    LAS float* xn = (LAS float*)(smem + L_XN);
    LAS float* red = (LAS float*)(smem + L_RED);
    u32x2 ev, tag;
    auto arrive = [&](unsigned cnt) -> int {
      unsigned arr;
      asm volatile("s_mov_b64 exec, 1\n\tds_add_rtn_u32 %0, %1, %2\n\ts_mov_b64 exec, -1\n\ts_waitcnt lgkmcnt(0)" : "=&v"(arr) : "v"(lbase + cnt), "v"(1u) : "memory");
      return __builtin_amdgcn_readfirstlane(arr);
    };
    auto make_tag = [&]() {
      unsigned e0, e1;
      asm volatile("s_waitcnt vmcnt(0)\n\tv_readfirstlane_b32 %0, %2\n\tv_readfirstlane_b32 %1, %3\n\ts_nop 1" : "=s"(e0), "=s"(e1) : "v"(ev[0]), "v"(ev[1]));
      const u64 z = ((u64)e1 << 32 | e0) * 6364136223846793005ull + 1442695040888963407ull;
      tag = u32x2{(unsigned)z, (unsigned)(z >> 32)};
    };
    asm volatile("global_load_dwordx2 %0, %1, %2 sc1" : "=v"(ev) : "v"(0u), "s"(ws + WS_E) : "memory");
    if (make_q) {
      const int qi = role >> 1;
      if (wave == 0 && (qi & 4) == 0) asm volatile("global_store_dword %0, %1, %2 sc1" :: "v"(0u), "v"(0u), "s"(ws + WS_CTR + ((qi >> 3) * 4 + (qi & 3)) * 256) : "memory");
      u32x4 xv, wv[14];
      asm volatile("global_load_dwordx4 %0, %1, %2" : "=v"(xv) : "v"((lane < 56 ? lane : 55) * 16u), "s"(x));
      const unsigned o = (unsigned)(tid >> 3) * 8192u + (tid & 7) * 16u;
#pragma unroll
      for (int i = 0; i < 14; ++i) LD_NT(wv[i], o, wq + qi * 128 + i * (32 * 8192), 0);
      wg_barrier();
      WAITV(14, xv);
      layer_norm(xv, xn, lane);
      slice_partials<0>(wv, xn, red + wave * 64, tid);
      make_tag();
      if (arrive(L_QCNT) == NC - 1 && lane < 16) {
        const LAS f32x4* rq = (const LAS f32x4*)red + lane;
        const f32x4 s = (rq[0] + rq[16]) + (rq[32] + rq[48]);
        const u32x4 g = {pack_bf16(s[0], s[1]), pack_bf16(s[2], s[3]), tag[0], tag[1]};
        ST4_SC1((unsigned)(qi * 256 + lane * 16), g, ws + WS_Q);
      }
    } else {
      wg_barrier();
    }
    bool bad = false;
    unsigned o_pd = (unsigned)(b * PD_STRIDE + tid * 16), o_y = (unsigned)(role * Y_STRIDE + (tid < 224 ? tid : 0) * 16);
    unsigned o_g0 = own_o ? (unsigned)((h * 32 + (tid >> 4)) * PD_STRIDE + (role & 15) * 256 + (tid & 15) * 16)
                          : (unsigned)((tid >> 2) * Y_STRIDE + (own_out ? role >> 1 : 0) * 64 + (tid & 3) * 16);
    unsigned o_g1 = o_g0 + (own_o ? 16 * PD_STRIDE : 64 * Y_STRIDE);
    asm volatile("" : "+v"(o_pd), "+v"(o_y), "+v"(o_g0), "+v"(o_g1));
    const char* gbase = ws + (own_o ? WS_PD : WS_Y);
    f32x2 y = {0.f, 0.f};
    unsigned xo = 0, bo = 0;
    const unsigned tc = (tid < 224 ? tid : 223) * 4u;
    if (has_mlp) {
      const int hg = role >> 2;
      u32x4 xv, wv[14]; unsigned b1w, wa[32], wb[32];
      asm volatile("global_load_dwordx4 %0, %1, %2" : "=v"(xv) : "v"((lane < 56 ? lane : 55) * 16u), "s"(x));
      asm volatile("global_load_ushort %0, %1, %2" : "=v"(b1w) : "v"(lane * 2u), "s"(b1 + hg * 64));
      const unsigned o = (unsigned)(tid >> 3) * (F * 2u) + (tid & 7) * 16u;
#pragma unroll
      for (int i = 0; i < 14; ++i) LD_NT(wv[i], o, w1 + hg * 128 + i * (32 * F * 2), 0);
#pragma unroll
      for (int r = 0; r < 32; ++r) LD1_NT(wa[r], tc, w2 + (size_t)(hg * 64 + (r & ~3)) * (D * 2), (r & 3) * (D * 2));
      WAITV(46, xv); WAITV(46, b1w);
      layer_norm(xv, xn, lane);
      slice_partials<32>(wv, xn, red + wave * 64, tid);
      arrive(L_HCNT);
#pragma unroll
      for (int r = 0; r < 28; ++r) LD1_NT(wb[r], tc, w2 + (size_t)(hg * 64 + 32 + (r & ~3)) * (D * 2), (r & 3) * (D * 2));
      while (lds_ld(lbase + L_HCNT) < NC) __builtin_amdgcn_s_sleep(1);
      const float hv = rbf(rbf((red[lane] + red[64 + lane]) + (red[128 + lane] + red[192 + lane])) + lo16(b1w));
      const float u = 0.7978845608028654f * (hv + 0.044715f * hv * hv * hv);
      const float e = __builtin_amdgcn_exp2f(u * -2.885390081777927f);
      const float ge = rbf(hv * __builtin_amdgcn_rcpf(1.f + e));
#pragma unroll
      for (int r = 0; r < 32; ++r) WAITV(28, wa[r]);
#pragma unroll
      for (int r = 28; r < 32; ++r) LD1_NT(wb[r], tc, w2 + (size_t)(hg * 64 + 32 + (r & ~3)) * (D * 2), (r & 3) * (D * 2));
#pragma unroll
      for (int r = 0; r < 32; ++r) y += f32x2{lo16(wa[r]), hi16(wa[r])} * lane_of(ge, r);
      asm volatile("" : "+v"(y));
#pragma unroll
      for (int r = 0; r < 32; ++r) WAITV(0, wb[r]);
#pragma unroll
      for (int r = 0; r < 32; ++r) y += f32x2{lo16(wb[r]), hi16(wb[r])} * lane_of(ge, 32 + r);
    }
    if (!own_o) {
      asm volatile("global_load_dword %0, %2, %3\n\tglobal_load_dword %1, %2, %4\n\ts_waitcnt vmcnt(0)"
                   : "=&v"(xo), "=&v"(bo) : "v"((lane & 3) * 4u), "s"(x + (own_out ? role >> 1 : 0) * 8), "s"(b2 + (own_out ? role >> 1 : 0) * 8));
    }
    asm volatile("" : "+v"(y));
    make_tag();
    float m = -INFINITY, macc = -INFINITY, l = 0.f;
    f32x2 a0[4], a1[4];
#pragma unroll
    for (int k = 0; k < 4; ++k) { a0[k] = f32x2{0.f, 0.f}; a1[k] = f32x2{0.f, 0.f}; }
    unsigned landed = 0, endq = 0xffffffffu;
    unsigned pc = wave; int slot = wave, gen = 1;
    u32x4 q0, q1;
    LAS u16* scw = (LAS u16*)(smem + L_REC + wave * 2048) + (lane >> 1);
    auto refresh = [&]() {
      u32x2 c; asm volatile("ds_read_b64 %0, %1\n\ts_waitcnt lgkmcnt(0)" : "=v"(c) : "v"(lbase + L_LANDED) : "memory");
      landed = __builtin_amdgcn_readfirstlane(c[0]); endq = __builtin_amdgcn_readfirstlane(c[1]);
    };
    auto advance = [&](int n) { pc += n; slot += n; if (slot >= NSLOT) { slot -= NSLOT; ++gen; } };
    auto k_read = [&](u32x4 (&R)[8]) {
      const LAS u32x4* kt = (const LAS u32x4*)(smem + slot * PIECE) + lane;
#pragma unroll
      for (int i = 0; i < 8; ++i) R[i] = kt[i * 64];
      lds_st(lbase + L_FREE + slot * 4, gen);
    };
    auto k_scores = [&](const u32x4 (&R)[8], int t) {
      float sc[8];
#pragma unroll
      for (int i = 0; i < 8; ++i) {
        float a = 0.f, c = 0.f;
#pragma unroll
        for (int k = 0; k < 4; ++k) { a = dot2(R[i][k], q0[k], a); c = dot2(R[i][k], q1[k], c); }
        sc[i] = swap16_add(a, c);
      }
      const float s = row_transpose_sum8(sc, 0xccccccccccccccccull);
      const unsigned sb = pack_bf16(s, s);
      scw[t * 32] = (u16)sb;
      m = vmax(m, lo16(sb));
    };
    auto v_step = [&](int t) {
      LAS float* pw = (LAS float*)(smem + L_P) + wave * 32;
      const float p = __builtin_amdgcn_exp2f((lo16(scw[t * 32]) - m) * C2);
      pw[lane >> 1] = p;
      l += p;
      f32x4 pp[4];
#pragma unroll
      for (int k = 0; k < 4; ++k) pp[k] = *(const LAS f32x4*)(pw + (lane >> 5) * 16 + 4 * k);
      const LAS u32x4* vt = (const LAS u32x4*)(smem + slot * PIECE) + lane;
#pragma unroll
      for (int hf = 0; hf < 2; ++hf) {
        while (landed < 2 * pc + 1 + hf) refresh();
        u32x4 R[4];
#pragma unroll
        for (int i = 0; i < 4; ++i) R[i] = vt[(hf * 4 + i) * 64];
        if (hf == 1) lds_st(lbase + L_FREE + slot * 4, gen);
#pragma unroll
        for (int i = 0; i < 4; ++i)
#pragma unroll
          for (int k = 0; k < 4; ++k) { const f32x2 v = {lo16(R[i][k]), hi16(R[i][k])}; a0[k] += v * pp[hf][i]; a1[k] += v * pp[2 + hf][i]; }
        asm volatile("" : "+v"(a0[0]), "+v"(a0[1]), "+v"(a0[2]), "+v"(a0[3]), "+v"(a1[0]), "+v"(a1[1]), "+v"(a1[2]), "+v"(a1[3]));
      }
    };
    {
      u32x4 R0[8], R1[8], R2[8], R3[8];
      while (landed < 2 * pc + 2) refresh();
      k_read(R0); advance(4);
      while (landed < 2 * pc + 2) refresh();
      k_read(R1); advance(4);
      while (landed < 2 * pc + 2) refresh();
      k_read(R2); advance(4);
      if (wave != 0) {
        while (landed < 2 * pc + 2) refresh();
        k_read(R3);
      }
      if (wave == 0) {
        u32x4 g0 = {0u, 0u, ~tag[0], 0u}, g1 = g0;
        const unsigned qo = (unsigned)(h * 2048 + lane * 32);
        unsigned spins = 0;
        for (;;) {
          const u64 miss = __ballot(!(g0[2] == tag[0] && g0[3] == tag[1] && g1[2] == tag[0] && g1[3] == tag[1]));
          if (miss == 0) break;
          if (++spins > SPIN_CAP) { bad = true; break; }
          repoll2(g0, g1, qo, qo + 16, ws + WS_Q, miss, miss);
        }
        *(LAS u32x4*)(smem + L_Q + lane * 16) = u32x4{g0[0], g0[1], g1[0], g1[1]};
        lds_st(lbase + L_QFLAG, 1u);
        while (landed < 2 * pc + 2) refresh();
        k_read(R3);
      } else {
        while (lds_ld(lbase + L_QFLAG) == 0) __builtin_amdgcn_s_sleep(1);
      }
      q0 = *(const LAS u32x4*)(smem + L_Q + (lane & 31) * 16); q1 = *(const LAS u32x4*)(smem + L_Q + 512 + (lane & 31) * 16);
      advance(4);
      k_scores(R0, 0); k_scores(R1, 1); k_scores(R2, 2); k_scores(R3, 3);
    }
    for (int t = 4; t < S / 2; ++t) {
      u32x4 R[8];
      while (landed < 2 * pc + 2) refresh();
      k_read(R); advance(4);
      k_scores(R, t);
    }
    m = head_max(m); macc = m;
    const int nv = S / 2 - (wave >> 1);
    for (int t = 0; t < nv; ++t) { v_step(t); advance(4); }
    if (wave >= 2) { advance(2); v_step(S / 2 - 1); advance(2); } else { pc -= 2; slot -= 2; if (slot < 0) { slot += NSLOT; --gen; } }
    bool pend = false;
    for (;;) {
      while (landed < 2 * pc + 2 && endq >= 2 * pc + 2) refresh();
      if (endq < 2 * pc + 2) break;
      u32x4 R[8];
      k_read(R); advance(6);
      k_scores(R, 0);
      m = head_max(m);
      const float alpha = __builtin_amdgcn_exp2f((macc - m) * C2);
      macc = m; l *= alpha;
      const float al0 = lane_of(alpha, 0), al1 = lane_of(alpha, 16);
#pragma unroll
      for (int k = 0; k < 4; ++k) { a0[k] *= al0; a1[k] *= al1; }
      refresh();
      if (endq < 2 * pc + 6) { pend = true; break; }
      v_step(0); advance(2);
    }
    unsigned wow[32];
    if (own_o) {
#pragma unroll
      for (int r = 0; r < 32; ++r) LD1_NT(wow[r], tc, wo + (size_t)(role * 32 + (r & ~3)) * (D * 2), (r & 3) * (D * 2));
    }
    if (pend) v_step(0);

    LAS float* rec = (LAS float*)(smem + L_REC);
    LAS f32x2* mlr = (LAS f32x2*)(smem + L_ML);
    {
      f32x4 o0, o1;
#pragma unroll
      for (int k = 0; k < 2; ++k) {
        o0[2 * k] = swap32_add(a0[k][0], a1[k][0]); o0[2 * k + 1] = swap32_add(a0[k][1], a1[k][1]);
        o1[2 * k] = swap32_add(a0[2 + k][0], a1[2 + k][0]); o1[2 * k + 1] = swap32_add(a0[2 + k][1], a1[2 + k][1]);
      }
      *(LAS f32x4*)(rec + wave * 512 + 8 * lane) = o0; *(LAS f32x4*)(rec + wave * 512 + 8 * lane + 4) = o1;
      l += dpp<ROR8>(l); l += dpp<HMIR>(l); l += dpp<XOR2>(l); l = swap32_add(l, l);
      if ((lane & 47) == 0) mlr[(lane >> 4) * NC + wave] = f32x2{m, l};
    }
    wg_barrier();
    const unsigned tag16 = tag[0] & 0xffffu;
    {
      const LAS f32x4* mp = (const LAS f32x4*)(mlr + (tid >> 7) * NC);
      const f32x4 m01 = mp[0], m23 = mp[1];
      const LAS f32x2* ap = (const LAS f32x2*)rec + tid;
      const f32x2 v0 = ap[0], v1 = ap[256], v2 = ap[512], v3 = ap[768];
      const float M = vmax(vmax(m01[0], m01[2]), vmax(m23[0], m23[2]));
      const float f0 = __builtin_amdgcn_exp2f((m01[0] - M) * C2), f1 = __builtin_amdgcn_exp2f((m01[2] - M) * C2);
      const float f2 = __builtin_amdgcn_exp2f((m23[0] - M) * C2), f3 = __builtin_amdgcn_exp2f((m23[2] - M) * C2);
      const f32x2 o = (v0 * f0 + v1 * f1) + (v2 * f2 + v3 * f3);
      const unsigned L = __float_as_uint((f0 * m01[1] + f1 * m01[3]) + (f2 * m23[1] + f3 * m23[3]));
      const unsigned pay = (lane & 2) ? L << 16 : ((lane & 1) ? L : __float_as_uint(M)) & 0xffff0000u;
      const u32x4 gd = {__float_as_uint(o[0]), __float_as_uint(o[1]), pay | tag16, tag[1]};
      ST4_SC1(o_pd, gd, ws + WS_PD);
    }

    u32x4 g0, g1;
    if (own_o) {
      LD4_SC1(g0, o_g0, gbase, 0); LD4_SC1(g1, o_g1, gbase, 0);
      f32x2 wof[32];
#pragma unroll
      for (int r = 0; r < 32; ++r) WAITV(3, wow[r]);
#pragma unroll
      for (int r = 0; r < 32; ++r) { wof[r] = f32x2{lo16(wow[r]), hi16(wow[r])}; asm volatile("" : "+v"(wof[r])); }
      WAITV(0, g0); WAITV(0, g1);
      unsigned spins = 0;
      for (;;) {
        const u64 m0 = __ballot(!((g0[2] & 0xffffu) == tag16 && g0[3] == tag[1])), m1 = __ballot(!((g1[2] & 0xffffu) == tag16 && g1[3] == tag[1]));
        if ((m0 | m1) == 0) break;
        if (++spins > SPIN_CAP) { bad = true; break; }
        repoll2(g0, g1, o_g0, o_g1, gbase, m0, m1);
      }

      const float ma = hi16(dppu<0x00>(g0[2])), mb = hi16(dppu<0x00>(g1[2]));
      const float Mw = rows_max(vmax(ma, mb));
      const float fa = __builtin_amdgcn_exp2f((ma - Mw) * C2), fb = __builtin_amdgcn_exp2f((mb - Mw) * C2);
      const float la = __uint_as_float((dppu<0x55>(g0[2]) & 0xffff0000u) | (dppu<0xAA>(g0[2]) >> 16));
      const float lb = __uint_as_float((dppu<0x55>(g1[2]) & 0xffff0000u) | (dppu<0xAA>(g1[2]) >> 16));
      const float oe = fa * __uint_as_float(g0[0]) + fb * __uint_as_float(g1[0]), oo = fa * __uint_as_float(g0[1]) + fb * __uint_as_float(g1[1]);
      const float Lp = fa * la + fb * lb;
      const float t = swap32_add(swap16_add(oe, oo), swap16_add(Lp, Lp));
      LAS float* redo = (LAS float*)(smem + L_REDO);
      LAS float* ml2 = (LAS float*)(smem + L_ML2);
      if (lane < 32) redo[(2 * (lane & 15) + (lane >> 4)) * NC + wave] = t;
      if (lane == 32) { ml2[wave] = Mw; ml2[NC + wave] = t; }
      wg_barrier();
      const f32x4 ov = ((const LAS f32x4*)redo)[lane & 31], Mv = ((const LAS f32x4*)ml2)[0], Lv = ((const LAS f32x4*)ml2)[1];
      const float M = vmax(vmax(Mv[0], Mv[1]), vmax(Mv[2], Mv[3]));
      f32x4 fv;
#pragma unroll
      for (int w = 0; w < NC; ++w) fv[w] = __builtin_amdgcn_exp2f((Mv[w] - M) * C2);
      const f32x4 on4 = ov * fv, L4 = Lv * fv;
      const float on = rbf(((on4[0] + on4[1]) + (on4[2] + on4[3])) * __builtin_amdgcn_rcpf((L4[0] + L4[1]) + (L4[2] + L4[3])));
      f32x2 y1 = {0.f, 0.f}, y2 = y1, y3 = y1;
#pragma unroll
      for (int r = 0; r < 32; r += 4) {
        y += wof[r] * lane_of(on, r); y1 += wof[r + 1] * lane_of(on, r + 1); y2 += wof[r + 2] * lane_of(on, r + 2); y3 += wof[r + 3] * lane_of(on, r + 3);
      }
      y = (y + y1) + (y2 + y3);
      if (bad) y[0] = __uint_as_float(0x7fc00000u);
      if (tid < 224) {
        const u32x4 gy = {__float_as_uint(y[0]), __float_as_uint(y[1]), tag[0], tag[1]};
        ST4_SC1(o_y, gy, ws + WS_Y);
      }

    } else if (own_out) {
      g0 = u32x4{0u, 0u, ~tag[0], 0u}; g1 = g0;
      unsigned spins = 0;
      for (;;) {
        const u64 m0 = __ballot(!(g0[2] == tag[0] && g0[3] == tag[1])), m1 = __ballot(!(g1[2] == tag[0] && g1[3] == tag[1]));
        if ((m0 | m1) == 0) break;
        if (++spins > SPIN_CAP) { bad = true; break; }
        const u64 gate = (spins == 1 || (m0 & m1) != ~0ull) ? ~0ull : 1ull;
        repoll2(g0, g1, o_g0, o_g1, gbase, m0 & gate, m1 & gate);
      }

      float s0 = __uint_as_float(g0[0]) + __uint_as_float(g1[0]), s1 = __uint_as_float(g0[1]) + __uint_as_float(g1[1]);
      if (bad) s0 = __uint_as_float(0x7fc00000u);
      s0 += dpp<ROR8>(s0); s1 += dpp<ROR8>(s1); s0 += dpp<ROR4>(s0); s1 += dpp<ROR4>(s1);
      float ss = swap16_add(s0, s1); ss = swap32_add(ss, ss);
      LAS float* redy = (LAS float*)(smem + L_REDO);
      if ((lane & 44) == 0) redy[wave * 8 + 2 * (lane & 3) + (lane >> 4)] = ss;
      wg_barrier();
      if (tid < 4) {
        const LAS f32x2* ry = (const LAS f32x2*)redy + tid;
        const f32x2 t = (ry[0] + ry[4]) + (ry[8] + ry[12]);
        out[(role >> 1) * 4 + tid] = pack_bf16(lo16(xo) + t[0] + lo16(bo), hi16(xo) + t[1] + hi16(bo));
        if (role == 1 && tid == 0)
          asm volatile("global_store_dwordx2 %0, %1, %2 sc1" :: "v"(0u), "v"(tag), "s"(ws + WS_E) : "memory");
      }

    }
  }
}

extern "C" void kernel_launch(void* const* d_in, const int* in_sizes, int n_in, void* d_out, int out_size, void* d_ws, size_t ws_size, hipStream_t stream) {
  fused_kernel<<<NWG, 320, 0, stream>>>((const char*)d_in[1], (const char*)d_in[2], (char*)d_ws, (const u16*)d_in[0], (const char*)d_in[3], (const char*)d_in[4],
                                        (const char*)d_in[6], (const u16*)d_in[7], (const char*)d_in[8], (const u16*)d_in[9], (unsigned*)d_out);
}
```
